```python
import torch

def _softmax(x, axis=-1):
    return torch.softmax(x, dim=axis)

def _gelu(x):
    return torch.nn.functional.gelu(x, approximate='tanh')

def _silu(x):
    return torch.nn.functional.silu(x)

def _sigmoid(x):
    return torch.sigmoid(x)

def _layernorm(x, axis=-1, eps=1e-05):
    m = x.mean(dim=axis, keepdim=True)
    v = x.var(dim=axis, keepdim=True, unbiased=False)
    return (x - m) / torch.sqrt(v + eps)

def _scatter_add(tgt, idx, src, axis=0):
    return tgt.clone().index_add_(axis, idx, src)

def _segment_sum(src, seg, n):
    out = torch.zeros((n,) + src.shape[1:], dtype=src.dtype)
    return out.index_add_(0, seg, src)

def _pad(x, pw):
    flat = tuple((v for pair in reversed(pw) for v in pair))
    return torch.nn.functional.pad(x, flat)
kernel_name = 'standalone__mm_dit__diffusion'

def setup_inputs():

    _w = lambda a: (lambda t: t.to(torch.bfloat16) if t.is_floating_point() else t)(torch.as_tensor(a))
    B, Ni, Nt, D = (2, 1024, 32, 768)
    return {'x_img': _w((lambda *s: torch.randn(*s, dtype=torch.bfloat16) * 0.05)(B, Ni, D)), 'x_txt': _w((lambda *s: torch.randn(*s, dtype=torch.bfloat16) * 0.05)(B, Nt, D)), 'c': _w((lambda *s: torch.randn(*s, dtype=torch.bfloat16) * 0.05)(B, D)), 'wqkv_img': _w((lambda *s: torch.randn(*s, dtype=torch.bfloat16) * 0.05)(D, 3 * D)), 'wqkv_txt': _w((lambda *s: torch.randn(*s, dtype=torch.bfloat16) * 0.05)(D, 3 * D)), 'wo_img': _w((lambda *s: torch.randn(*s, dtype=torch.bfloat16) * 0.05)(D, D)), 'wo_txt': _w((lambda *s: torch.randn(*s, dtype=torch.bfloat16) * 0.05)(D, D)), 'wmod_img': _w((lambda *s: torch.randn(*s, dtype=torch.bfloat16) * 0.05)(D, 6 * D)), 'wmod_txt': _w((lambda *s: torch.randn(*s, dtype=torch.bfloat16) * 0.05)(D, 6 * D))}

def reference(x_img, x_txt, c, wqkv_img, wqkv_txt, wo_img, wo_txt, wmod_img, wmod_txt):
    B, Ni, D = x_img.shape
    Nt = x_txt.shape[1]
    mod_i = (_silu(c) @ wmod_img).reshape(B, 6, 1, D)
    mod_t = (_silu(c) @ wmod_txt).reshape(B, 6, 1, D)
    shi, sci, gai = (mod_i[:, 0].clone(), mod_i[:, 1].clone(), mod_i[:, 2].clone())
    sht, sct, gat = (mod_t[:, 0].clone(), mod_t[:, 1].clone(), mod_t[:, 2].clone())
    hi = _layernorm(x_img, axis=-1) * (1 + sci) + shi
    ht = _layernorm(x_txt, axis=-1) * (1 + sct) + sht
    qkvi = hi @ wqkv_img
    qkvt = ht @ wqkv_txt
    qi, ki, vi = (qkvi[..., :D].clone(), qkvi[..., D:2 * D].clone(), qkvi[..., 2 * D:].clone())
    qt, kt, vt = (qkvt[..., :D].clone(), qkvt[..., D:2 * D].clone(), qkvt[..., 2 * D:].clone())
    q = torch.concatenate([qt, qi], axis=1)
    k = torch.concatenate([kt, ki], axis=1)
    v = torch.concatenate([vt, vi], axis=1)
    scores = torch.einsum('bqd,bkd->bqk', q, k) * D ** (-0.5)
    p = _softmax(scores, axis=-1)
    a = torch.einsum('bqk,bkd->bqd', p, v)
    at, ai = (a[:, :Nt].clone(), a[:, Nt:].clone())
    out_i = x_img + gai * (ai @ wo_img)
    out_t = x_txt + gat * (at @ wo_txt)
    return (out_i, out_t)
```

```cpp
#include <hip/hip_runtime.h>
#include <hip/hip_bf16.h>
#include <stdint.h>

typedef __attribute__((ext_vector_type(8))) __bf16 bf16x8;
typedef __attribute__((ext_vector_type(4))) __bf16 bf16x4;
typedef __attribute__((ext_vector_type(2))) __bf16 bf16x2;
typedef __attribute__((ext_vector_type(4))) float f32x4;
typedef __attribute__((ext_vector_type(2))) float f32x2;
typedef __attribute__((ext_vector_type(4))) unsigned u32x4;
typedef __attribute__((ext_vector_type(2))) unsigned u32x2;
#define LDS_AS __attribute__((address_space(3)))
typedef __amdgpu_buffer_rsrc_t rsrc_t;
typedef unsigned short u16;

constexpr int D = 768, NI = 1024, NTX = 32, NK = 1056;
constexpr int RT = 66;
constexpr int KD = 24;
constexpr int KK = 33;
constexpr int KSPL = 2;
constexpr int OOB = 0x7f000000;

constexpr unsigned SZ_TOK = 2u * RT * KD * 1024u;
constexpr unsigned SZ_VT = 2u * 48u * KK * 1024u;
constexpr unsigned OFF_XN = 0;
constexpr unsigned OFF_Q = OFF_XN + SZ_TOK;
constexpr unsigned OFF_K = OFF_Q + SZ_TOK;
constexpr unsigned OFF_VX = OFF_K + SZ_TOK;
constexpr unsigned OFF_VT1 = OFF_VX + 4u * KD * 1024u;
constexpr unsigned OFF_VT2 = OFF_VT1 + SZ_VT;
constexpr unsigned OFF_P = OFF_VT2 + SZ_VT;
constexpr unsigned OFF_WVO = OFF_P + 2u * RT * KK * 1024u;
constexpr unsigned OFF_MOD = OFF_WVO + 96u * KD * 1024u;
constexpr unsigned OFF_SM = OFF_MOD + KSPL * 4u * 2304u * 4u;
constexpr unsigned OFF_SL = OFF_SM + 2u * 11u * NK * 4u;
constexpr unsigned WS_END = OFF_SL + 2u * 22u * NK * 4u;

#define WAIT_VM(n) asm volatile("s_waitcnt vmcnt(%0)" ::"n"(n) : "memory")
#define WAIT_LGKM0() asm volatile("s_waitcnt lgkmcnt(0)" ::: "memory")
#define BARRIER() __builtin_amdgcn_s_barrier()

__device__ __forceinline__ rsrc_t make_rsrc(const void* p, unsigned bytes) {
  return __builtin_amdgcn_make_buffer_rsrc((void*)p, 0, (int)bytes, 0x00020000);
}
__device__ __forceinline__ u32x4 make_srd(const void* p, unsigned bytes) {
  unsigned long long a = (unsigned long long)p;
  u32x4 r;
  r[0] = (unsigned)a; r[1] = (unsigned)(a >> 32) & 0xffffu; r[2] = bytes; r[3] = 0x00020000u;
  return r;
}
template <int IMM>
__device__ __forceinline__ void dma(rsrc_t rs, LDS_AS char* lds, int voff, int soff) {
  __builtin_amdgcn_raw_ptr_buffer_load_lds(rs, (LDS_AS void*)lds, 16, voff, soff, IMM, 0);
}
__device__ __forceinline__ u32x4 aload(const u32x4& srd, int voff, int soff) {
  u32x4 r;
  asm volatile("buffer_load_dwordx4 %0, %1, %2, %3 offen" : "=v"(r) : "v"(voff), "s"(srd), "s"(soff));
  return r;
}
__device__ __forceinline__ bf16x8 ldsfrag(LDS_AS char* p) { return *(LDS_AS bf16x8*)p; }
__device__ __forceinline__ bf16x8 ldsfrag_tr(LDS_AS char* p) {
  bf16x4 lo = __builtin_amdgcn_ds_read_tr16_b64_v4bf16((LDS_AS bf16x4*)p);
  bf16x4 hi = __builtin_amdgcn_ds_read_tr16_b64_v4bf16((LDS_AS bf16x4*)(p + 512));
  return __builtin_shufflevector(lo, hi, 0, 1, 2, 3, 4, 5, 6, 7);
}
__device__ __forceinline__ f32x4 mfma(bf16x8 a, bf16x8 b, f32x4 c) {
  return __builtin_amdgcn_mfma_f32_16x16x32_bf16(a, b, c, 0, 0, 0);
}
__device__ __forceinline__ unsigned pack2(float a, float b) {
  f32x2 f = {a, b};
  bf16x2 h = __builtin_convertvector(f, bf16x2);
  return __builtin_bit_cast(unsigned, h);
}
__device__ __forceinline__ float bflo(unsigned v) { return __uint_as_float(v << 16); }
__device__ __forceinline__ float bfhi(unsigned v) { return __uint_as_float(v & 0xffff0000u); }
__device__ __forceinline__ unsigned tiled_off(int rowtile, int KS, int T16, int lane) {
  int g = lane >> 4;
  return (unsigned)((rowtile * KS + (T16 >> 1)) * 1024 + ((lane & 15) + 16 * (2 * (T16 & 1) + (g >> 1))) * 16 + (g & 1) * 8);
}
__device__ __forceinline__ int timg_src(int lane, int sigma, int pitch) {
  int r = lane >> 3, c = (lane & 7) ^ (2 * ((r >> 1) & 1) + 4 * sigma);
  return r * pitch + c * 16;
}
template <int NB>
__device__ __forceinline__ int timg_rd(int lane, int nt) {
  int g = lane >> 4, q = (lane & 15) >> 2, p = lane & 3;
  int f = 2 * ((q >> 1) & 1) + 4 * (g & 1);
  return (g * NB + (nt >> 2)) * 1024 + q * 128 + (((2 * (nt & 3)) ^ f) + (p >> 1)) * 16 + (p & 1) * 8;
}

template <int MW, int NW, int NS, bool A_ROWMAJOR>
__device__ __forceinline__ void small_gemm(LDS_AS char* smem, rsrc_t ra, int a_voff0, int a_tile_stride, rsrc_t rb, int b_off,
                                           int b_pitch, int wave, int lane, f32x4 (&acc)[MW][NW]) {
  constexpr int NB = (2 * NW) / 4;
  constexpr int ABLK = 4 * MW;
  constexpr int STAGE = (ABLK + 8 * NB) * 1024;
  constexpr int AV = ABLK / 4, BV = 2 * NB;
  constexpr int VM = AV + BV;
  static_assert(MW == 2 || MW == 3, "");
  int av[AV];
#pragma unroll
  for (int i = 0; i < AV; ++i) {
    int u = wave * AV + i;
    av[i] = a_voff0 + (u >> 1) * a_tile_stride + (u & 1) * (A_ROWMAJOR ? 64 : 1024);
  }
  int bv[2];
  bv[0] = b_off + (wave * 16) * b_pitch + timg_src(lane, 0, b_pitch);
  bv[1] = b_off + (wave * 16 + 8) * b_pitch + timg_src(lane, 1, b_pitch);
  auto issue = [&](int t, int buf) {
    LDS_AS char* sb = smem + buf * STAGE;
    int soa = t < 12 ? t * (A_ROWMAJOR ? 128 : 2048) : OOB;
    int sob = t < 12 ? t * 64 * b_pitch : OOB;
#pragma unroll
    for (int i = 0; i < AV; ++i) dma<0>(ra, sb + (wave * AV + i) * 1024, av[i], soa);
#pragma unroll
    for (int h = 0; h < 2; ++h)
#pragma unroll
      for (int nb = 0; nb < NB; ++nb) dma<0>(rb, sb + (ABLK + (2 * wave + h) * NB + nb) * 1024, bv[h] + nb * 128, sob);
  };
  const int wm = wave >> 1, wn = wave & 1;
  int ard = wm * MW * 2048 + lane * 16;
  int brd[NW];
#pragma unroll
  for (int n = 0; n < NW; ++n) brd[n] = ABLK * 1024 + timg_rd<NB>(lane, wn * NW + n);
#pragma unroll
  for (int s = 0; s < NS - 1; ++s) issue(s, s);
  int buf = 0;
#pragma unroll 1
  for (int t = 0; t < 12; ++t) {
    WAIT_VM(VM * (NS - 2));
    WAIT_LGKM0();
    BARRIER();
    int nb_ = buf + NS - 1; if (nb_ >= NS) nb_ -= NS;
    issue(t + NS - 1, nb_);
    LDS_AS char* sb = smem + buf * STAGE;
#pragma unroll
    for (int ks = 0; ks < 2; ++ks) {
      bf16x8 a[MW], b[NW];
#pragma unroll
      for (int m = 0; m < MW; ++m) a[m] = ldsfrag(sb + ard + m * 2048 + ks * 1024);
#pragma unroll
      for (int n = 0; n < NW; ++n) b[n] = ldsfrag_tr(sb + brd[n] + ks * 4 * NB * 1024);
#pragma unroll
      for (int m = 0; m < MW; ++m)
#pragma unroll
        for (int n = 0; n < NW; ++n) acc[m][n] = mfma(a[m], b[n], acc[m][n]);
    }
    buf = buf + 1; if (buf >= NS) buf = 0;
  }
  WAIT_VM(0);
}

constexpr int K0_T3 = 192, K0_T1 = 72 * KSPL, K0_T2 = 2 * RT;
__global__ __launch_bounds__(256, 2) void k0_prep(const u16* __restrict__ x_img, const u16* __restrict__ x_txt,
                                                  const u16* __restrict__ cvec, const u16* __restrict__ wqkv_img,
                                                  const u16* __restrict__ wo_img, const u16* __restrict__ wo_txt,
                                                  const u16* __restrict__ wmod_img, const u16* __restrict__ wmod_txt,
                                                  char* __restrict__ ws) {
  __shared__ __attribute__((aligned(1024))) char smem_[81920];
  LDS_AS char* smem = (LDS_AS char*)smem_;
  const int bid = blockIdx.x, tid = threadIdx.x, lane = tid & 63;
  const int wave = __builtin_amdgcn_readfirstlane(tid >> 6);
  if (bid < K0_T3) {
    const int x = bid & 7, idx = bid >> 3;
    const int mb = (x & 1) * 4 + (idx & 3), nbk = (x >> 1) * 6 + (idx >> 2);
    rsrc_t ra = make_rsrc(wqkv_img, D * 3 * D * 2);
    rsrc_t rb = make_rsrc(nbk < 12 ? wo_img : wo_txt, D * D * 2);
    const int a_voff0 = (mb * 96 + (lane & 15)) * (3 * D * 2) + 2 * D * 2 + (lane >> 4) * 16;
    f32x4 acc[3][2] = {};
    small_gemm<3, 2, 4, true>(smem, ra, a_voff0, 16 * 3 * D * 2, rb, (nbk % 12) * 128, D * 2, wave, lane, acc);
    const int wm = wave >> 1, wn = wave & 1;
#pragma unroll
    for (int m = 0; m < 3; ++m)
#pragma unroll
      for (int n = 0; n < 2; ++n) {
        u32x2 o = {pack2(acc[m][n][0], acc[m][n][1]), pack2(acc[m][n][2], acc[m][n][3])};
        *(u32x2*)(ws + OFF_WVO + tiled_off(nbk * 4 + wn * 2 + n, KD, mb * 6 + wm * 3 + m, lane)) = o;
      }
  } else if (bid < K0_T3 + K0_T1) {
    constexpr int ROWS = D / KSPL, PER = ROWS / 32;
    const int i = bid - K0_T3, s = i / (36 * KSPL), rem = i % (36 * KSPL), cb = rem % 36, kh = rem / 36;
    const u16* w = (s ? wmod_txt : wmod_img) + (size_t)(kh * ROWS + (tid >> 3)) * (6 * D) + cb * 64 + (tid & 7) * 8;
    u32x4 wv[PER];
#pragma unroll
    for (int r = 0; r < PER; ++r) wv[r] = __builtin_nontemporal_load((const u32x4*)(w + (size_t)r * 32 * (6 * D)));
    LDS_AS float* sc = (LDS_AS float*)smem;
    LDS_AS float* red = (LDS_AS float*)(smem + 8192);
    for (int e = tid; e < 2 * ROWS; e += 256) {
      int b = e / ROWS, r = e % ROWS;
      float v = __uint_as_float((unsigned)cvec[b * D + kh * ROWS + r] << 16);
      sc[r * 2 + b] = v / (1.0f + __expf(-v));
    }
    __syncthreads();
    float a0[8] = {}, a1[8] = {};
#pragma unroll
    for (int r = 0; r < PER; ++r) {
      f32x2 sv = *(LDS_AS f32x2*)(sc + ((tid >> 3) + r * 32) * 2);
#pragma unroll
      for (int j = 0; j < 4; ++j) {
        float lo = bflo(wv[r][j]), hi = bfhi(wv[r][j]);
        a0[2 * j] = fmaf(sv[0], lo, a0[2 * j]); a0[2 * j + 1] = fmaf(sv[0], hi, a0[2 * j + 1]);
        a1[2 * j] = fmaf(sv[1], lo, a1[2 * j]); a1[2 * j + 1] = fmaf(sv[1], hi, a1[2 * j + 1]);
      }
    }
    LDS_AS float* rp = red + (tid >> 3) * 128 + (tid & 7) * 8;
    *(LDS_AS f32x4*)(rp) = f32x4{a0[0], a0[1], a0[2], a0[3]};
    *(LDS_AS f32x4*)(rp + 4) = f32x4{a0[4], a0[5], a0[6], a0[7]};
    *(LDS_AS f32x4*)(rp + 64) = f32x4{a1[0], a1[1], a1[2], a1[3]};
    *(LDS_AS f32x4*)(rp + 68) = f32x4{a1[4], a1[5], a1[6], a1[7]};
    __syncthreads();
    if (tid < 128) {
      float t = 0.f;
#pragma unroll
      for (int g = 0; g < 32; ++g) t += red[g * 128 + tid];
      float* mod = (float*)(ws + OFF_MOD);
      mod[((kh * 2 + s) * 2 + (tid >> 6)) * 2304 + cb * 64 + (tid & 63)] = t;
    }
  } else {
    const int i = bid - K0_T3 - K0_T1, b = i / RT, rt = i % RT;
    const u16* src = rt < 64 ? x_img + (size_t)(b * NI + rt * 16) * D : x_txt + (size_t)(b * NTX + (rt - 64) * 16) * D;
    src += (lane & 15) * D + wave * 192 + (lane >> 4) * 8;
    u32x4 v[6];
#pragma unroll
    for (int k = 0; k < 6; ++k) v[k] = *(const u32x4*)(src + k * 32);
    LDS_AS float* red = (LDS_AS float*)smem;
    float s = 0.f;
#pragma unroll
    for (int k = 0; k < 6; ++k)
#pragma unroll
      for (int j = 0; j < 4; ++j) s += bflo(v[k][j]) + bfhi(v[k][j]);
    s += __shfl_xor(s, 16); s += __shfl_xor(s, 32);
    if (lane < 16) red[wave * 16 + lane] = s;
    __syncthreads();
    const int r = lane & 15;
    const float mean = (red[r] + red[16 + r] + red[32 + r] + red[48 + r]) * (1.0f / D);
    float q = 0.f;
#pragma unroll
    for (int k = 0; k < 6; ++k)
#pragma unroll
      for (int j = 0; j < 4; ++j) {
        float lo = bflo(v[k][j]) - mean, hi = bfhi(v[k][j]) - mean;
        q = fmaf(lo, lo, q); q = fmaf(hi, hi, q);
      }
    q += __shfl_xor(q, 16); q += __shfl_xor(q, 32);
    if (lane < 16) red[64 + wave * 16 + lane] = q;
    __syncthreads();
    const float var = (red[64 + r] + red[80 + r] + red[96 + r] + red[112 + r]) * (1.0f / D);
    const float rstd = 1.0f / sqrtf(var + 1e-5f);
    char* dst = ws + OFF_XN + (size_t)(i * KD + wave * 6) * 1024 + lane * 16;
#pragma unroll
    for (int k = 0; k < 6; ++k) {
      u32x4 o;
#pragma unroll
      for (int j = 0; j < 4; ++j) o[j] = pack2((bflo(v[k][j]) - mean) * rstd, (bfhi(v[k][j]) - mean) * rstd);
      *(u32x4*)(dst + k * 1024) = o;
    }
  }
}

constexpr int K1_A = 0, K1_ASZ = 18 * 1024, K1_B = 2 * K1_ASZ, K1_BSZ = 24 * 1024, K1_GS = K1_B + 4 * K1_BSZ;
constexpr int K1_LDS = K1_GS + 2 * 1536 * 4;

__device__ __forceinline__ u32x4 modulate(u32x4 v, f32x4 g0, f32x4 g1, f32x4 s0, f32x4 s1) {
  u32x4 o;
  o[0] = pack2(fmaf(bflo(v[0]), g0[0], s0[0]), fmaf(bfhi(v[0]), g0[1], s0[1]));
  o[1] = pack2(fmaf(bflo(v[1]), g0[2], s0[2]), fmaf(bfhi(v[1]), g0[3], s0[3]));
  o[2] = pack2(fmaf(bflo(v[2]), g1[0], s1[0]), fmaf(bfhi(v[2]), g1[1], s1[1]));
  o[3] = pack2(fmaf(bflo(v[3]), g1[2], s1[2]), fmaf(bfhi(v[3]), g1[3], s1[3]));
  return o;
}

template <int MT, int MTA, int MTB, bool VT>
__device__ __forceinline__ void k1_body(LDS_AS char* smem, char* __restrict__ ws, const u16* __restrict__ wsrc, int gA, int gB,
                                        int cb, int smod, int wave, int lane, int tid) {
  constexpr int MTT = MTA + MTB;
  constexpr int NA = (MTT == 9) ? 3 : 1;
  const int wm = wave >> 2, wn = wave & 3;
  const u32x4 srd_ws = make_srd(ws, WS_END);
  rsrc_t rb = VT ? make_rsrc(ws + OFF_WVO, 96 * KD * 1024) : make_rsrc(wsrc, D * 3 * D * 2);
  constexpr int WP = 3 * D * 2;
  int avo[NA], awr[NA];
  bool aact[NA];
#pragma unroll
  for (int i = 0; i < NA; ++i) {
    int lt = (i < 2) ? (wave >> 1) + 4 * i : 8;
    int gt = lt < MTA ? gA + lt : gB + (lt - MTA);
    bool act = (i < 2) ? (lt < MTT) : (((lane & 15) >> 2) == (wave >> 1));
    aact[i] = act;
    avo[i] = act ? (int)OFF_XN + (gt * KD + (wave & 1)) * 1024 + lane * 16 : OOB;
    awr[i] = (lt * 2 + (wave & 1)) * 1024 + lane * 16;
  }
  const int lt0 = wave >> 1;
  const int gt0 = lt0 < MTA ? gA + lt0 : gB + (lt0 - MTA);
  const int bsel = gt0 >= RT ? 1 : 0;
  const int gsrd = K1_GS + bsel * 6144 + ((wave & 1) * 32 + (lane >> 4) * 8) * 4;
  int bvo[3];
  if (VT) {
#pragma unroll
    for (int i = 0; i < 3; ++i) {
      int u = wave * 3 + i;
      bvo[i] = (((cb - 8) * 12 + (u >> 1)) * KD + (u & 1)) * 1024 + lane * 16;
    }
  } else {
#pragma unroll
    for (int i = 0; i < 3; ++i) bvo[i] = wave * 8 * WP + cb * 384 + i * 128 + timg_src(lane, wave & 1, WP);
  }
  auto issueB = [&](int t) {
    LDS_AS char* sb = smem + K1_B + (t & 3) * K1_BSZ + wave * 3072;
    int so = t < 12 ? t * (VT ? 2048 : 64 * WP) : OOB;
#pragma unroll
    for (int i = 0; i < 3; ++i) dma<0>(rb, sb + i * 1024, bvo[i], so);
  };
  auto issueA = [&](u32x4 (&r)[NA], int t) {
    int so = t < 12 ? t * 2048 : 0x00400000;
#pragma unroll
    for (int i = 0; i < NA; ++i) r[i] = aload(srd_ws, avo[i], so);
  };
  auto xform = [&](u32x4 (&r)[NA], int t) {
    LDS_AS char* gp = smem + gsrd + t * 256;
    f32x4 s0 = *(LDS_AS f32x4*)gp, s1 = *(LDS_AS f32x4*)(gp + 16);
    f32x4 g0 = *(LDS_AS f32x4*)(gp + 3072), g1 = *(LDS_AS f32x4*)(gp + 3088);
    LDS_AS char* ab = smem + K1_A + (t & 1) * K1_ASZ;
#pragma unroll
    for (int i = 0; i < NA; ++i) {
      u32x4 o = modulate(r[i], g0, g1, s0, s1);
      if (aact[i]) *(LDS_AS u32x4*)(ab + awr[i]) = o;
    }
  };
  const int ard = (wm ? MTA : 0) * 2048 + lane * 16;
  int brd[3];
#pragma unroll
  for (int n = 0; n < 3; ++n) brd[n] = VT ? (wn * 3 + n) * 2048 + lane * 16 : timg_rd<3>(lane, wn * 3 + n);
  f32x4 acc[MT][3] = {};
  auto kstep = [&](int t, int ks) {
    LDS_AS char* ab = smem + K1_A + (t & 1) * K1_ASZ + ard + ks * 1024;
    LDS_AS char* bb = smem + K1_B + (t & 3) * K1_BSZ + ks * (VT ? 1024 : 12288);
    bf16x8 a[MT], b[3];
#pragma unroll
    for (int n = 0; n < 3; ++n) b[n] = VT ? ldsfrag(bb + brd[n]) : ldsfrag_tr(bb + brd[n]);
#pragma unroll
    for (int m = 0; m < MT; ++m) a[m] = ldsfrag(ab + m * 2048);
#pragma unroll
    for (int m = 0; m < MT; ++m)
#pragma unroll
      for (int n = 0; n < 3; ++n) acc[m][n] = VT ? mfma(a[m], b[n], acc[m][n]) : mfma(b[n], a[m], acc[m][n]);
  };

  u32x4 mp[2][KSPL];
#pragma unroll
  for (int h = 0; h < 2; ++h) {
    int item = tid + h * 512;
    int b = item >= 384 ? 1 : 0, e = item - b * 384;
#pragma unroll
    for (int k = 0; k < KSPL; ++k)
      mp[h][k] = aload(srd_ws, item < 768 ? (int)OFF_MOD + (((k * 2 + smod) * 2 + b) * 2304 + e * 4) * 4 : OOB, 0);
  }
  u32x4 r0[NA], r1[NA];
  issueA(r0, 0);
  issueB(0); issueB(1);
  issueA(r1, 1);
  issueB(2);
  asm volatile("s_waitcnt vmcnt(%4)" : "+v"(mp[0][0]), "+v"(mp[0][1]), "+v"(mp[1][0]), "+v"(mp[1][1]) : "n"(2 * NA + 9) : "memory");
#pragma unroll
  for (int h = 0; h < 2; ++h) {
    int item = tid + h * 512;
    int e = item >= 384 ? item - 384 : item;
    f32x4 v = __builtin_bit_cast(f32x4, mp[h][0]) + __builtin_bit_cast(f32x4, mp[h][1]);
    if (e >= 192) v += 1.0f;
    if (item < 768) *(LDS_AS f32x4*)(smem + K1_GS + item * 16) = v;
  }
  WAIT_LGKM0();
  BARRIER();
  if constexpr (NA == 3) asm volatile("s_waitcnt vmcnt(%3)" : "+v"(r0[0]), "+v"(r0[NA - 2]), "+v"(r0[NA - 1]) : "n"(NA + 9) : "memory");
  else asm volatile("s_waitcnt vmcnt(%1)" : "+v"(r0[0]) : "n"(NA + 9) : "memory");
  xform(r0, 0);
  issueA(r0, 2);

#define K1_STEP(T, RN)                                                                                                   \
  do {                                                                                                                   \
    if constexpr (NA == 3) asm volatile("s_waitcnt vmcnt(%3)" : "+v"(RN[0]), "+v"(RN[NA - 2]), "+v"(RN[NA - 1]) : "n"(NA + 3) : "memory"); \
    else asm volatile("s_waitcnt vmcnt(%1)" : "+v"(RN[0]) : "n"(NA + 3) : "memory");                                     \
    WAIT_LGKM0();                                                                                                        \
    BARRIER();                                                                                                           \
    issueB((T) + 3);                                                                                                     \
    if ((T) < 11) xform(RN, (T) + 1);                                                                                    \
    kstep((T), 0);                                                                                                       \
    issueA(RN, (T) + 3);                                                                                                 \
    kstep((T), 1);                                                                                                       \
  } while (0)
#pragma unroll 1
  for (int t = 0; t < 12; t += 2) {
    K1_STEP(t, r1);
    K1_STEP(t + 1, r0);
  }
#undef K1_STEP
  WAIT_VM(0);
  asm volatile("" ::"v"(r0[0]), "v"(r0[NA - 1]), "v"(r1[0]), "v"(r1[NA - 1]));
  if constexpr (NA == 3) asm volatile("" ::"v"(r0[1]), "v"(r1[1]));

  const int g0t = wm ? gB : gA;
  const int sec = cb >> 2;
  if (VT) {
    const int b = g0t >= RT ? 1 : 0, rt0 = g0t - b * RT;
    char* dst = ws + (sec == 2 ? OFF_VT1 : OFF_VT2);
#pragma unroll
    for (int m = 0; m < MT; ++m)
#pragma unroll
      for (int n = 0; n < 3; ++n) {
        u32x2 o = {pack2(acc[m][n][0], acc[m][n][1]), pack2(acc[m][n][2], acc[m][n][3])};
        *(u32x2*)(dst + tiled_off(b * 48 + (cb & 3) * 12 + wn * 3 + n, KK, rt0 + m, lane)) = o;
      }
  } else {
    const float sc = sec == 0 ? 0.036084391824351615f * 1.4426950408889634f : 1.0f;
    char* dst = ws + (sec == 0 ? OFF_Q : (sec == 1 ? OFF_K : OFF_VX));
    const int rbase = sec == 2 ? wm * 2 : g0t;
#pragma unroll
    for (int m = 0; m < MT; ++m)
#pragma unroll
      for (int n = 0; n < 3; ++n) {
        u32x2 o = {pack2(acc[m][n][0] * sc, acc[m][n][1] * sc), pack2(acc[m][n][2] * sc, acc[m][n][3] * sc)};
        *(u32x2*)(dst + tiled_off(rbase + m, KD, (cb & 3) * 12 + wn * 3 + n, lane)) = o;
      }
  }
}

__global__ __launch_bounds__(512, 2) void k1_proj(const u16* __restrict__ wqkv_img, const u16* __restrict__ wqkv_txt,
                                                  char* __restrict__ ws) {
  __shared__ __attribute__((aligned(1024))) char smem_[K1_LDS];
  LDS_AS char* smem = (LDS_AS char*)smem_;
  const int bid = blockIdx.x, tid = threadIdx.x, lane = tid & 63;
  const int wave = __builtin_amdgcn_readfirstlane(tid >> 6);
  if (bid < 224) {
    const int x = bid & 7, idx = bid >> 3;
    const int gA = (x & 1) * RT + (idx % 7) * 9, cb = (x >> 1) * 4 + idx / 7;
    if (cb < 8) {
      if (wave < 4) k1_body<5, 5, 4, false>(smem, ws, wqkv_img, gA, gA + 5, cb, 0, wave, lane, tid);
      else k1_body<4, 5, 4, false>(smem, ws, wqkv_img, gA, gA + 5, cb, 0, wave, lane, tid);
    } else {
      if (wave < 4) k1_body<5, 5, 4, true>(smem, ws, wqkv_img, gA, gA + 5, cb, 0, wave, lane, tid);
      else k1_body<4, 5, 4, true>(smem, ws, wqkv_img, gA, gA + 5, cb, 0, wave, lane, tid);
    }
  } else if (bid < 240) {
    const int cb = bid - 224;
    if (cb < 8) k1_body<1, 1, 1, false>(smem, ws, wqkv_img, 63, RT + 63, cb, 0, wave, lane, tid);
    else k1_body<1, 1, 1, true>(smem, ws, wqkv_img, 63, RT + 63, cb, 0, wave, lane, tid);
  } else {
    k1_body<2, 2, 2, false>(smem, ws, wqkv_txt, 64, RT + 64, bid - 240, 1, wave, lane, tid);
  }
}

constexpr int K2_NS = 6, K2_STAGE = 24 * 1024;
__global__ __launch_bounds__(256) void k2_scores(const u16* __restrict__ wo_img, const u16* __restrict__ wo_txt,
                                                 char* __restrict__ ws) {
  __shared__ __attribute__((aligned(1024))) char smem_[K2_NS * K2_STAGE + 1024];
  LDS_AS char* smem = (LDS_AS char*)smem_;
  const int bid = blockIdx.x, tid = threadIdx.x, lane = tid & 63;
  const int wave = __builtin_amdgcn_readfirstlane(tid >> 6);
  const int x = bid & 7, idx = bid >> 3;
  if (idx < 30 || (idx == 30 && x < 2)) {
    const int b = x & 1, qd = x >> 1;
    int ti, tj;
    if (idx == 30) { ti = 5; tj = 5; }
    else if (qd == 0) { ti = idx / 6; tj = idx % 6; }
    else if (qd == 1) { ti = idx / 5; tj = 6 + idx % 5; }
    else if (qd == 2) { ti = 6 + idx / 6; tj = 5 + idx % 6; }
    else { ti = 5 + idx / 5; tj = idx % 5; }
    rsrc_t rs = make_rsrc(ws, WS_END);
    const int vbase = (wave < 2 ? (int)OFF_K + (b * RT + tj * 6 + wave * 3) * KD * 1024
                                : (int)OFF_Q + (b * RT + ti * 6 + (wave - 2) * 3) * KD * 1024) + lane * 16;
    auto issue = [&](int t, int buf) {
      LDS_AS char* sb = smem + buf * K2_STAGE + wave * 6144;
      int so = t < 12 ? t * 2048 : OOB;
#pragma unroll
      for (int i = 0; i < 3; ++i) {
        dma<0>(rs, sb + i * 2048, vbase + i * KD * 1024, so);
        dma<1024>(rs, sb + i * 2048, vbase + i * KD * 1024, so);
      }
    };
    const int wk = wave >> 1, wq = wave & 1;
    const int krd = wk * 6144 + lane * 16, qrd = 12288 + wq * 6144 + lane * 16;
    f32x4 acc[3][3] = {};
#pragma unroll
    for (int s = 0; s < K2_NS - 1; ++s) issue(s, s);
    int buf = 0;
#pragma unroll 1
    for (int t = 0; t < 12; ++t) {
      WAIT_VM(6 * (K2_NS - 2));
      WAIT_LGKM0();
      BARRIER();
      int nb = buf + K2_NS - 1; if (nb >= K2_NS) nb -= K2_NS;
      issue(t + K2_NS - 1, nb);
      LDS_AS char* sb = smem + buf * K2_STAGE;
#pragma unroll
      for (int ks = 0; ks < 2; ++ks) {
        bf16x8 kf[3], qf[3];
#pragma unroll
        for (int i = 0; i < 3; ++i) {
          kf[i] = ldsfrag(sb + krd + i * 2048 + ks * 1024);
          qf[i] = ldsfrag(sb + qrd + i * 2048 + ks * 1024);
        }
#pragma unroll
        for (int i = 0; i < 3; ++i)
#pragma unroll
          for (int j = 0; j < 3; ++j) acc[i][j] = mfma(kf[i], qf[j], acc[i][j]);
      }
      buf = buf + 1; if (buf >= K2_NS) buf = 0;
    }
    WAIT_VM(0);
    LDS_AS float* red = (LDS_AS float*)(smem + K2_NS * K2_STAGE);
    float mx[3];
#pragma unroll
    for (int j = 0; j < 3; ++j) {
      float m = acc[0][j][0];
#pragma unroll
      for (int i = 0; i < 3; ++i)
#pragma unroll
        for (int r = 0; r < 4; ++r) m = fmaxf(m, acc[i][j][r]);
      m = fmaxf(m, __shfl_xor(m, 16));
      m = fmaxf(m, __shfl_xor(m, 32));
      mx[j] = m;
      if (lane < 16) red[wk * 96 + wq * 48 + j * 16 + lane] = m;
    }
    __syncthreads();
    float* SM = (float*)(ws + OFF_SM);
    float* SL = (float*)(ws + OFF_SL);
#pragma unroll
    for (int j = 0; j < 3; ++j) {
      const float m = fmaxf(mx[j], red[(wk ^ 1) * 96 + wq * 48 + j * 16 + (lane & 15)]);
      float l = 0.f;
      const int qt = ti * 6 + wq * 3 + j;
#pragma unroll
      for (int i = 0; i < 3; ++i) {
        float p0 = __builtin_amdgcn_exp2f(acc[i][j][0] - m), p1 = __builtin_amdgcn_exp2f(acc[i][j][1] - m);
        float p2 = __builtin_amdgcn_exp2f(acc[i][j][2] - m), p3 = __builtin_amdgcn_exp2f(acc[i][j][3] - m);
        l += (p0 + p1) + (p2 + p3);
        u32x2 o = {pack2(p0, p1), pack2(p2, p3)};
        *(u32x2*)(ws + OFF_P + tiled_off(b * RT + qt, KK, tj * 6 + wk * 3 + i, lane)) = o;
      }
      l += __shfl_xor(l, 16);
      l += __shfl_xor(l, 32);
      if (lane < 16) {
        SL[(b * 22 + tj * 2 + wk) * NK + qt * 16 + lane] = l;
        if (wk == 0) SM[(b * 11 + tj) * NK + qt * 16 + lane] = m;
      }
    }
  } else {
    const int e = (idx - 30) * 8 + x - 2;
    rsrc_t ra = make_rsrc(ws + OFF_VX, 4 * KD * 1024);
    rsrc_t rb = make_rsrc(e < 6 ? wo_img : wo_txt, D * D * 2);
    f32x4 acc[2][4] = {};
    small_gemm<2, 4, K2_NS, false>(smem, ra, lane * 16, KD * 1024, rb, (e % 6) * 256, D * 2, wave, lane, acc);
    const int wm = wave >> 1, wn = wave & 1;
    char* dst = ws + (e < 6 ? OFF_VT1 : OFF_VT2);
#pragma unroll
    for (int m = 0; m < 2; ++m)
#pragma unroll
      for (int n = 0; n < 4; ++n) {
        u32x2 o = {pack2(acc[m][n][0], acc[m][n][1]), pack2(acc[m][n][2], acc[m][n][3])};
        *(u32x2*)(dst + tiled_off(wm * 48 + (e % 6) * 8 + wn * 4 + n, KK, 64 + m, lane)) = o;
      }
  }
}

constexpr int K3_NS = 4, K3_STAGE = 36 * 1024;
template <int QT>
__device__ __forceinline__ void k3_body(LDS_AS char* smem, rsrc_t rs, const int (&vo)[3], int wave, int lane, int qoff,
                                        const u16* __restrict__ xres, u16* __restrict__ outp, const f32x4 (&gate)[3], int col0) {
  auto issue = [&](int t, int buf) {
    LDS_AS char* sb = smem + buf * K3_STAGE + wave * 9216;
    int so = t < 11 ? t * 3072 : OOB;
#pragma unroll
    for (int i = 0; i < 3; ++i) {
      dma<0>(rs, sb + i * 3072, vo[i], so);
      dma<1024>(rs, sb + i * 3072, vo[i], so);
      dma<2048>(rs, sb + i * 3072, vo[i], so);
    }
  };
  const int wn = wave & 1;
  const int vrd = wn * 9216 + lane * 16, prd = 18432 + qoff * 3072 + lane * 16;
  LDS_AS float* fs = (LDS_AS float*)(smem + K3_NS * K3_STAGE) + qoff * 16 + (lane & 15);
  f32x4 acc[3][QT] = {};
  int buf = 0;
#pragma unroll 1
  for (int t = 0; t < 11; ++t) {
    WAIT_VM(9 * (K3_NS - 2));
    WAIT_LGKM0();
    BARRIER();
    int nb = buf + K3_NS - 1; if (nb >= K3_NS) nb -= K3_NS;
    issue(t + K3_NS - 1, nb);
    LDS_AS char* sb = smem + buf * K3_STAGE;
    f32x4 tmp[3][QT];
    float f[QT];
#pragma unroll
    for (int j = 0; j < QT; ++j) f[j] = fs[t * 80 + j * 16];
#pragma unroll
    for (int ks = 0; ks < 3; ++ks) {
      bf16x8 vf[3], pf[QT];
#pragma unroll
      for (int i = 0; i < 3; ++i) vf[i] = ldsfrag(sb + vrd + i * 3072 + ks * 1024);
#pragma unroll
      for (int j = 0; j < QT; ++j) pf[j] = ldsfrag(sb + prd + j * 3072 + ks * 1024);
#pragma unroll
      for (int i = 0; i < 3; ++i)
#pragma unroll
        for (int j = 0; j < QT; ++j) tmp[i][j] = mfma(vf[i], pf[j], ks == 0 ? f32x4{0.f, 0.f, 0.f, 0.f} : tmp[i][j]);
    }
#pragma unroll
    for (int i = 0; i < 3; ++i)
#pragma unroll
      for (int j = 0; j < QT; ++j) acc[i][j] += tmp[i][j] * f[j];
    buf = buf + 1; if (buf >= K3_NS) buf = 0;
  }
  WAIT_VM(0);
#pragma unroll
  for (int j = 0; j < QT; ++j)
#pragma unroll
    for (int i = 0; i < 3; ++i) {
      const size_t o = (size_t)((qoff + j) * 16 + (lane & 15)) * D + col0 + i * 16;
      u32x2 xv = *(const u32x2*)(xres + o);
      u32x2 r = {pack2(fmaf(gate[i][0], acc[i][j][0], bflo(xv[0])), fmaf(gate[i][1], acc[i][j][1], bfhi(xv[0]))),
                 pack2(fmaf(gate[i][2], acc[i][j][2], bflo(xv[1])), fmaf(gate[i][3], acc[i][j][3], bfhi(xv[1])))};
      *(u32x2*)(outp + o) = r;
    }
}

__global__ __launch_bounds__(256) void k3_pv(const u16* __restrict__ x_img, const u16* __restrict__ x_txt, char* __restrict__ ws,
                                             u16* __restrict__ out) {
  __shared__ __attribute__((aligned(1024))) char smem_[K3_NS * K3_STAGE + 11 * 80 * 4];
  LDS_AS char* smem = (LDS_AS char*)smem_;
  const int bid = blockIdx.x, tid = threadIdx.x, lane = tid & 63;
  const int wave = __builtin_amdgcn_readfirstlane(tid >> 6);
  const int x = bid & 7, idx = bid >> 3, b = x & 1;
  int rt0, nrt, nb, txt;
  if (idx < 26) {
    const int u = (x >> 1) * 26 + idx, rb = (u % 52) >> 2;
    nb = (u / 52) * 4 + (u & 3); rt0 = rb * 5; nrt = rb == 12 ? 4 : 5; txt = 0;
  } else {
    nb = (x >> 1) * 2 + idx - 26; rt0 = 64; nrt = 2; txt = 1;
  }
  rsrc_t rs = make_rsrc(ws, WS_END);
  int vo[3];
#pragma unroll
  for (int i = 0; i < 3; ++i) {
    const int u = wave * 3 + i;
    int o = u < 6 ? (int)(txt ? OFF_VT2 : OFF_VT1) + (b * 48 + nb * 6 + u) * KK * 1024
                  : (int)OFF_P + (b * RT + rt0 + u - 6) * KK * 1024;
    vo[i] = (u - 6 < nrt) ? o + lane * 16 : OOB;
  }
  {
    auto issue0 = [&](int t) {
      LDS_AS char* sb = smem + t * K3_STAGE + wave * 9216;
#pragma unroll
      for (int i = 0; i < 3; ++i) {
        dma<0>(rs, sb + i * 3072, vo[i], t * 3072);
        dma<1024>(rs, sb + i * 3072, vo[i], t * 3072);
        dma<2048>(rs, sb + i * 3072, vo[i], t * 3072);
      }
    };
    const float* SM = (const float*)(ws + OFF_SM) + b * 11 * NK + rt0 * 16;
    const float* SL = (const float*)(ws + OFF_SL) + b * 22 * NK + rt0 * 16;
    LDS_AS float* fs = (LDS_AS float*)(smem + K3_NS * K3_STAGE);
    if (tid < nrt * 16) {
      float m[11], l[11];
#pragma unroll
      for (int t = 0; t < 11; ++t) { m[t] = SM[t * NK + tid]; l[t] = SL[2 * t * NK + tid] + SL[(2 * t + 1) * NK + tid]; }
      float M = m[0];
#pragma unroll
      for (int t = 1; t < 11; ++t) M = fmaxf(M, m[t]);
      float L = 0.f;
#pragma unroll
      for (int t = 0; t < 11; ++t) { m[t] = __builtin_amdgcn_exp2f(m[t] - M); L = fmaf(m[t], l[t], L); }
      const float inv = 1.0f / L;
#pragma unroll
      for (int t = 0; t < 11; ++t) fs[t * 80 + tid] = m[t] * inv;
    }
#pragma unroll
    for (int s = 0; s < K3_NS - 1; ++s) issue0(s);
  }
  const int wn = wave & 1, wq = wave >> 1;
  const int col0 = nb * 96 + wn * 48 + (lane >> 4) * 4;
  f32x4 gate[3];
  {
    const float* mod = (const float*)(ws + OFF_MOD) + (txt * 2 + b) * 2304 + 1536 + col0;
#pragma unroll
    for (int i = 0; i < 3; ++i) {
      gate[i] = *(const f32x4*)(mod + i * 16);
#pragma unroll
      for (int k = 1; k < KSPL; ++k) gate[i] += *(const f32x4*)(mod + k * 4 * 2304 + i * 16);
    }
  }
  const u16* xres = txt ? x_txt + (size_t)b * NTX * D : x_img + (size_t)(b * NI + rt0 * 16) * D;
  u16* outp = txt ? out + (size_t)2 * NI * D + (size_t)b * NTX * D : out + (size_t)(b * NI + rt0 * 16) * D;
  const int q0 = (nrt + 1) >> 1;
  const int qt = wq ? nrt - q0 : q0, qoff = wq ? q0 : 0;
  if (qt == 3) k3_body<3>(smem, rs, vo, wave, lane, qoff, xres, outp, gate, col0);
  else if (qt == 2) k3_body<2>(smem, rs, vo, wave, lane, qoff, xres, outp, gate, col0);
  else k3_body<1>(smem, rs, vo, wave, lane, qoff, xres, outp, gate, col0);
}

extern "C" void kernel_launch(void* const* d_in, const int* in_sizes, int n_in, void* d_out, int out_size, void* d_ws,
                              size_t ws_size, hipStream_t stream) {
  const u16* x_img = (const u16*)d_in[0];
  const u16* x_txt = (const u16*)d_in[1];
  const u16* cvec = (const u16*)d_in[2];
  const u16* wqkv_img = (const u16*)d_in[3];
  const u16* wqkv_txt = (const u16*)d_in[4];
  const u16* wo_img = (const u16*)d_in[5];
  const u16* wo_txt = (const u16*)d_in[6];
  const u16* wmod_img = (const u16*)d_in[7];
  const u16* wmod_txt = (const u16*)d_in[8];
  char* ws = (char*)d_ws;
  k0_prep<<<K0_T3 + K0_T1 + K0_T2, 256, 0, stream>>>(x_img, x_txt, cvec, wqkv_img, wo_img, wo_txt, wmod_img, wmod_txt, ws);
  k1_proj<<<252, 512, 0, stream>>>(wqkv_img, wqkv_txt, ws);
  k2_scores<<<254, 256, 0, stream>>>(wo_img, wo_txt, ws);
  k3_pv<<<224, 256, 0, stream>>>(x_img, x_txt, ws, (u16*)d_out);
}
```

```cpp
#include <hip/hip_runtime.h>
#include <hip/hip_bf16.h>
#include <stdint.h>
#include <type_traits>

typedef __attribute__((ext_vector_type(8))) __bf16 bf16x8;
typedef __attribute__((ext_vector_type(4))) __bf16 bf16x4;
typedef __attribute__((ext_vector_type(2))) __bf16 bf16x2;
typedef __attribute__((ext_vector_type(4))) float f32x4;
typedef __attribute__((ext_vector_type(2))) float f32x2;
typedef __attribute__((ext_vector_type(4))) unsigned u32x4;
typedef __attribute__((ext_vector_type(2))) unsigned u32x2;
#define LDS_AS __attribute__((address_space(3)))
typedef __amdgpu_buffer_rsrc_t rsrc_t;
typedef unsigned short u16;

constexpr int D = 768, NI = 1024, NTX = 32, NK = 1056;
constexpr int RT = 66;
constexpr int KD = 24;
constexpr int KK = 33;
constexpr int KSPL = 2;
constexpr int OOB = 0x7f000000;

constexpr unsigned SZ_TOK = 2u * RT * KD * 1024u;
constexpr unsigned SZ_VT = 2u * 48u * KK * 1024u;
constexpr unsigned OFF_XN = 0;
constexpr unsigned OFF_Q = OFF_XN + SZ_TOK;
constexpr unsigned OFF_K = OFF_Q + SZ_TOK;
constexpr unsigned OFF_VX = OFF_K + SZ_TOK;
constexpr unsigned OFF_VT1 = OFF_VX + 4u * KD * 1024u;
constexpr unsigned OFF_VT2 = OFF_VT1 + SZ_VT;
constexpr unsigned OFF_P = OFF_VT2 + SZ_VT;
constexpr unsigned OFF_WVO = OFF_P + 2u * RT * KK * 1024u;
constexpr unsigned OFF_MOD = OFF_WVO + 96u * KD * 1024u;
constexpr unsigned OFF_SM = OFF_MOD + KSPL * 4u * 2304u * 4u;
constexpr unsigned OFF_SL = OFF_SM + 2u * 11u * NK * 4u;
constexpr unsigned WS_END = OFF_SL + 2u * 22u * NK * 4u;

#define WAIT_VM(n) asm volatile("s_waitcnt vmcnt(%0)" ::"n"(n) : "memory")
#define WAIT_LGKM(n) asm volatile("s_waitcnt lgkmcnt(%0)" ::"n"(n) : "memory")
#define BARRIER() __builtin_amdgcn_s_barrier()
#define TIE(x) asm volatile("" : "+v"(x))
#define SCHED_FENCE() __builtin_amdgcn_sched_barrier(0)

#define STAMP(ws, k, i) do { } while (0)

template <int I, int N, class F>
__device__ __forceinline__ void static_for(F&& f) {
  if constexpr (I < N) { f(std::integral_constant<int, I>{}); static_for<I + 1, N>(f); }
}
__device__ __forceinline__ rsrc_t make_rsrc(const void* p, unsigned bytes) {
  return __builtin_amdgcn_make_buffer_rsrc((void*)p, 0, (int)bytes, 0x00020000);
}
__device__ __forceinline__ u32x4 make_srd(const void* p, unsigned bytes) {
  unsigned long long a = (unsigned long long)p;
  u32x4 r;
  r[0] = (unsigned)a; r[1] = (unsigned)(a >> 32) & 0xffffu; r[2] = bytes; r[3] = 0x00020000u;
  return r;
}
template <int IMM>
__device__ __forceinline__ void dma(rsrc_t rs, LDS_AS char* lds, int voff, int soff) {
  __builtin_amdgcn_raw_ptr_buffer_load_lds(rs, (LDS_AS void*)lds, 16, voff, soff, IMM, 0);
}
__device__ __forceinline__ u32x4 aload(const u32x4& srd, int voff, int soff) {
  u32x4 r;
  asm volatile("buffer_load_dwordx4 %0, %1, %2, %3 offen" : "=v"(r) : "v"(voff), "s"(srd), "s"(soff));
  return r;
}
template <int OFF>
__device__ __forceinline__ void rd_tr(bf16x4& lo, bf16x4& hi, unsigned addr) {
  asm volatile("ds_read_b64_tr_b16 %0, %2 offset:%c3\n\tds_read_b64_tr_b16 %1, %2 offset:%c4"
               : "=&v"(lo), "=&v"(hi) : "v"(addr), "i"(OFF), "i"(OFF + 512));
}
template <int OFF>
__device__ __forceinline__ void rd_128(bf16x8& r, unsigned addr) {
  asm volatile("ds_read_b128 %0, %1 offset:%c2" : "=v"(r) : "v"(addr), "i"(OFF));
}
__device__ __forceinline__ bf16x8 cat(bf16x4 lo, bf16x4 hi) { return __builtin_shufflevector(lo, hi, 0, 1, 2, 3, 4, 5, 6, 7); }
__device__ __forceinline__ unsigned lds_addr(LDS_AS char* p) { return (unsigned)(size_t)p; }
__device__ __forceinline__ int wrapn(int v, int n) { return v >= n ? v - n : v; }
__device__ __forceinline__ f32x4 mfma(bf16x8 a, bf16x8 b, f32x4 c) {
  return __builtin_amdgcn_mfma_f32_16x16x32_bf16(a, b, c, 0, 0, 0);
}
__device__ __forceinline__ unsigned pack2(float a, float b) {
  f32x2 f = {a, b};
  bf16x2 h = __builtin_convertvector(f, bf16x2);
  return __builtin_bit_cast(unsigned, h);
}
__device__ __forceinline__ u32x2 pack4(f32x4 v) { return u32x2{pack2(v[0], v[1]), pack2(v[2], v[3])}; }
__device__ __forceinline__ float bflo(unsigned v) { return __uint_as_float(v << 16); }
__device__ __forceinline__ float bfhi(unsigned v) { return __uint_as_float(v & 0xffff0000u); }
__device__ __forceinline__ float red4_max(float x) {
  auto a = __builtin_amdgcn_permlane16_swap(__float_as_uint(x), __float_as_uint(x), false, false);
  x = fmaxf(__uint_as_float(a[0]), __uint_as_float(a[1]));
  auto b = __builtin_amdgcn_permlane32_swap(__float_as_uint(x), __float_as_uint(x), false, false);
  return fmaxf(__uint_as_float(b[0]), __uint_as_float(b[1]));
}
__device__ __forceinline__ float red4_sum(float x) {
  auto a = __builtin_amdgcn_permlane16_swap(__float_as_uint(x), __float_as_uint(x), false, false);
  x = __uint_as_float(a[0]) + __uint_as_float(a[1]);
  auto b = __builtin_amdgcn_permlane32_swap(__float_as_uint(x), __float_as_uint(x), false, false);
  return __uint_as_float(b[0]) + __uint_as_float(b[1]);
}
__device__ __forceinline__ unsigned tiled_lane(int lane) {
  int g = lane >> 4;
  return (unsigned)(((lane & 15) + 16 * (g >> 1)) * 16 + (g & 1) * 8);
}
__device__ __forceinline__ unsigned tiled_blk(int rowtile, int KS, int T16) {
  return (unsigned)((rowtile * KS + (T16 >> 1)) * 1024 + (T16 & 1) * 512);
}
__device__ __forceinline__ int timg_src(int lane, int sigma, int pitch) {
  int r = lane >> 3, c = (lane & 7) ^ (2 * ((r >> 1) & 1) + 4 * sigma);
  return r * pitch + c * 16;
}
template <int NB>
__device__ __forceinline__ int timg_rd(int lane, int nt) {
  int g = lane >> 4, q = (lane & 15) >> 2, p = lane & 3;
  int f = 2 * ((q >> 1) & 1) + 4 * (g & 1);
  return (g * NB + (nt >> 2)) * 1024 + q * 128 + (((2 * (nt & 3)) ^ f) + (p >> 1)) * 16 + (p & 1) * 8;
}

template <int MT, int NT>
struct Frags { bf16x8 a[MT]; bf16x8 b[NT]; bf16x4 l[NT], h[NT]; };

template <int MT, int NT, bool BTR, int AM, int BM, int AO, int BO, int I>
__device__ __forceinline__ void rd_item(Frags<MT, NT>& n, unsigned ab, const unsigned (&bb)[NT]) {
  if constexpr (I < NT) {
    if constexpr (BTR) rd_tr<BO>(n.l[I], n.h[I], bb[I]);
    else rd_128<BO + I * BM>(n.b[I], bb[0]);
  } else if constexpr (I < NT + MT) {
    rd_128<AO + (I - NT) * AM>(n.a[I - NT], ab);
  }
}
template <int MT, int NT, bool BTR, int AM, int BM, int AO, int BO>
__device__ __forceinline__ void rd_set(Frags<MT, NT>& n, unsigned ab, const unsigned (&bb)[NT]) {
  static_for<0, MT + NT>([&](auto ic) { rd_item<MT, NT, BTR, AM, BM, AO, BO, decltype(ic)::value>(n, ab, bb); });
}
template <int MT, int NT, bool BTR>
__device__ __forceinline__ void land(Frags<MT, NT>& c) {
  WAIT_LGKM(0);
#pragma unroll
  for (int j = 0; j < NT; ++j) {
    if constexpr (BTR) { TIE(c.l[j]); TIE(c.h[j]); } else TIE(c.b[j]);
  }
#pragma unroll
  for (int m = 0; m < MT; ++m) TIE(c.a[m]);
  SCHED_FENCE();
}
template <int MT, int NT, bool BTR, bool SWAP, bool ZC, int AM, int BM, int AO, int BO, class X>
__device__ __forceinline__ void kstep(f32x4 (&acc)[MT][NT], Frags<MT, NT>& c, Frags<MT, NT>& n, unsigned ab,
                                      const unsigned (&bb)[NT], X&& extra) {
  land<MT, NT, BTR>(c);
  static_for<0, MT * NT>([&](auto ic) {
    constexpr int I = decltype(ic)::value, m = I / NT, j = I % NT;
    bf16x8 bf;
    if constexpr (BTR) bf = cat(c.l[j], c.h[j]); else bf = c.b[j];
    const f32x4 ci = ZC ? f32x4{0.f, 0.f, 0.f, 0.f} : acc[m][j];
    acc[m][j] = SWAP ? mfma(bf, c.a[m], ci) : mfma(c.a[m], bf, ci);
    rd_item<MT, NT, BTR, AM, BM, AO, BO, I>(n, ab, bb);
    extra(ic);
    SCHED_FENCE();
  });
  static_for<MT * NT, (MT + NT > MT * NT ? MT + NT : MT * NT)>(
      [&](auto ic) { rd_item<MT, NT, BTR, AM, BM, AO, BO, decltype(ic)::value>(n, ab, bb); });
}
struct NoExtra { template <class T> __device__ __forceinline__ void operator()(T) const {} };

template <int NST, int NS, int V, class Issue>
__device__ __forceinline__ void loader_loop(Issue&& issue) {
#pragma unroll
  for (int s = 0; s < NS - 1; ++s) issue(s, s);
  WAIT_VM(V * (NS - 2));
  BARRIER();
  int slot = NS - 1;
#pragma unroll 1
  for (int t = 0; t < NST; ++t) {
    WAIT_VM(V * (NS - 3));
    BARRIER();
    issue(t + NS - 1, slot);
    slot = wrapn(slot + 1, NS);
  }
}

template <int MW, int NW, int NS, bool A_ROWMAJOR>
__device__ __forceinline__ void small_gemm(LDS_AS char* smem, rsrc_t ra, int a_off, int a_pitch, rsrc_t rb, int b_off,
                                           int b_pitch, int rot, int wave8, int lane, f32x4 (&acc)[MW][NW]) {
  constexpr int NB = (2 * NW) / 4;
  constexpr int ABLK = 4 * MW;
  constexpr int STAGE = (ABLK + 8 * NB) * 1024;
  constexpr int AV = ABLK / 4, BV = 2 * NB;
  const int wave = wave8 & 3;
  if (wave8 >= 4) {
    int av[AV];
#pragma unroll
    for (int i = 0; i < AV; ++i) {
      const int u = wave * AV + i;
      if (A_ROWMAJOR) {
        const int r = u * 8 + (lane >> 3);
        av[i] = a_off + r * a_pitch + (((lane & 7) ^ ((r >> 1) & 7)) * 16);
      } else {
        av[i] = a_off + (u >> 1) * a_pitch + (u & 1) * 1024 + lane * 16;
      }
    }
    int bv[2];
    bv[0] = b_off + (wave * 16) * b_pitch + timg_src(lane, 0, b_pitch);
    bv[1] = b_off + (wave * 16 + 8) * b_pitch + timg_src(lane, 1, b_pitch);
    loader_loop<12, NS, AV + BV>([&](int t, int slot) {
      LDS_AS char* sb = smem + slot * STAGE;
      const int kt = wrapn(t + rot, 12);
      const int soa = t < 12 ? kt * (A_ROWMAJOR ? 128 : 2048) : OOB;
      const int sob = t < 12 ? kt * 64 * b_pitch : OOB;
#pragma unroll
      for (int i = 0; i < AV; ++i) dma<0>(ra, sb + (wave * AV + i) * 1024, av[i], soa);
#pragma unroll
      for (int h = 0; h < 2; ++h)
#pragma unroll
        for (int nb = 0; nb < NB; ++nb) dma<0>(rb, sb + (ABLK + (2 * wave + h) * NB + nb) * 1024, bv[h] + nb * 128, sob);
    });
    return;
  }
  const int wm = wave >> 1, wn = wave & 1;
  unsigned a0, a1;
  if (A_ROWMAJOR) {
    const int s = (lane & 15) >> 1, g = lane >> 4;
    a0 = lds_addr(smem) + wm * MW * 2048 + (lane & 15) * 128 + ((g ^ s) * 16);
    a1 = lds_addr(smem) + wm * MW * 2048 + (lane & 15) * 128 + (((4 + g) ^ s) * 16);
  } else {
    a0 = lds_addr(smem) + wm * MW * 2048 + lane * 16;
    a1 = a0 + 1024;
  }
  unsigned brd[NW], bc[NW];
#pragma unroll
  for (int n = 0; n < NW; ++n) brd[n] = lds_addr(smem) + ABLK * 1024 + timg_rd<NB>(lane, wn * NW + n);
  Frags<MW, NW> f0, f1;
  BARRIER();
  rd_set<MW, NW, true, 2048, 0, 0, 0>(f0, a0, brd);
  int so = 0;
#pragma unroll 1
  for (int t = 0; t < 12; ++t) {
    BARRIER();
#pragma unroll
    for (int n = 0; n < NW; ++n) bc[n] = brd[n] + so;
    kstep<MW, NW, true, false, false, 2048, 0, 0, 4 * NB * 1024>(acc, f0, f1, a1 + so, bc, NoExtra{});
    so = wrapn(so + STAGE, NS * STAGE);
#pragma unroll
    for (int n = 0; n < NW; ++n) bc[n] = brd[n] + so;
    kstep<MW, NW, true, false, false, 2048, 0, 0, 0>(acc, f1, f0, a0 + so, bc, NoExtra{});
  }
  land<MW, NW, true>(f0);
}

constexpr int K0_T1 = 72 * KSPL, K0_T2 = 2 * RT, K0_T3 = 192, K0_PAD = (8 - (K0_T1 + K0_T2) % 8) % 8;
__global__ __launch_bounds__(512, 4) void k0_prep(const u16* __restrict__ x_img, const u16* __restrict__ x_txt,
                                                  const u16* __restrict__ cvec, const u16* __restrict__ wqkv_img,
                                                  const u16* __restrict__ wo_img, const u16* __restrict__ wo_txt,
                                                  const u16* __restrict__ wmod_img, const u16* __restrict__ wmod_txt,
                                                  char* __restrict__ ws) {
  __shared__ __attribute__((aligned(1024))) char smem_[81920];
  LDS_AS char* smem = (LDS_AS char*)smem_;
  const int bid = blockIdx.x, tid = threadIdx.x, lane = tid & 63;
  const int wave = __builtin_amdgcn_readfirstlane(tid >> 6);
  STAMP(ws, 0, 0);
  if (bid >= K0_T1 + K0_T2 + K0_PAD) {
    const int id = bid - (K0_T1 + K0_T2 + K0_PAD), x = id & 7, idx = id >> 3;
    const int mb = (x & 1) * 4 + (idx & 3), nbk = (x >> 1) * 6 + (idx >> 2);
    rsrc_t ra = make_rsrc(wqkv_img, D * 3 * D * 2);
    rsrc_t rb = make_rsrc(nbk < 12 ? wo_img : wo_txt, D * D * 2);
    f32x4 acc[3][2] = {};
    const int rot = ((idx & 3) * 3 + (idx >> 2) * 2) % 12;
    small_gemm<3, 2, 4, true>(smem, ra, mb * 96 * (3 * D * 2) + 2 * D * 2, 3 * D * 2, rb, (nbk % 12) * 128, D * 2, rot, wave, lane, acc);
    if (wave >= 4) return;
    STAMP(ws, 0, 2);
    const int wm = wave >> 1, wn = wave & 1;
    char* dst = ws + OFF_WVO + tiled_lane(lane);
#pragma unroll
    for (int m = 0; m < 3; ++m)
#pragma unroll
      for (int n = 0; n < 2; ++n) *(u32x2*)(dst + tiled_blk(nbk * 4 + wn * 2 + n, KD, mb * 6 + wm * 3 + m)) = pack4(acc[m][n]);
  } else if (wave >= 4 || bid >= K0_T1 + K0_T2) {
    return;
  } else if (bid < K0_T1) {
    constexpr int ROWS = D / KSPL, PER = ROWS / 32;
    const int i = bid, s = i / (36 * KSPL), rem = i % (36 * KSPL), cb = rem % 36, kh = rem / 36;
    const u16* w = (s ? wmod_txt : wmod_img) + (size_t)(kh * ROWS + (tid >> 3)) * (6 * D) + cb * 64 + (tid & 7) * 8;
    u16 cv[3];
#pragma unroll
    for (int e = 0; e < 3; ++e) { const int z = tid + e * 256; cv[e] = cvec[(z / ROWS) * D + kh * ROWS + z % ROWS]; }
    u32x4 wv[PER];
#pragma unroll
    for (int r = 0; r < PER; ++r) wv[r] = *(const u32x4*)(w + (size_t)r * 32 * (6 * D));
    LDS_AS float* sc = (LDS_AS float*)smem;
    LDS_AS float* red = (LDS_AS float*)(smem + 8192);
#pragma unroll
    for (int e = 0; e < 3; ++e) {
      const int z = tid + e * 256;
      const float v = __uint_as_float((unsigned)cv[e] << 16);
      sc[(z % ROWS) * 2 + z / ROWS] = v / (1.0f + __expf(-v));
    }
    __syncthreads();
    float a0[8] = {}, a1[8] = {};
#pragma unroll
    for (int r = 0; r < PER; ++r) {
      f32x2 sv = *(LDS_AS f32x2*)(sc + ((tid >> 3) + r * 32) * 2);
#pragma unroll
      for (int j = 0; j < 4; ++j) {
        float lo = bflo(wv[r][j]), hi = bfhi(wv[r][j]);
        a0[2 * j] = fmaf(sv[0], lo, a0[2 * j]); a0[2 * j + 1] = fmaf(sv[0], hi, a0[2 * j + 1]);
        a1[2 * j] = fmaf(sv[1], lo, a1[2 * j]); a1[2 * j + 1] = fmaf(sv[1], hi, a1[2 * j + 1]);
      }
    }
    LDS_AS float* rp = red + (tid >> 3) * 128 + (tid & 7) * 8;
    *(LDS_AS f32x4*)(rp) = f32x4{a0[0], a0[1], a0[2], a0[3]};
    *(LDS_AS f32x4*)(rp + 4) = f32x4{a0[4], a0[5], a0[6], a0[7]};
    *(LDS_AS f32x4*)(rp + 64) = f32x4{a1[0], a1[1], a1[2], a1[3]};
    *(LDS_AS f32x4*)(rp + 68) = f32x4{a1[4], a1[5], a1[6], a1[7]};
    __syncthreads();
    {
      const int o = tid >> 1, hf = tid & 1;
      float t0 = 0.f, t1 = 0.f;
#pragma unroll
      for (int g = 0; g < 16; g += 2) { t0 += red[(hf * 16 + g) * 128 + o]; t1 += red[(hf * 16 + g + 1) * 128 + o]; }
      float t = t0 + t1;
      t += __shfl_xor(t, 1);
      float* mod = (float*)(ws + OFF_MOD);
      if (hf == 0) mod[((kh * 2 + s) * 2 + (o >> 6)) * 2304 + cb * 64 + (o & 63)] = t;
    }
  } else {
    const int i = bid - K0_T1, b = i / RT, rt = i % RT;
    const u16* src = rt < 64 ? x_img + (size_t)(b * NI + rt * 16) * D : x_txt + (size_t)(b * NTX + (rt - 64) * 16) * D;
    src += (lane & 15) * D + wave * 192 + (lane >> 4) * 8;
    u32x4 v[6];
#pragma unroll
    for (int k = 0; k < 6; ++k) v[k] = *(const u32x4*)(src + k * 32);
    LDS_AS float* red = (LDS_AS float*)smem;
    float s0 = 0.f, s1 = 0.f;
#pragma unroll
    for (int k = 0; k < 6; ++k)
#pragma unroll
      for (int j = 0; j < 4; ++j) { s0 += bflo(v[k][j]); s1 += bfhi(v[k][j]); }
    const float s = red4_sum(s0 + s1);
    if (lane < 16) red[wave * 16 + lane] = s;
    __syncthreads();
    const int r = lane & 15;
    const float mean = ((red[r] + red[16 + r]) + (red[32 + r] + red[48 + r])) * (1.0f / D);
    float q0 = 0.f, q1 = 0.f;
#pragma unroll
    for (int k = 0; k < 6; ++k)
#pragma unroll
      for (int j = 0; j < 4; ++j) {
        float lo = bflo(v[k][j]) - mean, hi = bfhi(v[k][j]) - mean;
        q0 = fmaf(lo, lo, q0); q1 = fmaf(hi, hi, q1);
      }
    const float q = red4_sum(q0 + q1);
    if (lane < 16) red[64 + wave * 16 + lane] = q;
    __syncthreads();
    const float var = ((red[64 + r] + red[80 + r]) + (red[96 + r] + red[112 + r])) * (1.0f / D);
    const float rstd = 1.0f / sqrtf(var + 1e-5f);
    char* dst = ws + OFF_XN + (size_t)(i * KD + wave * 6) * 1024 + lane * 16;
#pragma unroll
    for (int k = 0; k < 6; ++k) {
      u32x4 o;
#pragma unroll
      for (int j = 0; j < 4; ++j) o[j] = pack2((bflo(v[k][j]) - mean) * rstd, (bfhi(v[k][j]) - mean) * rstd);
      *(u32x4*)(dst + k * 1024) = o;
    }
  }
  STAMP(ws, 0, 3);
}

constexpr int K1_DUMP = 3 * 18432, K1_B = K1_DUMP + 1024, K1_BSZ = 24 * 1024, K1_GS = K1_B + 4 * K1_BSZ;
constexpr int K1_LDS = K1_GS + 6144;

__device__ __forceinline__ u32x4 modulate(u32x4 v, f32x4 g0, f32x4 g1, f32x4 s0, f32x4 s1) {
  u32x4 o;
  o[0] = pack2(fmaf(bflo(v[0]), g0[0], s0[0]), fmaf(bfhi(v[0]), g0[1], s0[1]));
  o[1] = pack2(fmaf(bflo(v[1]), g0[2], s0[2]), fmaf(bfhi(v[1]), g0[3], s0[3]));
  o[2] = pack2(fmaf(bflo(v[2]), g1[0], s1[0]), fmaf(bfhi(v[2]), g1[1], s1[1]));
  o[3] = pack2(fmaf(bflo(v[3]), g1[2], s1[2]), fmaf(bfhi(v[3]), g1[3], s1[3]));
  return o;
}

template <int MT, bool VT>
__device__ __forceinline__ void k1_body(LDS_AS char* smem, char* __restrict__ ws, const u16* __restrict__ wsrc, int gA, int gB,
                                        int cb, int smod, int rot, int wave8, int lane, int tid) {
  constexpr bool BIG = MT == 9;
  constexpr int MH = BIG ? 9 : MT / 2;
  constexpr int NA = BIG ? 5 : (MT == 4 ? 2 : 1);
  constexpr int ASZ = MT * 2048;
  constexpr int GS = BIG ? K1_GS : 32768;
  constexpr int WP = 3 * D * 2;
  const int wave = wave8 & 3;
  rsrc_t rb = VT ? make_rsrc(ws + OFF_WVO, 96 * KD * 1024) : make_rsrc(wsrc, D * 3 * D * 2);
  int bvo[3];
  if (VT) {
#pragma unroll
    for (int i = 0; i < 3; ++i) bvo[i] = ((cb - 8) * 12 + wave * 3 + i) * KD * 1024 + lane * 16;
  } else {
    bvo[0] = wave * 16 * WP + cb * 384 + timg_src(lane, 0, WP);
    bvo[1] = (wave * 16 + 8) * WP + cb * 384 + timg_src(lane, 1, WP);
    bvo[2] = 0;
  }
  auto issueB = [&](int t) {
    LDS_AS char* sb = smem + K1_B + (t & 3) * K1_BSZ + wave * 6144;
    const int so = t < 12 ? wrapn(t + rot, 12) * (VT ? 2048 : 64 * WP) : OOB;
    if (VT) {
#pragma unroll
      for (int i = 0; i < 3; ++i) { dma<0>(rb, sb + i * 2048, bvo[i], so); dma<1024>(rb, sb + i * 2048, bvo[i], so); }
    } else {
#pragma unroll
      for (int h = 0; h < 2; ++h)
#pragma unroll
        for (int i = 0; i < 3; ++i) dma<0>(rb, sb + (h * 3 + i) * 1024, bvo[h] + i * 128, so);
    }
  };

  if (wave8 >= 4) {
    const u32x4 srd_ws = make_srd(ws, WS_END);
    const int ptid = tid - 256;
    int avo[NA], awr[NA];
#pragma unroll
    for (int i = 0; i < NA; ++i) {
      const int lt = (BIG && i == 4) ? 8 : (wave >> 1) + 2 * i;
      const int gt = lt < MH ? gA + lt : gB + (lt - MH);
      const bool act = (BIG && i == 4) ? (((lane & 15) >> 3) == (wave >> 1)) : true;
      avo[i] = act ? (int)OFF_XN + (gt * KD + (wave & 1)) * 1024 + lane * 16 : OOB;
      awr[i] = act ? (lt * 2 + (wave & 1)) * 1024 + lane * 16 : -1;
    }
    const unsigned gsrd = lds_addr(smem) + GS + ((wave & 1) * 32 + (lane >> 4) * 8) * 4;
    auto issueA = [&](u32x4 (&r)[NA], int t) {
      const int so = t < 12 ? wrapn(t + rot, 12) * 2048 : OOB;
#pragma unroll
      for (int i = 0; i < NA; ++i) r[i] = aload(srd_ws, avo[i], so);
    };
    auto xform = [&](u32x4 (&r)[NA], int t, int slot, auto vm) {
      constexpr int NG = BIG ? 1 : 2;
      f32x4 gs[NG][4];
#pragma unroll
      for (int z = 0; z < NG; ++z) {
        const unsigned gp = gsrd + wrapn(wrapn(t + rot, 12), 12) * 256 + (MT == 4 ? z * 6144 : (MT == 2 ? (wave >> 1) * 6144 : 0));
        if (z < NA)
          asm volatile("ds_read_b128 %0, %4\n\tds_read_b128 %1, %4 offset:16\n\tds_read_b128 %2, %4 offset:3072\n\tds_read_b128 %3, %4 offset:3088"
                       : "=&v"(gs[z][0]), "=&v"(gs[z][1]), "=&v"(gs[z][2]), "=&v"(gs[z][3]) : "v"(gp));
      }
      WAIT_VM(decltype(vm)::value);
      WAIT_LGKM(0);
#pragma unroll
      for (int z = 0; z < NG; ++z)
        if (z < NA) { TIE(gs[z][0]); TIE(gs[z][1]); TIE(gs[z][2]); TIE(gs[z][3]); }
#pragma unroll
      for (int i = 0; i < NA; ++i) {
        TIE(r[i]);
        const int z = MT == 4 ? i : 0;
        const u32x4 o = modulate(r[i], gs[z][2], gs[z][3], gs[z][0], gs[z][1]);
        const int dst = (BIG && i == 4) ? (awr[i] < 0 ? K1_DUMP + lane * 16 : slot * ASZ + awr[i]) : slot * ASZ + awr[i];
        *(LDS_AS u32x4*)(smem + dst) = o;
      }
    };
    constexpr int NM = BIG ? 2 : 3;
    u32x4 mp[NM][KSPL];
    const int bA = gA >= RT ? 1 : 0;
#pragma unroll
    for (int h = 0; h < NM; ++h) {
      const int item = ptid + h * 256;
      const int b = BIG ? bA : (item >= 384 ? 1 : 0), e = BIG ? item : (item >= 384 ? item - 384 : item);
#pragma unroll
      for (int k = 0; k < KSPL; ++k)
        mp[h][k] = aload(srd_ws, (BIG && item >= 384) ? OOB : (int)OFF_MOD + (((k * 2 + smod) * 2 + b) * 2304 + e * 4) * 4, 0);
    }
    u32x4 r0[NA], r1[NA];
    issueA(r0, 0);
    issueA(r1, 1);
    WAIT_VM(2 * NA);
#pragma unroll
    for (int h = 0; h < NM; ++h) {
      const int item = ptid + h * 256;
      const int e = item >= 384 ? item - 384 : item;
      TIE(mp[h][0]); TIE(mp[h][1]);
      f32x4 v = __builtin_bit_cast(f32x4, mp[h][0]) + __builtin_bit_cast(f32x4, mp[h][1]);
      if (e >= 192) v += 1.0f;
      const int dst = (BIG && item >= 384) ? K1_DUMP + lane * 16 : GS + item * 16;
      *(LDS_AS f32x4*)(smem + dst) = v;
    }
    WAIT_LGKM(0);
    BARRIER();
    xform(r0, 0, 0, std::integral_constant<int, NA>{});
    issueA(r0, 2);
    issueB(2);
    WAIT_LGKM(0);
    BARRIER();
    xform(r1, 1, 1, std::integral_constant<int, NA + 6>{});
    issueA(r1, 3);
    WAIT_LGKM(0);
    int slot = 2;
    auto pstep = [&](int t, u32x4 (&r)[NA]) {
      BARRIER();
      issueB(t + 3);
      xform(r, t + 2, slot, std::integral_constant<int, NA + 12>{});
      issueA(r, t + 4);
      WAIT_VM(2 * NA + 6);
      WAIT_LGKM(0);
      slot = wrapn(slot + 1, 3);
    };
#pragma unroll 1
    for (int t = 0; t < 12; t += 2) {
      pstep(t, r0);
      pstep(t + 1, r1);
    }
    WAIT_VM(0);
#pragma unroll
    for (int i = 0; i < NA; ++i) { TIE(r0[i]); TIE(r1[i]); }
    return;
  }

  issueB(0);
  issueB(1);
  const unsigned ard = lds_addr(smem) + lane * 16;
  unsigned brd[3], bc[3];
#pragma unroll
  for (int n = 0; n < 3; ++n) brd[n] = lds_addr(smem) + K1_B + (VT ? wave * 6144 + lane * 16 : timg_rd<3>(lane, wave * 3 + n));
  f32x4 acc[MT][3] = {};
  Frags<MT, 3> f0, f1;
  BARRIER();
  WAIT_VM(6);
  BARRIER();
  STAMP(ws, 1, 1);
  rd_set<MT, 3, !VT, 2048, 2048, 0, 0>(f0, ard, brd);
  WAIT_VM(0);
  int ao = 0;
#pragma unroll 1
  for (int t = 0; t < 12; ++t) {
    BARRIER();
#pragma unroll
    for (int n = 0; n < 3; ++n) bc[n] = brd[n] + (t & 3) * K1_BSZ;
    kstep<MT, 3, !VT, !VT, false, 2048, 2048, 1024, (VT ? 1024 : 12288)>(acc, f0, f1, ard + ao, bc, NoExtra{});
    ao = wrapn(ao + ASZ, 3 * ASZ);
#pragma unroll
    for (int n = 0; n < 3; ++n) bc[n] = brd[n] + ((t + 1) & 3) * K1_BSZ;
    kstep<MT, 3, !VT, !VT, false, 2048, 2048, 0, 0>(acc, f1, f0, ard + ao, bc, NoExtra{});
  }
  land<MT, 3, !VT>(f0);
  STAMP(ws, 1, 2);

  const int sec = cb >> 2;
  const int nt0 = (cb & 3) * 12 + wave * 3;
  if (VT) {
    char* dst = ws + (sec == 2 ? OFF_VT1 : OFF_VT2) + tiled_lane(lane);
#pragma unroll
    for (int m = 0; m < MT; ++m) {
      const int gt = m < MH ? gA + m : gB + (m - MH);
      const int b = gt >= RT ? 1 : 0, rt = gt - b * RT;
#pragma unroll
      for (int n = 0; n < 3; ++n) *(u32x2*)(dst + tiled_blk(b * 48 + nt0 + n, KK, rt)) = pack4(acc[m][n]);
    }
  } else {
    const float sc = sec == 0 ? 0.036084391824351615f * 1.4426950408889634f : 1.0f;
    char* dst = ws + (sec == 0 ? OFF_Q : (sec == 1 ? OFF_K : OFF_VX)) + tiled_lane(lane);
#pragma unroll
    for (int m = 0; m < MT; ++m) {
      const int gt = sec == 2 ? m : (m < MH ? gA + m : gB + (m - MH));
#pragma unroll
      for (int n = 0; n < 3; ++n) *(u32x2*)(dst + tiled_blk(gt, KD, nt0 + n)) = pack4(acc[m][n] * sc);
    }
  }
  STAMP(ws, 1, 3);
}

__global__ __launch_bounds__(512, 2) void k1_proj(const u16* __restrict__ wqkv_img, const u16* __restrict__ wqkv_txt,
                                                  char* __restrict__ ws) {
  __shared__ __attribute__((aligned(1024))) char smem_[K1_LDS];
  LDS_AS char* smem = (LDS_AS char*)smem_;
  const int bid = blockIdx.x, tid = threadIdx.x, lane = tid & 63;
  const int wave = __builtin_amdgcn_readfirstlane(tid >> 6);
  STAMP(ws, 1, 0);
  if (bid < 224) {
    const int x = bid & 7, idx = bid >> 3;
    const int gA = (x & 1) * RT + (idx % 7) * 9, cb = (x >> 1) * 4 + idx / 7;
    const int rot = ((idx % 7) * 2 + (idx / 7) * 3) % 12;
    if (cb < 8) k1_body<9, false>(smem, ws, wqkv_img, gA, gA, cb, 0, rot, wave, lane, tid);
    else k1_body<9, true>(smem, ws, wqkv_img, gA, gA, cb, 0, rot, wave, lane, tid);
  } else if (bid < 240) {
    const int cb = bid - 224;
    if (cb < 8) k1_body<2, false>(smem, ws, wqkv_img, 63, RT + 63, cb, 0, cb % 12, wave, lane, tid);
    else k1_body<2, true>(smem, ws, wqkv_img, 63, RT + 63, cb, 0, cb % 12, wave, lane, tid);
  } else {
    k1_body<4, false>(smem, ws, wqkv_txt, 64, RT + 64, bid - 240, 1, bid - 240, wave, lane, tid);
  }
}

constexpr int K2_NS = 6, K2_STAGE = 24 * 1024;
__global__ __launch_bounds__(512) void k2_scores(const u16* __restrict__ wo_img, const u16* __restrict__ wo_txt,
                                                 char* __restrict__ ws) {
  __shared__ __attribute__((aligned(1024))) char smem_[K2_NS * K2_STAGE + 1024];
  LDS_AS char* smem = (LDS_AS char*)smem_;
  const int bid = blockIdx.x, tid = threadIdx.x, lane = tid & 63;
  const int wave8 = __builtin_amdgcn_readfirstlane(tid >> 6), wave = wave8 & 3;
  const int x = bid & 7, idx = bid >> 3;
  STAMP(ws, 2, 0);
  if (idx < 30 || (idx == 30 && x < 2)) {
    const int b = x & 1, qd = x >> 1;
    int ti, tj;
    if (idx == 30) { ti = 5; tj = 5; }
    else if (qd == 0) { ti = idx / 6; tj = idx % 6; }
    else if (qd == 1) { ti = idx / 5; tj = 6 + idx % 5; }
    else if (qd == 2) { ti = 6 + idx / 6; tj = 5 + idx % 6; }
    else { ti = 5 + idx / 5; tj = idx % 5; }
    if (wave8 >= 4) {
      const int rot = (2 * (ti + tj)) % 12;
      rsrc_t rs = make_rsrc(ws, WS_END);
      const int vbase = (wave < 2 ? (int)OFF_K + (b * RT + tj * 6 + wave * 3) * KD * 1024
                                  : (int)OFF_Q + (b * RT + ti * 6 + (wave - 2) * 3) * KD * 1024) + lane * 16;
      loader_loop<12, K2_NS, 6>([&](int t, int slot) {
        LDS_AS char* sb = smem + slot * K2_STAGE + wave * 6144;
        const int so = t < 12 ? wrapn(t + rot, 12) * 2048 : OOB;
#pragma unroll
        for (int i = 0; i < 3; ++i) {
          dma<0>(rs, sb + i * 2048, vbase + i * KD * 1024, so);
          dma<1024>(rs, sb + i * 2048, vbase + i * KD * 1024, so);
        }
      });
      return;
    }
    const int wk = wave >> 1, wq = wave & 1;
    const unsigned krd = lds_addr(smem) + wk * 6144 + lane * 16, qrd = lds_addr(smem) + 12288 + wq * 6144 + lane * 16;
    f32x4 acc[3][3] = {};
    Frags<3, 3> f0, f1;
    unsigned bc[3] = {qrd, 0, 0};
    BARRIER();
    rd_set<3, 3, false, 2048, 2048, 0, 0>(f0, krd, bc);
    int so = 0;
#pragma unroll 1
    for (int t = 0; t < 12; ++t) {
      BARRIER();
      kstep<3, 3, false, false, false, 2048, 2048, 1024, 1024>(acc, f0, f1, krd + so, bc, NoExtra{});
      so = wrapn(so + K2_STAGE, K2_NS * K2_STAGE);
      bc[0] = qrd + so;
      kstep<3, 3, false, false, false, 2048, 2048, 0, 0>(acc, f1, f0, krd + so, bc, NoExtra{});
    }
    land<3, 3, false>(f0);
    STAMP(ws, 2, 2);
    LDS_AS float* red = (LDS_AS float*)(smem + K2_NS * K2_STAGE);
    float mx[3];
#pragma unroll
    for (int j = 0; j < 3; ++j) {
      float m0 = fmaxf(fmaxf(acc[0][j][0], acc[0][j][1]), fmaxf(acc[0][j][2], acc[0][j][3]));
      float m1 = fmaxf(fmaxf(acc[1][j][0], acc[1][j][1]), fmaxf(acc[1][j][2], acc[1][j][3]));
      float m2 = fmaxf(fmaxf(acc[2][j][0], acc[2][j][1]), fmaxf(acc[2][j][2], acc[2][j][3]));
      mx[j] = red4_max(fmaxf(fmaxf(m0, m1), m2));
    }
    {
      const int g = lane >> 4;
      const float mine = g == 0 ? mx[0] : (g == 1 ? mx[1] : mx[2]);
      if (lane < 48) red[wk * 96 + wq * 48 + lane] = mine;
    }
    WAIT_LGKM(0);
    BARRIER();
    float* SM = (float*)(ws + OFF_SM);
    float* SL = (float*)(ws + OFF_SL);
    char* pdst = ws + OFF_P + tiled_lane(lane);
    float lsum[3];
#pragma unroll
    for (int j = 0; j < 3; ++j) mx[j] = fmaxf(mx[j], red[(wk ^ 1) * 96 + wq * 48 + j * 16 + (lane & 15)]);
#pragma unroll
    for (int j = 0; j < 3; ++j) {
      float l = 0.f;
#pragma unroll
      for (int i = 0; i < 3; ++i) {
        f32x4 p;
#pragma unroll
        for (int r = 0; r < 4; ++r) p[r] = __builtin_amdgcn_exp2f(acc[i][j][r] - mx[j]);
        l += (p[0] + p[1]) + (p[2] + p[3]);
        *(u32x2*)(pdst + tiled_blk(b * RT + ti * 6 + wq * 3 + j, KK, tj * 6 + wk * 3 + i)) = pack4(p);
      }
      lsum[j] = red4_sum(l);
    }
    {
      const int g = lane >> 4;
      const float ml = g == 0 ? lsum[0] : (g == 1 ? lsum[1] : lsum[2]);
      const float mm = g == 0 ? mx[0] : (g == 1 ? mx[1] : mx[2]);
      if (lane < 48) {
        SL[(b * 22 + tj * 2 + wk) * NK + ti * 96 + wq * 48 + lane] = ml;
        if (wk == 0) SM[(b * 11 + tj) * NK + ti * 96 + wq * 48 + lane] = mm;
      }
    }
  } else {
    const int e = (idx - 30) * 8 + x - 2;
    rsrc_t ra = make_rsrc(ws + OFF_VX, 4 * KD * 1024);
    rsrc_t rb = make_rsrc(e < 6 ? wo_img : wo_txt, D * D * 2);
    f32x4 acc[2][4] = {};
    small_gemm<2, 4, K2_NS, false>(smem, ra, 0, KD * 1024, rb, (e % 6) * 256, D * 2, e, wave8, lane, acc);
    if (wave8 >= 4) return;
    STAMP(ws, 2, 2);
    const int wm = wave >> 1, wn = wave & 1;
    char* dst = ws + (e < 6 ? OFF_VT1 : OFF_VT2) + tiled_lane(lane);
#pragma unroll
    for (int m = 0; m < 2; ++m)
#pragma unroll
      for (int n = 0; n < 4; ++n) *(u32x2*)(dst + tiled_blk(wm * 48 + (e % 6) * 8 + wn * 4 + n, KK, 64 + m)) = pack4(acc[m][n]);
  }
  STAMP(ws, 2, 3);
}

constexpr int K3_NS = 4, K3_STAGE = 36 * 1024;
template <int QT>
__device__ __forceinline__ void k3_body(LDS_AS char* smem, char* __restrict__ ws, int b, int rt0, int nrt, int nb, int txt, int rot,
                                        int wave, int lane, int tid, int qoff, const u16* __restrict__ xres, u16* __restrict__ outp) {
  const int wn = wave & 1;
  const int col0 = nb * 96 + wn * 48 + (lane >> 4) * 4;
  const int srow = min(tid, nrt * 16 - 1);
  const float* SM = (const float*)(ws + OFF_SM) + b * 11 * NK + rt0 * 16 + srow;
  const float* SL = (const float*)(ws + OFF_SL) + b * 22 * NK + rt0 * 16 + srow;
  float m[11], l0[11], l1[11];
#pragma unroll
  for (int t = 0; t < 11; ++t) { m[t] = SM[t * NK]; l0[t] = SL[2 * t * NK]; l1[t] = SL[(2 * t + 1) * NK]; }
  f32x4 gp[3][KSPL];
  u32x2 xv[QT][3];
  {
    const float* mod = (const float*)(ws + OFF_MOD) + (txt * 2 + b) * 2304 + 1536 + col0;
#pragma unroll
    for (int i = 0; i < 3; ++i)
#pragma unroll
      for (int k = 0; k < KSPL; ++k) gp[i][k] = *(const f32x4*)(mod + k * 4 * 2304 + i * 16);
#pragma unroll
    for (int j = 0; j < QT; ++j)
#pragma unroll
      for (int i = 0; i < 3; ++i) xv[j][i] = *(const u32x2*)(xres + (size_t)((qoff + j) * 16 + (lane & 15)) * D + col0 + i * 16);
  }
  SCHED_FENCE();
  {
    float M = m[0];
#pragma unroll
    for (int t = 1; t < 11; ++t) M = fmaxf(M, m[t]);
    float L = 0.f;
#pragma unroll
    for (int t = 0; t < 11; ++t) { m[t] = __builtin_amdgcn_exp2f(m[t] - M); L = fmaf(m[t], l0[t] + l1[t], L); }
    const float inv = 1.0f / L;
    LDS_AS float* fw = (LDS_AS float*)(smem + K3_NS * K3_STAGE);
    if (tid < 80) {
#pragma unroll
      for (int t = 0; t < 11; ++t) fw[wrapn(t + 11 - rot, 11) * 80 + tid] = m[t] * inv;
    }
  }
  f32x4 gate[3];
#pragma unroll
  for (int i = 0; i < 3; ++i) { gate[i] = gp[i][0] + gp[i][1]; TIE(gate[i]); }
  STAMP(ws, 3, 1);
  const unsigned vrd = lds_addr(smem) + wn * 9216 + lane * 16, prd = lds_addr(smem) + 18432 + qoff * 3072 + lane * 16;
  LDS_AS float* fs = (LDS_AS float*)(smem + K3_NS * K3_STAGE) + qoff * 16 + (lane & 15);
  f32x4 acc[3][QT] = {}, t0[3][QT], t1[3][QT] = {};
  Frags<3, QT> f0, f1;
  unsigned bc[QT];
  bc[0] = prd;
  float fp[QT] = {};
  WAIT_LGKM(0);
  BARRIER();
  rd_set<3, QT, false, 3072, 3072, 0, 0>(f0, vrd, bc);
  int so = 0;
  auto stage = [&](int t, Frags<3, QT>& fa, Frags<3, QT>& fb, f32x4 (&cur)[3][QT], f32x4 (&prv)[3][QT]) {
    BARRIER();
    float fn[QT];
#pragma unroll
    for (int j = 0; j < QT; ++j) fn[j] = fs[t * 80 + j * 16];
    kstep<3, QT, false, false, true, 3072, 3072, 1024, 1024>(cur, fa, fb, vrd + so, bc, NoExtra{});
    kstep<3, QT, false, false, false, 3072, 3072, 2048, 2048>(cur, fb, fa, vrd + so, bc, [&](auto ic) {
      constexpr int I = decltype(ic)::value;
      acc[I / QT][I % QT] += prv[I / QT][I % QT] * fp[I % QT];
    });
    so = wrapn(so + K3_STAGE, K3_NS * K3_STAGE);
    bc[0] = prd + so;
    kstep<3, QT, false, false, false, 3072, 3072, 0, 0>(cur, fa, fb, vrd + so, bc, NoExtra{});
#pragma unroll
    for (int j = 0; j < QT; ++j) fp[j] = fn[j];
  };
#pragma unroll 1
  for (int t = 0; t < 10; t += 2) {
    stage(t, f0, f1, t0, t1);
    stage(t + 1, f1, f0, t1, t0);
  }
  stage(10, f0, f1, t0, t1);
  land<3, QT, false>(f1);
  STAMP(ws, 3, 2);
#pragma unroll
  for (int j = 0; j < QT; ++j)
#pragma unroll
    for (int i = 0; i < 3; ++i) {
      const f32x4 o = acc[i][j] + t0[i][j] * fp[j];
      u32x2 r = {pack2(fmaf(gate[i][0], o[0], bflo(xv[j][i][0])), fmaf(gate[i][1], o[1], bfhi(xv[j][i][0]))),
                 pack2(fmaf(gate[i][2], o[2], bflo(xv[j][i][1])), fmaf(gate[i][3], o[3], bfhi(xv[j][i][1])))};
      *(u32x2*)(outp + (size_t)((qoff + j) * 16 + (lane & 15)) * D + col0 + i * 16) = r;
    }
  STAMP(ws, 3, 3);
}

__global__ __launch_bounds__(512) void k3_pv(const u16* __restrict__ x_img, const u16* __restrict__ x_txt, char* __restrict__ ws,
                                             u16* __restrict__ out) {
  __shared__ __attribute__((aligned(1024))) char smem_[K3_NS * K3_STAGE + 11 * 80 * 4];
  LDS_AS char* smem = (LDS_AS char*)smem_;
  const int bid = blockIdx.x, tid = threadIdx.x, lane = tid & 63;
  const int wave8 = __builtin_amdgcn_readfirstlane(tid >> 6), wave = wave8 & 3;
  STAMP(ws, 3, 0);
  const int x = bid & 7, idx = bid >> 3, b = x & 1;
  int rt0, nrt, nb, txt, rot;
  if (idx < 26) {
    const int u = (x >> 1) * 26 + idx, rb = (u % 52) >> 2;
    nb = (u / 52) * 4 + (u & 3); rt0 = rb * 5; nrt = rb == 12 ? 4 : 5; txt = 0;
    rot = (rb * 2 + (u & 3) * 3) % 11;
  } else {
    nb = (x >> 1) * 2 + idx - 26; rt0 = 64; nrt = 2; txt = 1; rot = nb;
  }
  if (wave8 >= 4) {
    rsrc_t rs = make_rsrc(ws, WS_END);
    int vo[3];
#pragma unroll
    for (int i = 0; i < 3; ++i) {
      const int u = wave * 3 + i;
      int o = u < 6 ? (int)(txt ? OFF_VT2 : OFF_VT1) + (b * 48 + nb * 6 + u) * KK * 1024
                    : (int)OFF_P + (b * RT + rt0 + u - 6) * KK * 1024;
      vo[i] = (u - 6 < nrt) ? o + lane * 16 : OOB;
    }
    loader_loop<11, K3_NS, 9>([&](int t, int slot) {
      LDS_AS char* sb = smem + slot * K3_STAGE + wave * 9216;
      const int so = t < 11 ? wrapn(t + rot, 11) * 3072 : OOB;
#pragma unroll
      for (int i = 0; i < 3; ++i) {
        dma<0>(rs, sb + i * 3072, vo[i], so);
        dma<1024>(rs, sb + i * 3072, vo[i], so);
        dma<2048>(rs, sb + i * 3072, vo[i], so);
      }
    });
    return;
  }
  const int wq = wave >> 1;
  const u16* xres = txt ? x_txt + (size_t)b * NTX * D : x_img + (size_t)(b * NI + rt0 * 16) * D;
  u16* outp = txt ? out + (size_t)2 * NI * D + (size_t)b * NTX * D : out + (size_t)(b * NI + rt0 * 16) * D;
  const int q0 = (nrt + 1) >> 1;
  const int qt = wq ? nrt - q0 : q0, qoff = wq ? q0 : 0;
  if (qt == 3) k3_body<3>(smem, ws, b, rt0, nrt, nb, txt, rot, wave, lane, tid, qoff, xres, outp);
  else if (qt == 2) k3_body<2>(smem, ws, b, rt0, nrt, nb, txt, rot, wave, lane, tid, qoff, xres, outp);
  else k3_body<1>(smem, ws, b, rt0, nrt, nb, txt, rot, wave, lane, tid, qoff, xres, outp);
}

extern "C" void kernel_launch(void* const* d_in, const int* in_sizes, int n_in, void* d_out, int out_size, void* d_ws,
                              size_t ws_size, hipStream_t stream) {
  const u16* x_img = (const u16*)d_in[0];
  const u16* x_txt = (const u16*)d_in[1];
  const u16* cvec = (const u16*)d_in[2];
  const u16* wqkv_img = (const u16*)d_in[3];
  const u16* wqkv_txt = (const u16*)d_in[4];
  const u16* wo_img = (const u16*)d_in[5];
  const u16* wo_txt = (const u16*)d_in[6];
  const u16* wmod_img = (const u16*)d_in[7];
  const u16* wmod_txt = (const u16*)d_in[8];
  char* ws = (char*)d_ws;
  k0_prep<<<K0_T1 + K0_T2 + K0_PAD + K0_T3, 512, 0, stream>>>(x_img, x_txt, cvec, wqkv_img, wo_img, wo_txt, wmod_img, wmod_txt, ws);
  k1_proj<<<252, 512, 0, stream>>>(wqkv_img, wqkv_txt, ws);
  k2_scores<<<254, 512, 0, stream>>>(wo_img, wo_txt, ws);
  k3_pv<<<224, 512, 0, stream>>>(x_img, x_txt, ws, (u16*)d_out);
}
```

```cpp
#include <hip/hip_runtime.h>
#include <hip/hip_bf16.h>
#include <stdint.h>
#include <type_traits>

typedef __attribute__((ext_vector_type(8))) __bf16 bf16x8;
typedef __attribute__((ext_vector_type(4))) __bf16 bf16x4;
typedef __attribute__((ext_vector_type(2))) __bf16 bf16x2;
typedef __attribute__((ext_vector_type(4))) float f32x4;
typedef __attribute__((ext_vector_type(2))) float f32x2;
typedef __attribute__((ext_vector_type(4))) unsigned u32x4;
typedef __attribute__((ext_vector_type(2))) unsigned u32x2;
#define LDS_AS __attribute__((address_space(3)))
typedef __amdgpu_buffer_rsrc_t rsrc_t;
typedef unsigned short u16;

constexpr int D = 768, NI = 1024, NTX = 32, NK = 1056;
constexpr int RT = 66;
constexpr int KD = 24;
constexpr int KK = 33;
constexpr int KSPL = 2;
constexpr int OOB = 0x7f000000;

constexpr unsigned SZ_TOK = 2u * RT * KD * 1024u;
constexpr unsigned SZ_VT = 2u * 48u * KK * 1024u;
constexpr unsigned OFF_XN = 0;
constexpr unsigned OFF_Q = OFF_XN + SZ_TOK;
constexpr unsigned OFF_K = OFF_Q + SZ_TOK;
constexpr unsigned OFF_VX = OFF_K + SZ_TOK;
constexpr unsigned OFF_VT1 = OFF_VX + 4u * KD * 1024u;
constexpr unsigned OFF_VT2 = OFF_VT1 + SZ_VT;
constexpr unsigned OFF_P = OFF_VT2 + SZ_VT;
constexpr unsigned OFF_WVO = OFF_P + 2u * RT * KK * 1024u;
constexpr unsigned OFF_MOD = OFF_WVO + 96u * KD * 1024u;
constexpr unsigned OFF_ST = OFF_MOD + KSPL * 4u * 2304u * 4u;
constexpr unsigned OFF_FLAG = OFF_ST + 2u * NK * 24u * 4u;
constexpr unsigned OFF_H = OFF_FLAG + 4096u;
constexpr unsigned WS_END = OFF_H + SZ_TOK;

#define WAIT_VM(n) asm volatile("s_waitcnt vmcnt(%0)" ::"n"(n) : "memory")
#define WAIT_LGKM(n) asm volatile("s_waitcnt lgkmcnt(%0)" ::"n"(n) : "memory")
#define BARRIER() __builtin_amdgcn_s_barrier()
#define TIE(x) asm volatile("" : "+v"(x))
#define SCHED_FENCE() __builtin_amdgcn_sched_barrier(0)

#define STAMP(ws, k, i) do { } while (0)
#define TRACE(ws, k, i) do { } while (0)

template <int I, int N, class F>
__device__ __forceinline__ void static_for(F&& f) {
  if constexpr (I < N) { f(std::integral_constant<int, I>{}); static_for<I + 1, N>(f); }
}
__device__ __forceinline__ rsrc_t make_rsrc(const void* p, unsigned bytes) {
  return __builtin_amdgcn_make_buffer_rsrc((void*)p, 0, (int)bytes, 0x00020000);
}
__device__ __forceinline__ u32x4 make_srd(const void* p, unsigned bytes) {
  unsigned long long a = (unsigned long long)p;
  u32x4 r;
  r[0] = (unsigned)a; r[1] = (unsigned)(a >> 32) & 0xffffu; r[2] = bytes; r[3] = 0x00020000u;
  return r;
}
template <int IMM>
__device__ __forceinline__ void dma(rsrc_t rs, LDS_AS char* lds, int voff, int soff) {
  __builtin_amdgcn_raw_ptr_buffer_load_lds(rs, (LDS_AS void*)lds, 16, voff, soff, IMM, 0);
}
__device__ __forceinline__ void dma_sc1(rsrc_t rs, LDS_AS char* lds, int voff, int soff) {
  __builtin_amdgcn_raw_ptr_buffer_load_lds(rs, (LDS_AS void*)lds, 16, voff, soff, 0, 16);
}
__device__ __forceinline__ u32x4 aload(const u32x4& srd, int voff, int soff) {
  u32x4 r;
  asm volatile("buffer_load_dwordx4 %0, %1, %2, %3 offen" : "=v"(r) : "v"(voff), "s"(srd), "s"(soff));
  return r;
}
template <int OFF>
__device__ __forceinline__ void rd_tr(bf16x4& lo, bf16x4& hi, unsigned addr) {
  asm volatile("ds_read_b64_tr_b16 %0, %2 offset:%c3\n\tds_read_b64_tr_b16 %1, %2 offset:%c4"
               : "=&v"(lo), "=&v"(hi) : "v"(addr), "i"(OFF), "i"(OFF + 512));
}
template <int OFF>
__device__ __forceinline__ void rd_128(bf16x8& r, unsigned addr) {
  asm volatile("ds_read_b128 %0, %1 offset:%c2" : "=v"(r) : "v"(addr), "i"(OFF));
}
template <int OFF>
__device__ __forceinline__ void rd_32(float& r, unsigned addr) {
  asm volatile("ds_read_b32 %0, %1 offset:%c2" : "=v"(r) : "v"(addr), "i"(OFF));
}
__device__ __forceinline__ void tie1(float& x) { TIE(x); }
__device__ __forceinline__ void tie4(f32x4& x) { TIE(x); }
__device__ __forceinline__ void tie8(bf16x8& x) { TIE(x); }
__device__ __forceinline__ void tie4h(bf16x4& x) { TIE(x); }
__device__ __forceinline__ bf16x8 cat(bf16x4 lo, bf16x4 hi) { return __builtin_shufflevector(lo, hi, 0, 1, 2, 3, 4, 5, 6, 7); }
__device__ __forceinline__ unsigned lds_addr(LDS_AS char* p) { return (unsigned)(size_t)p; }
__device__ __forceinline__ int wrapn(int v, int n) { return v >= n ? v - n : v; }
__device__ __forceinline__ f32x4 mfma(bf16x8 a, bf16x8 b, f32x4 c) {
  return __builtin_amdgcn_mfma_f32_16x16x32_bf16(a, b, c, 0, 0, 0);
}
__device__ __forceinline__ unsigned pack2(float a, float b) {
  f32x2 f = {a, b};
  bf16x2 h = __builtin_convertvector(f, bf16x2);
  return __builtin_bit_cast(unsigned, h);
}
__device__ __forceinline__ u32x2 pack4(f32x4 v) { return u32x2{pack2(v[0], v[1]), pack2(v[2], v[3])}; }
__device__ __forceinline__ float bflo(unsigned v) { return __uint_as_float(v << 16); }
__device__ __forceinline__ float bfhi(unsigned v) { return __uint_as_float(v & 0xffff0000u); }
__device__ __forceinline__ float red4_max(float x) {
  auto a = __builtin_amdgcn_permlane16_swap(__float_as_uint(x), __float_as_uint(x), false, false);
  x = fmaxf(__uint_as_float(a[0]), __uint_as_float(a[1]));
  auto b = __builtin_amdgcn_permlane32_swap(__float_as_uint(x), __float_as_uint(x), false, false);
  return fmaxf(__uint_as_float(b[0]), __uint_as_float(b[1]));
}
__device__ __forceinline__ float red4_sum(float x) {
  auto a = __builtin_amdgcn_permlane16_swap(__float_as_uint(x), __float_as_uint(x), false, false);
  x = __uint_as_float(a[0]) + __uint_as_float(a[1]);
  auto b = __builtin_amdgcn_permlane32_swap(__float_as_uint(x), __float_as_uint(x), false, false);
  return __uint_as_float(b[0]) + __uint_as_float(b[1]);
}
__device__ __forceinline__ unsigned tiled_lane(int lane) {
  int g = lane >> 4;
  return (unsigned)(((lane & 15) + 16 * (g >> 1)) * 16 + (g & 1) * 8);
}
__device__ __forceinline__ unsigned tiled_blk(int rowtile, int KS, int T16) {
  return (unsigned)((rowtile * KS + (T16 >> 1)) * 1024 + (T16 & 1) * 512);
}
__device__ __forceinline__ u32x4 pair16(f32x4 x, f32x4 y) {
  const u32x2 px = pack4(x), py = pack4(y);
  auto a = __builtin_amdgcn_permlane16_swap(px[0], py[0], false, false);
  auto b = __builtin_amdgcn_permlane16_swap(px[1], py[1], false, false);
  return u32x4{a[0], b[0], a[1], b[1]};
}
__device__ __forceinline__ unsigned tiled_lane16(int lane) { return (unsigned)(((lane & 15) + 16 * (lane >> 5)) * 16); }
template <int NTILE, class Blk, class Val>
__device__ __forceinline__ void store_tiles(char* __restrict__ base, int lane, Blk&& blk, Val&& val) {
  const bool odd = (lane >> 4) & 1;
  char* p16 = base + tiled_lane16(lane);
#pragma unroll
  for (int i = 0; i + 1 < NTILE; i += 2) *(u32x4*)(p16 + (odd ? blk(i + 1) : blk(i))) = pair16(val(i), val(i + 1));
  if (NTILE & 1) *(u32x2*)(base + tiled_lane(lane) + blk(NTILE - 1)) = pack4(val(NTILE - 1));
}
__device__ __forceinline__ int timg_src(int lane, int sigma, int pitch) {
  int r = lane >> 3, c = (lane & 7) ^ (2 * ((r >> 1) & 1) + 4 * sigma);
  return r * pitch + c * 16;
}
template <int NB>
__device__ __forceinline__ int timg_rd(int lane, int nt) {
  int g = lane >> 4, q = (lane & 15) >> 2, p = lane & 3;
  int f = 2 * ((q >> 1) & 1) + 4 * (g & 1);
  return (g * NB + (nt >> 2)) * 1024 + q * 128 + (((2 * (nt & 3)) ^ f) + (p >> 1)) * 16 + (p & 1) * 8;
}

template <int MT, int NT>
struct Frags { bf16x8 a[MT]; bf16x8 b[NT]; bf16x4 l[NT], h[NT]; };

template <int MT, int NT, bool BTR, int AM, int BM, int AO, int BO, int I>
__device__ __forceinline__ void rd_item(Frags<MT, NT>& n, unsigned ab, const unsigned (&bb)[NT]) {
  if constexpr (I < NT) {
    if constexpr (BTR) rd_tr<BO>(n.l[I], n.h[I], bb[I]);
    else rd_128<BO + I * BM>(n.b[I], bb[0]);
  } else if constexpr (I < NT + MT) {
    rd_128<AO + (I - NT) * AM>(n.a[I - NT], ab);
  }
}
template <int MT, int NT, bool BTR, int AM, int BM, int AO, int BO>
__device__ __forceinline__ void rd_set(Frags<MT, NT>& n, unsigned ab, const unsigned (&bb)[NT]) {
  static_for<0, MT + NT>([&](auto ic) { rd_item<MT, NT, BTR, AM, BM, AO, BO, decltype(ic)::value>(n, ab, bb); });
}
template <int MT, int NT, bool BTR, bool AH = false>
__device__ __forceinline__ void land(Frags<MT, NT>& c) {
  WAIT_LGKM(AH ? MT + (BTR ? 2 : 1) * NT : 0);
#pragma unroll
  for (int j = 0; j < NT; ++j) {
    if constexpr (BTR) { TIE(c.l[j]); TIE(c.h[j]); } else TIE(c.b[j]);
  }
#pragma unroll
  for (int m = 0; m < MT; ++m) TIE(c.a[m]);
  SCHED_FENCE();
}
template <int MT, int NT, bool BTR, bool SWAP, bool ZC, int AM, int BM, int AO, int BO, bool AH = false, class X>
__device__ __forceinline__ void kstep(f32x4 (&acc)[MT][NT], Frags<MT, NT>& c, Frags<MT, NT>& n, unsigned ab,
                                      const unsigned (&bb)[NT], X&& extra) {
  land<MT, NT, BTR, AH>(c);
  static_for<0, MT * NT>([&](auto ic) {
    constexpr int I = decltype(ic)::value, m = I / NT, j = I % NT;
    bf16x8 bf;
    if constexpr (BTR) bf = cat(c.l[j], c.h[j]); else bf = c.b[j];
    const f32x4 ci = ZC ? f32x4{0.f, 0.f, 0.f, 0.f} : acc[m][j];
    acc[m][j] = SWAP ? mfma(bf, c.a[m], ci) : mfma(c.a[m], bf, ci);
    tie4(acc[m][j]);
    rd_item<MT, NT, BTR, AM, BM, AO, BO, I>(n, ab, bb);
    extra(ic);
    SCHED_FENCE();
  });
  static_for<MT * NT, (MT + NT > MT * NT ? MT + NT : MT * NT)>(
      [&](auto ic) { rd_item<MT, NT, BTR, AM, BM, AO, BO, decltype(ic)::value>(n, ab, bb); });
}
struct NoExtra { template <class T> __device__ __forceinline__ void operator()(T) const {} };

struct NoHook { __device__ __forceinline__ void operator()() const {} };
struct NoTail { __device__ __forceinline__ void operator()(int) const {} };
template <int NST, int NS, int V, int TV = 0, class Issue, class Hook = NoHook, class Tail = NoTail>
__device__ __forceinline__ void loader_loop(Issue&& issue, Hook&& hook = NoHook{}, Tail&& tail = NoTail{}) {
#pragma unroll
  for (int s = 0; s < NS - 1; ++s) issue(s, s);
  WAIT_VM(V * (NS - 2));
  BARRIER();
  hook();
  int slot = NS - 1;
#pragma unroll 1
  for (int t = 0; t <= NST - NS; ++t) {
    WAIT_VM(V * (NS - 3));
    BARRIER();
    issue(t + NS - 1, slot);
    slot = wrapn(slot + 1, NS);
  }
  static_for<1, NS>([&](auto rc) {
    constexpr int R = decltype(rc)::value;
    WAIT_VM(R == NS - 1 ? 0 : V * (NS - 2 - R) + (R > 1 ? TV : 0));
    BARRIER();
    if constexpr (R == 1) tail(slot);
  });
}

template <int MW, int NW, int NS, bool A_ROWMAJOR>
__device__ __forceinline__ void small_gemm(LDS_AS char* smem, rsrc_t ra, int a_off, int a_pitch, rsrc_t rb, int b_off,
                                           int b_pitch, int rot, int wave8, int lane, f32x4 (&acc)[MW][NW]) {
  constexpr int NB = (2 * NW) / 4;
  constexpr int ABLK = 4 * MW;
  constexpr int STAGE = (ABLK + 8 * NB) * 1024;
  constexpr int AV = ABLK / 4, BV = 2 * NB;
  const int wave = wave8 & 3;
  if (wave8 >= 4) {
    int av[AV];
#pragma unroll
    for (int i = 0; i < AV; ++i) {
      const int u = wave * AV + i;
      if (A_ROWMAJOR) {
        const int r = u * 8 + (lane >> 3);
        av[i] = a_off + r * a_pitch + (((lane & 7) ^ ((r >> 1) & 7)) * 16);
      } else {
        av[i] = a_off + (u >> 1) * a_pitch + (u & 1) * 1024 + lane * 16;
      }
    }
    int bv[2];
    bv[0] = b_off + (wave * 16) * b_pitch + timg_src(lane, 0, b_pitch);
    bv[1] = b_off + (wave * 16 + 8) * b_pitch + timg_src(lane, 1, b_pitch);
    loader_loop<12, NS, AV + BV>([&](int t, int slot) {
      LDS_AS char* sb = smem + slot * STAGE;
      const int kt = wrapn(t + rot, 12);
      const int soa = kt * (A_ROWMAJOR ? 128 : 2048);
      const int sob = kt * 64 * b_pitch;
#pragma unroll
      for (int i = 0; i < AV; ++i) dma<0>(ra, sb + (wave * AV + i) * 1024, av[i], soa);
#pragma unroll
      for (int h = 0; h < 2; ++h)
#pragma unroll
        for (int nb = 0; nb < NB; ++nb) dma<0>(rb, sb + (ABLK + (2 * wave + h) * NB + nb) * 1024, bv[h] + nb * 128, sob);
    });
    return;
  }
  const int wm = wave >> 1, wn = wave & 1;
  unsigned a0, a1;
  if (A_ROWMAJOR) {
    const int s = (lane & 15) >> 1, g = lane >> 4;
    a0 = lds_addr(smem) + wm * MW * 2048 + (lane & 15) * 128 + ((g ^ s) * 16);
    a1 = lds_addr(smem) + wm * MW * 2048 + (lane & 15) * 128 + (((4 + g) ^ s) * 16);
  } else {
    a0 = lds_addr(smem) + wm * MW * 2048 + lane * 16;
    a1 = a0 + 1024;
  }
  unsigned brd[NW], bc[NW];
#pragma unroll
  for (int n = 0; n < NW; ++n) brd[n] = lds_addr(smem) + ABLK * 1024 + timg_rd<NB>(lane, wn * NW + n);
  Frags<MW, NW> x0, x1, y0, y1;
  BARRIER();
  rd_set<MW, NW, true, 2048, 0, 0, 0>(x0, a0, brd);
  rd_set<MW, NW, true, 2048, 0, 0, 4 * NB * 1024>(x1, a1, brd);
  int so = 0;
  auto stage = [&](Frags<MW, NW>& c0, Frags<MW, NW>& c1, Frags<MW, NW>& n0, Frags<MW, NW>& n1) {
    so = wrapn(so + STAGE, NS * STAGE);
#pragma unroll
    for (int n = 0; n < NW; ++n) bc[n] = brd[n] + so;
    BARRIER();
    kstep<MW, NW, true, false, false, 2048, 0, 0, 0, true>(acc, c0, n0, a0 + so, bc, NoExtra{});
    kstep<MW, NW, true, false, false, 2048, 0, 0, 4 * NB * 1024, true>(acc, c1, n1, a1 + so, bc, NoExtra{});
  };
#pragma unroll 1
  for (int t = 0; t < 12; t += 2) {
    stage(x0, x1, y0, y1);
    stage(y0, y1, x0, x1);
  }
  land<MW, NW, true>(x0);
  land<MW, NW, true>(x1);
}

constexpr int K0_T1 = 72 * KSPL, K0_T2 = 2 * RT, K0_T3 = 192, K0_PAD = 8 - (K0_T1 + K0_T2) % 8;
__global__ __launch_bounds__(512, 4) void k0_prep(const u16* __restrict__ x_img, const u16* __restrict__ x_txt,
                                                  const u16* __restrict__ cvec, const u16* __restrict__ wqkv_img,
                                                  const u16* __restrict__ wo_img, const u16* __restrict__ wo_txt,
                                                  const u16* __restrict__ wmod_img, const u16* __restrict__ wmod_txt,
                                                  char* __restrict__ ws) {
  __shared__ __attribute__((aligned(1024))) char smem_[81920];
  LDS_AS char* smem = (LDS_AS char*)smem_;
  const int bid = blockIdx.x, tid = threadIdx.x, lane = tid & 63;
  const int wave = __builtin_amdgcn_readfirstlane(tid >> 6);
  STAMP(ws, 0, 0);
  if (bid >= K0_T1 + K0_T2 + K0_PAD) {
    const int id = bid - (K0_T1 + K0_T2 + K0_PAD), x = id & 7, idx = id >> 3;
    const int mb = (x & 1) * 4 + (idx & 3), nbk = (x >> 1) * 6 + (idx >> 2);
    rsrc_t ra = make_rsrc(wqkv_img, D * 3 * D * 2);
    rsrc_t rb = make_rsrc(nbk < 12 ? wo_img : wo_txt, D * D * 2);
    f32x4 acc[3][2] = {};
    const int rot = ((idx & 3) * 3 + (idx >> 2) * 2) % 12;
    small_gemm<3, 2, 4, true>(smem, ra, mb * 96 * (3 * D * 2) + 2 * D * 2, 3 * D * 2, rb, (nbk % 12) * 128, D * 2, rot, wave, lane, acc);
    if (wave >= 4) return;
    STAMP(ws, 0, 2);
    const int wm = wave >> 1, wn = wave & 1;
    store_tiles<6>(ws + OFF_WVO, lane, [&](int i) { return tiled_blk(nbk * 4 + wn * 2 + (i & 1), KD, mb * 6 + wm * 3 + (i >> 1)); },
                   [&](int i) { return acc[i >> 1][i & 1]; });
  } else if (wave >= 4 || bid >= K0_T1 + K0_T2) {
    if (bid == K0_T1 + K0_T2 && tid < 256) *(u32x4*)(ws + OFF_FLAG + tid * 16) = u32x4{0u, 0u, 0u, 0u};
    return;
  } else if (bid < K0_T1) {
    constexpr int ROWS = D / KSPL, PER = ROWS / 32;
    const int i = bid, s = i / (36 * KSPL), rem = i % (36 * KSPL), cb = rem % 36, kh = rem / 36;
    const u16* w = (s ? wmod_txt : wmod_img) + (size_t)(kh * ROWS + (tid >> 3)) * (6 * D) + cb * 64 + (tid & 7) * 8;
    u16 cv[3];
#pragma unroll
    for (int e = 0; e < 3; ++e) { const int z = tid + e * 256; cv[e] = cvec[(z / ROWS) * D + kh * ROWS + z % ROWS]; }
    u32x4 wv[PER];
#pragma unroll
    for (int r = 0; r < PER; ++r) wv[r] = *(const u32x4*)(w + (size_t)r * 32 * (6 * D));
    LDS_AS float* sc = (LDS_AS float*)smem;
    LDS_AS float* red = (LDS_AS float*)(smem + 8192);
#pragma unroll
    for (int e = 0; e < 3; ++e) {
      const int z = tid + e * 256;
      const float v = __uint_as_float((unsigned)cv[e] << 16);
      sc[(z % ROWS) * 2 + z / ROWS] = v / (1.0f + __expf(-v));
    }
    __syncthreads();
    float a0[8] = {}, a1[8] = {};
#pragma unroll
    for (int r = 0; r < PER; ++r) {
      f32x2 sv = *(LDS_AS f32x2*)(sc + ((tid >> 3) + r * 32) * 2);
#pragma unroll
      for (int j = 0; j < 4; ++j) {
        float lo = bflo(wv[r][j]), hi = bfhi(wv[r][j]);
        a0[2 * j] = fmaf(sv[0], lo, a0[2 * j]); a0[2 * j + 1] = fmaf(sv[0], hi, a0[2 * j + 1]);
        a1[2 * j] = fmaf(sv[1], lo, a1[2 * j]); a1[2 * j + 1] = fmaf(sv[1], hi, a1[2 * j + 1]);
      }
    }
    LDS_AS float* rp = red + (tid >> 3) * 128 + (tid & 7) * 8;
    *(LDS_AS f32x4*)(rp) = f32x4{a0[0], a0[1], a0[2], a0[3]};
    *(LDS_AS f32x4*)(rp + 4) = f32x4{a0[4], a0[5], a0[6], a0[7]};
    *(LDS_AS f32x4*)(rp + 64) = f32x4{a1[0], a1[1], a1[2], a1[3]};
    *(LDS_AS f32x4*)(rp + 68) = f32x4{a1[4], a1[5], a1[6], a1[7]};
    __syncthreads();
    {
      const int o = tid >> 1, hf = tid & 1;
      float t0 = 0.f, t1 = 0.f;
#pragma unroll
      for (int g = 0; g < 16; g += 2) { t0 += red[(hf * 16 + g) * 128 + o]; t1 += red[(hf * 16 + g + 1) * 128 + o]; }
      float t = t0 + t1;
      t += __shfl_xor(t, 1);
      float* mod = (float*)(ws + OFF_MOD);
      if (hf == 0) mod[((kh * 2 + s) * 2 + (o >> 6)) * 2304 + cb * 64 + (o & 63)] = t;
    }
  } else {
    const int i = bid - K0_T1, b = i / RT, rt = i % RT;
    const u16* src = rt < 64 ? x_img + (size_t)(b * NI + rt * 16) * D : x_txt + (size_t)(b * NTX + (rt - 64) * 16) * D;
    src += (lane & 15) * D + wave * 192 + (lane >> 4) * 8;
    u32x4 v[6];
#pragma unroll
    for (int k = 0; k < 6; ++k) v[k] = *(const u32x4*)(src + k * 32);
    LDS_AS float* red = (LDS_AS float*)smem;
    float s0 = 0.f, s1 = 0.f;
#pragma unroll
    for (int k = 0; k < 6; ++k)
#pragma unroll
      for (int j = 0; j < 4; ++j) { s0 += bflo(v[k][j]); s1 += bfhi(v[k][j]); }
    const float s = red4_sum(s0 + s1);
    if (lane < 16) red[wave * 16 + lane] = s;
    __syncthreads();
    const int r = lane & 15;
    const float mean = ((red[r] + red[16 + r]) + (red[32 + r] + red[48 + r])) * (1.0f / D);
    float q0 = 0.f, q1 = 0.f;
#pragma unroll
    for (int k = 0; k < 6; ++k)
#pragma unroll
      for (int j = 0; j < 4; ++j) {
        float lo = bflo(v[k][j]) - mean, hi = bfhi(v[k][j]) - mean;
        q0 = fmaf(lo, lo, q0); q1 = fmaf(hi, hi, q1);
      }
    const float q = red4_sum(q0 + q1);
    if (lane < 16) red[64 + wave * 16 + lane] = q;
    __syncthreads();
    const float var = ((red[64 + r] + red[80 + r]) + (red[96 + r] + red[112 + r])) * (1.0f / D);
    const float rstd = 1.0f / sqrtf(var + 1e-5f);
    char* dst = ws + OFF_XN + (size_t)(i * KD + wave * 6) * 1024 + lane * 16;
#pragma unroll
    for (int k = 0; k < 6; ++k) {
      u32x4 o;
#pragma unroll
      for (int j = 0; j < 4; ++j) {
        typedef __attribute__((ext_vector_type(2))) _Float16 f16x2;
        const f16x2 h = {(_Float16)((bflo(v[k][j]) - mean) * rstd), (_Float16)((bfhi(v[k][j]) - mean) * rstd)};
        o[j] = __builtin_bit_cast(unsigned, h);
      }
      *(u32x4*)(dst + k * 1024) = o;
    }
  }
  STAMP(ws, 0, 3);
}

constexpr int K1_NS = 7, K1_STAGE = 21 * 1024, K1_GS = K1_NS * K1_STAGE, K1_LDS = K1_GS + 2 * 6144;

__device__ __forceinline__ void fmix_lo(float& d, unsigned v, float g, float s) {
  asm volatile("v_fma_mix_f32 %0, %1, %2, %3 op_sel_hi:[1,0,0]" : "=v"(d) : "v"(v), "v"(g), "v"(s));
}
__device__ __forceinline__ void fmix_hi(float& d, unsigned v, float g, float s) {
  asm volatile("v_fma_mix_f32 %0, %1, %2, %3 op_sel:[1,0,0] op_sel_hi:[1,0,0]" : "=v"(d) : "v"(v), "v"(g), "v"(s));
}
__device__ __forceinline__ void cvt2(unsigned& d, float a, float b) {
  asm volatile("v_cvt_pk_bf16_f32 %0, %1, %2" : "=v"(d) : "v"(a), "v"(b));
}
template <int OFF>
__device__ __forceinline__ void rd_u4(u32x4& r, unsigned addr) {
  asm volatile("ds_read_b128 %0, %1 offset:%c2" : "=v"(r) : "v"(addr), "i"(OFF));
}
template <int OFF>
__device__ __forceinline__ void rd_f4(f32x4& r, unsigned addr) {
  asm volatile("ds_read_b128 %0, %1 offset:%c2" : "=v"(r) : "v"(addr), "i"(OFF));
}
__device__ __forceinline__ void tieu(u32x4& x) { TIE(x); }
template <bool BIG>
constexpr int k1_nextra(int k) { k = (k + 12) % 12; return (k == 7 || k == 8) ? (BIG ? 1 : 0) : (k >= 4 && k <= 10 ? 1 : 0); }
template <bool BIG, int NBR>
constexpr int k1_bwait(int v) { int n = 0; for (int k = v - 5; k < v; ++k) n += k1_nextra<BIG>(k) + NBR; return n; }

template <int MT, bool VT>
__device__ __forceinline__ void k1_body(LDS_AS char* smem, char* __restrict__ ws, const u16* __restrict__ wsrc, int gA, int gB,
                                        int cb, int smod, int wave8, int lane, int tid) {
  constexpr bool BIG = MT == 9;
  constexpr int MH = BIG ? 9 : MT / 2;
  constexpr int WP = 3 * D * 2;
  constexpr int AV = (MT + 2) / 3;
  auto gtile = [&](int lt) { return lt < MH ? gA + lt : gB + (lt - MH); };

  if (wave8 == 7) {
    const u32x4 srd_ws = make_srd(ws, WS_END);
    constexpr int NI4 = BIG ? 6 : 12;
    u32x4 mp[NI4][KSPL];
#pragma unroll
    for (int h = 0; h < NI4; ++h) {
      const int b = BIG ? (gA >= RT ? 1 : 0) : h / 6;
#pragma unroll
      for (int k = 0; k < KSPL; ++k) mp[h][k] = aload(srd_ws, lane * 16, (int)OFF_MOD + (((k * 2 + smod) * 2 + b) * 2304 + (h % 6) * 256) * 4);
    }
    WAIT_VM(0);
#pragma unroll
    for (int h = 0; h < NI4; ++h) {
      TIE(mp[h][0]); TIE(mp[h][1]);
      f32x4 v = __builtin_bit_cast(f32x4, mp[h][0]) + __builtin_bit_cast(f32x4, mp[h][1]);
      if (h % 6 >= 3) v += 1.0f;
      *(LDS_AS f32x4*)(smem + K1_GS + h * 1024 + lane * 16) = v;
    }
    WAIT_LGKM(0);
#pragma unroll 1
    for (int t = 0; t < 25; ++t) BARRIER();
    return;
  }
  if (wave8 >= 4) {
    const int l = wave8 - 4;
    rsrc_t rs = make_rsrc(ws, WS_END);
    rsrc_t rb = VT ? rs : make_rsrc(wsrc, D * 3 * D * 2);
    int bv[2];
    bv[0] = VT ? lane * 16 : cb * 384 + l * 128 + timg_src(lane, 0, WP);
    bv[1] = VT ? lane * 16 : 8 * WP + cb * 384 + l * 128 + timg_src(lane, 1, WP);
    int ab[AV];
#pragma unroll
    for (int i = 0; i < AV; ++i) ab[i] = (l * AV + i < MT) ? (int)OFF_XN + gtile(l * AV + i) * KD * 1024 : OOB;
#pragma unroll
    for (int i = 0; i < AV; ++i) dma<0>(rs, smem + (K1_NS - 1) * K1_STAGE + (l * AV + i) * 1024, lane * 16, ab[i]);
    loader_loop<24, K1_NS, 4 + AV>([&](int t, int slot) {
      LDS_AS char* sb = smem + slot * K1_STAGE;
#pragma unroll
      for (int i = 0; i < 4; ++i) {
        if (VT) dma<0>(rb, sb + 9216 + (l * 4 + i) * 1024, bv[0], (int)OFF_WVO + (((cb - 8) * 12 + l * 4 + i) * KD + t) * 1024);
        else dma<0>(rb, sb + 9216 + (i * 3 + l) * 1024, bv[i & 1], (t * 32 + (i >> 1) * 16) * WP);
      }
#pragma unroll
      for (int i = 0; i < AV; ++i) dma<0>(rs, sb + (l * AV + i) * 1024, lane * 16, ab[i] + (t + 1) * 1024);
    });
    return;
  }

  const int c = wave8;
  constexpr int NH = BIG ? 3 : 1;
  constexpr int NBR = VT ? 1 : 2;
  constexpr int NPOS = BIG ? 27 : 12;
  const int own = BIG ? 2 * c : (MT == 4 ? c : (c & 1));
  const unsigned l0 = lds_addr(smem);
  unsigned bb[4];
#pragma unroll
  for (int r = 0; r < 4; ++r) bb[r] = l0 + 9216 + (VT ? (r ^ c) * 1024 + lane * 16 : timg_rd<3>(lane, r ^ c));
  unsigned xa = l0 + (K1_NS - 1) * K1_STAGE + own * 1024 + lane * 16, x8 = l0 + (K1_NS - 1) * K1_STAGE + 8192 + lane * 16;
  unsigned ga = l0 + K1_GS + (lane >> 4) * 32 + (BIG ? 0 : (own >= MH ? 6144 : 0));
  f32x4 acc[2][12] = {}, acc8[3] = {};
  bf16x8 fb[6]; bf16x4 fl[6], fh[6];
  u32x4 xr[NH], hA[NH], hB[NH];
  f32x4 gs[4];
  float tm[4];

  auto rdB = [&](auto vc) {
    constexpr int V = decltype(vc)::value;
    if constexpr (VT) rd_128<(V >> 2) * 4096>(fb[V % 6], bb[V & 3]); else rd_tr<(V >> 2) * 1024>(fl[V % 6], fh[V % 6], bb[V & 3]);
  };
  auto extra = [&](auto kc) {
    constexpr int K = decltype(kc)::value;
    if constexpr (K == 4) rd_u4<0>(xr[0], xa);
    if constexpr (K == 5) rd_f4<0>(gs[0], ga);
    if constexpr (K == 6) rd_f4<3072>(gs[2], ga);
    if constexpr (K == 7 && BIG) rd_u4<1024>(xr[BIG ? 1 : 0], xa);
    if constexpr (K == 8 && BIG) rd_u4<0>(xr[BIG ? 2 : 0], x8);
    if constexpr (K == 9) rd_f4<16>(gs[1], ga);
    if constexpr (K == 10) rd_f4<3088>(gs[3], ga);
  };
  auto valu = [&](auto wc, u32x4 (&hn)[NH]) {
    constexpr int W = decltype(wc)::value, q = W / 6, o = W % 6;
    constexpr int blk = q % NH, d = (q / NH) * 2 + (o < 4 ? (o >> 1) : (o - 4));
    if constexpr (o < 4) {
      const float g = gs[2 + (d >> 1)][(d & 1) * 2 + (o & 1)], sh = gs[d >> 1][(d & 1) * 2 + (o & 1)];
      if constexpr (o & 1) fmix_hi(tm[o], xr[blk][d], g, sh); else fmix_lo(tm[o], xr[blk][d], g, sh);
    } else {
      unsigned r;
      cvt2(r, tm[(o - 4) * 2], tm[(o - 4) * 2 + 1]);
      hn[blk][d] = r;
    }
  };
  int aslot = K1_NS - 1, bslot = 0, gj = 0;
  auto advA = [&]() {
    const int ds = aslot == K1_NS - 1 ? -(K1_NS - 1) * K1_STAGE : K1_STAGE;
    aslot = aslot == K1_NS - 1 ? 0 : aslot + 1;
    xa += ds; x8 += ds;
    ga += gj < 23 ? 128 : 0;
    gj += 1;
  };
  auto advB = [&]() {
    const int ds = bslot == K1_NS - 1 ? -(K1_NS - 1) * K1_STAGE : K1_STAGE;
    bslot = bslot == K1_NS - 1 ? 0 : bslot + 1;
#pragma unroll
    for (int r = 0; r < 4; ++r) bb[r] += ds;
  };
  auto tie_side = [&]() {
#pragma unroll
    for (int i = 0; i < NH; ++i) tieu(xr[i]);
#pragma unroll
    for (int i = 0; i < 4; ++i) tie4(gs[i]);
  };
  auto step = [&](u32x4 (&hc)[NH], u32x4 (&hn)[NH]) {
    BARRIER();
    static_for<0, NPOS>([&](auto pc) {
      constexpr int P = decltype(pc)::value;
      constexpr int v = BIG ? (P / 9) * 4 + (P % 9 < 3 ? 0 : (P % 9 - 1) / 2) : P;
      constexpr int w = BIG ? (P % 9 < 3 ? P % 9 : (P % 9 - 1) % 2) : 0;
      constexpr bool first = w == 0, last = BIG ? (w == ((v & 3) == 0 ? 2 : 1)) : true;
      if constexpr (first) {
        WAIT_LGKM((k1_bwait<BIG, NBR>(v)));
        if constexpr (VT) tie8(fb[v % 6]); else { tie4h(fl[v % 6]); tie4h(fh[v % 6]); }
        if constexpr (v == 0) { tieu(xr[0]); tie4(gs[0]); tie4(gs[2]); }
        if constexpr (v == 1 && BIG) tieu(xr[BIG ? 1 : 0]);
        if constexpr (v == 2 && BIG) tieu(xr[BIG ? 2 : 0]);
        if constexpr (v == 4) { tie4(gs[1]); tie4(gs[3]); }
      }
      bf16x8 bf;
      if constexpr (VT) bf = fb[v % 6]; else bf = cat(fl[v % 6], fh[v % 6]);
      const bf16x8 af = __builtin_bit_cast(bf16x8, hc[w]);
      f32x4& d = w == 2 ? acc8[v >> 2] : acc[w & 1][v];
      d = VT ? mfma(af, bf, d) : mfma(bf, af, d);
      tie4(d);
      constexpr int W0 = BIG ? 2 * P : (P == 0 ? 0 : (P == 4 ? 6 : 12));
      constexpr int WN = BIG ? 2 : 6;
      static_for<0, WN>([&](auto ic) {
        constexpr int W = W0 + decltype(ic)::value;
        if constexpr (W < 12 * NH) valu(std::integral_constant<int, W>{}, hn);
      });
      if constexpr (last) {
        if constexpr (v == 4) advA();
        extra(std::integral_constant<int, v>{});
        if constexpr (v == 6) advB();
        rdB(std::integral_constant<int, (v + 6) % 12>{});
      }
      SCHED_FENCE();
    });
  };

  BARRIER();
  STAMP(ws, 1, 1);
  static_for<4, 11>(extra);
  WAIT_LGKM(0);
  tie_side();
  static_for<0, 12 * NH>([&](auto wc) { valu(wc, hA); });
  advA();
  static_for<4, 12>([&](auto kc) {
    extra(kc);
    if constexpr (decltype(kc)::value >= 6) rdB(std::integral_constant<int, decltype(kc)::value - 6>{});
  });
#pragma unroll 1
  for (int t = 0; t < 12; ++t) {
    step(hA, hB);
    step(hB, hA);
  }
  WAIT_LGKM(0);
  tie_side();
#pragma unroll
  for (int i = 0; i < 6; ++i) {
    if constexpr (VT) tie8(fb[i]); else { tie4h(fl[i]); tie4h(fh[i]); }
  }
  SCHED_FENCE();
  STAMP(ws, 1, 2);

  constexpr int NTILE = BIG ? 27 : 12;
  const int sec = cb >> 2;
  const int nt0 = (cb & 3) * 12;
  auto val = [&](int i) { return i < 24 ? acc[i / 12][i % 12] : acc8[i - 24]; };
  auto lt_of = [&](int i) { return i < 24 ? own + i / 12 : 8; };
  auto nt_of = [&](int i) { return nt0 + ((i < 24 ? i % 12 : 4 * (i - 24)) ^ c); };
  if (MT == 2 && c >= 2) return;
  if (VT) {
    store_tiles<NTILE>(ws + (sec == 2 ? OFF_VT1 : OFF_VT2), lane, [&](int i) {
      const int gt = gtile(lt_of(i)), b = gt >= RT ? 1 : 0;
      return tiled_blk(b * 48 + nt_of(i), KK, gt - b * RT);
    }, val);
  } else {
    store_tiles<NTILE>(ws + (sec == 0 ? OFF_Q : (sec == 1 ? OFF_K : OFF_VX)), lane, [&](int i) {
      return tiled_blk(sec == 2 ? lt_of(i) : gtile(lt_of(i)), KD, nt_of(i));
    }, val);
  }
  STAMP(ws, 1, 3);
}

template <bool VT>
__device__ __forceinline__ void k1_big(LDS_AS char* smem, char* __restrict__ ws, const u16* __restrict__ wsrc, int gA, int grp,
                                       int cb, int wave8, int lane) {
  constexpr int WP = 3 * D * 2;
  const int k0 = (cb & 3) * 6;
  const int hb = (int)OFF_H + gA * KD * 1024;
  const u32x4 srd_ws = make_srd(ws, WS_END);
  rsrc_t rs = make_rsrc(ws, WS_END);
  if (wave8 == 7) {
    u32x4 mp[2][KSPL];
#pragma unroll
    for (int h = 0; h < 2; ++h) {
      const int item = lane + 64 * h;
      const int e = item < 48 ? k0 * 32 + item * 4 : 768 + k0 * 32 + (item - 48) * 4;
#pragma unroll
      for (int k = 0; k < KSPL; ++k) mp[h][k] = aload(srd_ws, item < 96 ? e * 4 : OOB, (int)OFF_MOD + (k * 4 + (gA >= RT ? 1 : 0)) * 2304 * 4);
    }
    WAIT_VM(0);
#pragma unroll
    for (int h = 0; h < 2; ++h) {
      TIE(mp[h][0]); TIE(mp[h][1]);
      f32x4 v = __builtin_bit_cast(f32x4, mp[h][0]) + __builtin_bit_cast(f32x4, mp[h][1]);
      if (lane + 64 * h >= 48) v += 1.0f;
      *(LDS_AS f32x4*)(smem + K1_GS + h * 1024 + lane * 16) = v;
    }
    WAIT_LGKM(0);
    BARRIER();
    BARRIER();
    BARRIER();
    if (lane == 0) __hip_atomic_store((unsigned*)(ws + OFF_FLAG + grp * 256) + cb, 1u, __ATOMIC_RELAXED, __HIP_MEMORY_SCOPE_AGENT);
#pragma unroll 1
    for (int t = 0; t < 23; ++t) BARRIER();
    return;
  }
  if (wave8 >= 4) {
    const int l = wave8 - 4;
    rsrc_t rb = VT ? rs : make_rsrc(wsrc, D * 3 * D * 2);
    const int bv0 = VT ? lane * 16 : cb * 384 + l * 128 + timg_src(lane, 0, WP);
    const int bv1 = VT ? lane * 16 : 8 * WP + cb * 384 + l * 128 + timg_src(lane, 1, WP);
    auto issueB = [&](int s, int slot) {
      const int kk = wrapn(k0 + s, 24);
      LDS_AS char* sb = smem + slot * K1_STAGE + 9216;
#pragma unroll
      for (int i = 0; i < 4; ++i) {
        if (VT) dma<0>(rb, sb + (l * 4 + i) * 1024, bv0, (int)OFF_WVO + (((cb - 8) * 12 + l * 4 + i) * KD + kk) * 1024);
        else dma<0>(rb, sb + (i * 3 + l) * 1024, (i & 1) ? bv1 : bv0, (kk * 32 + (i >> 1) * 16) * WP);
      }
    };
    auto issueA = [&](int s, int slot) {
      const int kk = wrapn(k0 + s, 24);
#pragma unroll
      for (int i = 0; i < 3; ++i) dma_sc1(rs, smem + slot * K1_STAGE + (l * 3 + i) * 1024, lane * 16, hb + ((l * 3 + i) * KD + kk) * 1024);
    };
    issueB(0, 0); issueB(1, 1);
    BARRIER();
    issueB(2, 2); issueB(3, 3); issueB(4, 4); issueB(5, 5);
    WAIT_VM(20); BARRIER();
    WAIT_VM(16); BARRIER(); issueB(6, 6);
    WAIT_VM(16); BARRIER(); issueB(7, 0);
    WAIT_VM(16); BARRIER(); issueB(8, 1);
    {
      const int fo = (int)OFF_FLAG + grp * 256;
      #pragma unroll 1
      for (int spin = 0; spin < (1 << 20); ++spin) {
        unsigned f;
        asm volatile("buffer_load_dword %0, %1, %2, %3 offen sc1\n\ts_waitcnt vmcnt(0)" : "=v"(f) : "v"((lane & 15) * 4), "s"(srd_ws), "s"(fo) : "memory");
        if (__builtin_amdgcn_ballot_w64(f != 1u) == 0) break;
        __builtin_amdgcn_s_sleep(1);
      }
    }
    issueA(6, 6); issueA(7, 0); issueA(8, 1);
    BARRIER(); issueB(9, 2); issueA(9, 2);
    BARRIER(); issueB(10, 3); issueA(10, 3);
    WAIT_VM(20); BARRIER(); issueB(11, 4); issueA(11, 4);
    WAIT_VM(24); BARRIER(); issueB(12, 5); issueA(12, 5);
    int slot = 6;
#pragma unroll 1
    for (int t = 7; t < 18; ++t) {
      WAIT_VM(28); BARRIER();
      issueB(t + 6, slot); issueA(t + 6, slot);
      slot = wrapn(slot + 1, K1_NS);
    }
    WAIT_VM(28); BARRIER();
    WAIT_VM(21); BARRIER();
    WAIT_VM(14); BARRIER();
    WAIT_VM(7); BARRIER();
    WAIT_VM(0); BARRIER();
    BARRIER();
    return;
  }
  const int c = wave8;
  {
    const int ks2 = 4 + (c >> 1), t2 = (c & 1) * 5;
    u32x4 xr[14];
#pragma unroll
    for (int i = 0; i < 14; ++i) {
      const int tile = i < 9 ? i : min(t2 + i - 9, 8), ks = i < 9 ? c : ks2;
      xr[i] = aload(srd_ws, lane * 16, (int)OFF_XN + ((gA + tile) * KD + k0 + ks) * 1024);
    }
    BARRIER();
    f32x4 gs[2][4];
#pragma unroll
    for (int z = 0; z < 2; ++z) {
      const unsigned gp = lds_addr(smem) + K1_GS + (z ? ks2 : c) * 128 + (lane >> 4) * 32;
      rd_f4<0>(gs[z][0], gp); rd_f4<16>(gs[z][1], gp); rd_f4<768>(gs[z][2], gp); rd_f4<784>(gs[z][3], gp);
    }
    WAIT_VM(0);
    WAIT_LGKM(0);
#pragma unroll
    for (int z = 0; z < 2; ++z)
#pragma unroll
      for (int i = 0; i < 4; ++i) TIE(gs[z][i]);
#pragma unroll
    for (int i = 0; i < 14; ++i) {
      const int tile = i < 9 ? i : min(t2 + i - 9, 8), ks = i < 9 ? c : ks2, z = i < 9 ? 0 : 1;
      TIE(xr[i]);
      float t[8];
#pragma unroll
      for (int d = 0; d < 4; ++d) {
        fmix_lo(t[2 * d], xr[i][d], gs[z][2 + (d >> 1)][(d & 1) * 2], gs[z][d >> 1][(d & 1) * 2]);
        fmix_hi(t[2 * d + 1], xr[i][d], gs[z][2 + (d >> 1)][(d & 1) * 2 + 1], gs[z][d >> 1][(d & 1) * 2 + 1]);
      }
      const u32x4 o = {pack2(t[0], t[1]), pack2(t[2], t[3]), pack2(t[4], t[5]), pack2(t[6], t[7])};
      if (((tile + ks) & 3) == (cb >> 2)) __builtin_amdgcn_raw_buffer_store_b128(o, rs, lane * 16, hb + (tile * KD + k0 + ks) * 1024, 16);
      *(LDS_AS u32x4*)(smem + ks * K1_STAGE + tile * 1024 + lane * 16) = o;
    }
    WAIT_LGKM(0);
  }
  const unsigned ard = lds_addr(smem) + lane * 16;
  unsigned brd[3], bc[3];
#pragma unroll
  for (int n = 0; n < 3; ++n) brd[n] = lds_addr(smem) + 9216 + (VT ? c * 3072 + lane * 16 : timg_rd<3>(lane, c * 3 + n));
  f32x4 acc[9][3] = {};
  Frags<9, 3> f0, f1;
  BARRIER();
  STAMP(ws, 1, 1);
  rd_set<9, 3, !VT, 1024, 1024, 0, 0>(f0, ard, brd);
  WAIT_VM(0);
  int so = 0;
#pragma unroll 1
  for (int t = 0; t < 12; ++t) {
    so = wrapn(so + K1_STAGE, K1_NS * K1_STAGE);
#pragma unroll
    for (int n = 0; n < 3; ++n) bc[n] = brd[n] + so;
    BARRIER();
    TRACE(ws, 1, 2 * t);
    kstep<9, 3, !VT, !VT, false, 1024, 1024, 0, 0>(acc, f0, f1, ard + so, bc, NoExtra{});
    so = wrapn(so + K1_STAGE, K1_NS * K1_STAGE);
#pragma unroll
    for (int n = 0; n < 3; ++n) bc[n] = brd[n] + so;
    BARRIER();
    TRACE(ws, 1, 2 * t + 1);
    kstep<9, 3, !VT, !VT, false, 1024, 1024, 0, 0>(acc, f1, f0, ard + so, bc, NoExtra{});
  }
  land<9, 3, !VT>(f0);
  STAMP(ws, 1, 2);
  const int sec = cb >> 2, nt0 = (cb & 3) * 12 + c * 3;
  if (VT) {
    const int b = gA >= RT ? 1 : 0;
    store_tiles<27>(ws + (sec == 2 ? OFF_VT1 : OFF_VT2), lane, [&](int i) { return tiled_blk(b * 48 + nt0 + i % 3, KK, gA - b * RT + i / 3); },
                    [&](int i) { return acc[i / 3][i % 3]; });
  } else {
    store_tiles<27>(ws + (sec == 0 ? OFF_Q : OFF_K), lane, [&](int i) { return tiled_blk(gA + i / 3, KD, nt0 + i % 3); },
                    [&](int i) { return acc[i / 3][i % 3]; });
  }
  STAMP(ws, 1, 3);
}

__global__ __launch_bounds__(512, 2) void k1_proj(const u16* __restrict__ wqkv_img, const u16* __restrict__ wqkv_txt,
                                                  char* __restrict__ ws) {
  __shared__ __attribute__((aligned(1024))) char smem_[K1_LDS];
  LDS_AS char* smem = (LDS_AS char*)smem_;
  const int bid = blockIdx.x, tid = threadIdx.x, lane = tid & 63;
  const int wave = __builtin_amdgcn_readfirstlane(tid >> 6);
  STAMP(ws, 1, 0);
  if (bid < 224) {
    const int x = bid & 7, idx = bid >> 3;
    const int gA = (x & 1) * RT + (idx % 7) * 9, cb = (x >> 1) * 4 + idx / 7, grp = (x & 1) * 7 + idx % 7;
    if (cb < 8) k1_big<false>(smem, ws, wqkv_img, gA, grp, cb, wave, lane);
    else k1_big<true>(smem, ws, wqkv_img, gA, grp, cb, wave, lane);
  } else if (bid < 240) {
    const int x = bid & 7, cb = (x >> 1) * 4 + (x & 1) * 2 + ((bid - 224) >> 3);
    if (cb < 8) k1_body<2, false>(smem, ws, wqkv_img, 63, RT + 63, cb, 0, wave, lane, tid);
    else k1_body<2, true>(smem, ws, wqkv_img, 63, RT + 63, cb, 0, wave, lane, tid);
  } else {
    k1_body<4, false>(smem, ws, wqkv_txt, 64, RT + 64, bid - 240, 1, wave, lane, tid);
  }
}

constexpr int K2_NS = 4, K2_STAGE = 36 * 1024;
__global__ __launch_bounds__(512) void k2_scores(const u16* __restrict__ wo_img, const u16* __restrict__ wo_txt,
                                                 char* __restrict__ ws) {
  __shared__ __attribute__((aligned(1024))) char smem_[K2_NS * K2_STAGE];
  LDS_AS char* smem = (LDS_AS char*)smem_;
  const int bid = blockIdx.x, tid = threadIdx.x, lane = tid & 63;
  const int wave8 = __builtin_amdgcn_readfirstlane(tid >> 6), wave = wave8 & 3;
  const int x = bid & 7, idx = bid >> 3;
  STAMP(ws, 2, 0);
  if (idx < 30 || (idx == 30 && x < 2)) {
    const int b = x & 1, qd = x >> 1;
    int ti, tj;
    if (idx == 30) { ti = 5; tj = 5; }
    else if (qd == 0) { ti = idx / 6; tj = idx % 6; }
    else if (qd == 1) { ti = idx / 5; tj = 6 + idx % 5; }
    else if (qd == 2) { ti = 6 + idx / 6; tj = 5 + idx % 6; }
    else { ti = 5 + idx / 5; tj = idx % 5; }
    if (wave8 >= 6) {
      rsrc_t rs = make_rsrc(ws, WS_END);
      const int sbase = wave8 == 6 ? (int)OFF_K + (b * RT + tj * 6) * KD * 1024 : (int)OFF_Q + (b * RT + ti * 6) * KD * 1024;
      loader_loop<8, K2_NS, 18>([&](int t, int slot) {
        LDS_AS char* sb = smem + slot * K2_STAGE + (wave8 - 6) * 18432;
#pragma unroll
        for (int i = 0; i < 6; ++i) {
          const int so = sbase + i * KD * 1024 + t * 3072;
          dma<0>(rs, sb + i * 3072, lane * 16, so);
          dma<1024>(rs, sb + i * 3072, lane * 16, so);
          dma<2048>(rs, sb + i * 3072, lane * 16, so);
        }
      });
      return;
    }
    const unsigned krd = lds_addr(smem) + lane * 16, qrd = lds_addr(smem) + 18432 + wave8 * 3072 + lane * 16;
    f32x4 acc[6][1] = {};
    Frags<6, 1> f0, f1, f2;
    unsigned bc[1] = {qrd};
    BARRIER();
    rd_set<6, 1, false, 3072, 0, 0, 0>(f0, krd, bc);
    rd_set<6, 1, false, 3072, 0, 1024, 1024>(f1, krd, bc);
    int so = 0;
#pragma unroll 1
    for (int t = 0; t < 8; ++t) {
      BARRIER();
      TRACE(ws, 2, t);
      kstep<6, 1, false, false, false, 3072, 0, 2048, 2048, true>(acc, f0, f2, krd + so, bc, NoExtra{});
      so = wrapn(so + K2_STAGE, K2_NS * K2_STAGE);
      bc[0] = qrd + so;
      kstep<6, 1, false, false, false, 3072, 0, 0, 0, true>(acc, f1, f0, krd + so, bc, NoExtra{});
      kstep<6, 1, false, false, false, 3072, 0, 1024, 1024, true>(acc, f2, f1, krd + so, bc, NoExtra{});
    }
    land<6, 1, false>(f1);
    land<6, 1, false>(f0);
    STAMP(ws, 2, 2);
    constexpr float SC = 0.036084391824351615f * 1.4426950408889634f;
    float mi[6];
#pragma unroll
    for (int i = 0; i < 6; ++i) mi[i] = fmaxf(fmaxf(acc[i][0][0], acc[i][0][1]), fmaxf(acc[i][0][2], acc[i][0][3]));
    const float mx = red4_max(fmaxf(fmaxf(fmaxf(mi[0], mi[1]), fmaxf(mi[2], mi[3])), fmaxf(mi[4], mi[5]))) * SC;
#pragma unroll
    for (int i = 0; i < 6; ++i) {
#pragma unroll
      for (int r = 0; r < 4; ++r) acc[i][0][r] = __builtin_amdgcn_exp2f(fmaf(acc[i][0][r], SC, -mx));
      mi[i] = (acc[i][0][0] + acc[i][0][1]) + (acc[i][0][2] + acc[i][0][3]);
    }
    store_tiles<6>(ws + OFF_P, lane, [&](int i) { return tiled_blk(b * RT + ti * 6 + wave8, KK, tj * 6 + i); },
                   [&](int i) { return acc[i][0]; });
    const float ls = red4_sum(((mi[0] + mi[1]) + (mi[2] + mi[3])) + (mi[4] + mi[5]));
    if (lane < 32) ((float*)(ws + OFF_ST))[(b * NK + ti * 96 + wave8 * 16 + (lane & 15)) * 24 + (lane >> 4) * 12 + tj] = lane < 16 ? mx : ls;
  } else {
    const int e = (idx - 30) * 8 + x - 2;
    rsrc_t ra = make_rsrc(ws + OFF_VX, 4 * KD * 1024);
    rsrc_t rb = make_rsrc(e < 6 ? wo_img : wo_txt, D * D * 2);
    f32x4 acc[2][4] = {};
    small_gemm<2, 4, 6, false>(smem, ra, 0, KD * 1024, rb, (e % 6) * 256, D * 2, e, wave8, lane, acc);
    if (wave8 >= 4) return;
    STAMP(ws, 2, 2);
    const int wm = wave >> 1, wn = wave & 1;
    store_tiles<8>(ws + (e < 6 ? OFF_VT1 : OFF_VT2), lane, [&](int i) { return tiled_blk(wm * 48 + (e % 6) * 8 + wn * 4 + (i & 3), KK, 64 + (i >> 2)); },
                   [&](int i) { return acc[i >> 2][i & 3]; });
  }
  STAMP(ws, 2, 3);
}

constexpr int K3_NS = 4, K3_STAGE = 33 * 1024;
__global__ __launch_bounds__(512) void k3_pv(const u16* __restrict__ x_img, const u16* __restrict__ x_txt, char* __restrict__ ws,
                                             u16* __restrict__ out) {
  __shared__ __attribute__((aligned(1024))) char smem_[K3_NS * K3_STAGE + 12 * 80 * 4];
  LDS_AS char* smem = (LDS_AS char*)smem_;
  const int bid = blockIdx.x, tid = threadIdx.x, lane = tid & 63;
  const int wave8 = __builtin_amdgcn_readfirstlane(tid >> 6);
  STAMP(ws, 3, 0);
  const int x = bid & 7, idx = bid >> 3, b = x & 1;
  int rt0, nrt, nb, txt;
  if (idx < 26) {
    const int u = (x >> 1) * 26 + idx, rb = (u % 52) >> 2;
    nb = (u / 52) * 4 + (u & 3); rt0 = rb * 5; nrt = rb == 12 ? 4 : 5; txt = 0;
  } else {
    nb = (x >> 1) * 2 + idx - 26; rt0 = 64; nrt = 2; txt = 1;
  }
  if (wave8 >= 5) {
    const int lw = wave8 - 5;
    rsrc_t rs = make_rsrc(ws, WS_END);
    const int vb = (int)(txt ? OFF_VT2 : OFF_VT1) + (b * 48 + nb * 6) * KK * 1024, pb = (int)OFF_P + (b * RT + rt0) * KK * 1024;
    const int vo = lw * 1024 + lane * 16;
    const u32x4 srd_ws = make_srd(ws, WS_END);
    const int ltid = tid - 320;
    u32x4 st[6];
    if (lw < 2) {
      const int vs = (int)OFF_ST + (b * NK + rt0 * 16 + min(ltid, nrt * 16 - 1)) * 96;
#pragma unroll
      for (int i = 0; i < 6; ++i) st[i] = aload(srd_ws, vs, i * 16);
    }
    loader_loop<11, K3_NS, 11, 6>([&](int t, int slot) {
      LDS_AS char* sb = smem + slot * K3_STAGE + lw * 1024;
#pragma unroll
      for (int u = 0; u < 6; ++u) dma<0>(rs, sb + u * 3072, vo, vb + u * KK * 1024 + t * 3072);
#pragma unroll
      for (int u = 0; u < 5; ++u) dma<0>(rs, sb + (6 + u) * 3072, vo, u < nrt ? pb + u * KK * 1024 + t * 3072 : OOB);
    }, [&]() {
      if (lw < 2) {
#pragma unroll
        for (int i = 0; i < 6; ++i) TIE(st[i]);
        float m[11];
#pragma unroll
        for (int t = 0; t < 11; ++t) m[t] = __uint_as_float(st[t >> 2][t & 3]);
        float M = m[0];
#pragma unroll
        for (int t = 1; t < 11; ++t) M = fmaxf(M, m[t]);
        float L = 0.f;
#pragma unroll
        for (int t = 0; t < 11; ++t) {
          m[t] = __builtin_amdgcn_exp2f(m[t] - M);
          L = fmaf(m[t], __uint_as_float(st[3 + (t >> 2)][t & 3]), L);
        }
        const float inv = 1.0f / L;
        LDS_AS float* fw = (LDS_AS float*)(smem + K3_NS * K3_STAGE);
        if (ltid < 80) {
          fw[ltid] = 0.f;
#pragma unroll
          for (int t = 0; t < 11; ++t) fw[(1 + t) * 80 + ltid] = m[t] * inv;
        }
      }
      WAIT_LGKM(0);
    }, [&](int slot) {
      const u16* xres = txt ? x_txt + (size_t)b * NTX * D : x_img + (size_t)(b * NI + rt0 * 16) * D;
      rsrc_t rx = make_rsrc(xres, nrt * 16 * D * 2);
      LDS_AS char* sb = smem + slot * K3_STAGE + lw * 5120;
#pragma unroll
      for (int i = 0; i < 5; ++i) {
        const int c = (lw * 5 + i) * 64 + lane;
        dma<0>(rx, sb + i * 1024, (c / 12) * (D * 2) + nb * 192 + (c % 12) * 16, 0);
      }
      dma<0>(rs, smem + slot * K3_STAGE + 15360 + lw * 1024,
             (lw == 0 && lane < 24 * KSPL) ? (int)OFF_MOD + ((((lane / 24) * 2 + txt) * 2 + b) * 2304 + 1536 + nb * 96 + (lane % 24) * 4) * 4 : OOB, 0);
    });
    return;
  }
  const unsigned vrd = lds_addr(smem) + lane * 16, prd = lds_addr(smem) + 18432 + wave8 * 3072 + lane * 16;
  const unsigned fs = lds_addr(smem) + K3_NS * K3_STAGE + (wave8 * 16 + (lane & 15)) * 4;
  f32x4 acc[6][1] = {}, t0[6][1], t1[6][1] = {};
  Frags<6, 1> f0, f1, f2;
  unsigned bc[1] = {prd};
  BARRIER();
  rd_set<6, 1, false, 3072, 0, 0, 0>(f0, vrd, bc);
  rd_set<6, 1, false, 3072, 0, 1024, 1024>(f1, vrd, bc);
  int so = 0;
  auto stage = [&](int t, f32x4 (&cur)[6][1], f32x4 (&prv)[6][1]) {
    BARRIER();
    TRACE(ws, 3, t);
    float fp;
    rd_32<0>(fp, fs + t * 320);
    kstep<6, 1, false, false, true, 3072, 0, 2048, 2048, true>(cur, f0, f2, vrd + so, bc, NoExtra{});
    so = wrapn(so + K3_STAGE, K3_NS * K3_STAGE);
    bc[0] = prd + so;
    kstep<6, 1, false, false, false, 3072, 0, 0, 0, true>(cur, f1, f0, vrd + so, bc, [&](auto ic) {
      constexpr int I = decltype(ic)::value;
      if constexpr (I == 0) tie1(fp);
      acc[I][0] += prv[I][0] * fp;
      tie4(acc[I][0]);
    });
    kstep<6, 1, false, false, false, 3072, 0, 1024, 1024, true>(cur, f2, f1, vrd + so, bc, NoExtra{});
  };
#pragma unroll 1
  for (int t = 0; t < 10; t += 2) {
    stage(t, t0, t1);
    stage(t + 1, t1, t0);
  }
  stage(10, t0, t1);
  float fp;
  rd_32<0>(fp, fs + 11 * 320);
  land<6, 1, false>(f0);
  land<6, 1, false>(f1);
  TIE(fp);
  STAMP(ws, 3, 2);
  if (wave8 < nrt) {
    LDS_AS char* xs = smem + 3 * K3_STAGE;
    const int cw = (lane >> 4) * 4;
    f32x4 o[6];
#pragma unroll
    for (int i = 0; i < 6; ++i) {
      const f32x4 gate = *(LDS_AS f32x4*)(xs + 15360 + (cw + i * 16) * 4) + *(LDS_AS f32x4*)(xs + 15360 + 384 + (cw + i * 16) * 4);
      const u32x2 xv = *(LDS_AS u32x2*)(xs + (wave8 * 16 + (lane & 15)) * 192 + (cw + i * 16) * 2);
      const f32x4 a = acc[i][0] + t0[i][0] * fp;
      o[i] = f32x4{fmaf(gate[0], a[0], bflo(xv[0])), fmaf(gate[1], a[1], bfhi(xv[0])),
                   fmaf(gate[2], a[2], bflo(xv[1])), fmaf(gate[3], a[3], bfhi(xv[1]))};
    }
    u16* outp = txt ? out + (size_t)2 * NI * D + (size_t)b * NTX * D : out + (size_t)(b * NI + rt0 * 16) * D;
    u16* ob = outp + (size_t)(wave8 * 16 + (lane & 15)) * D + nb * 96 + ((lane >> 4) & 1) * 16 + (lane >> 5) * 8;
#pragma unroll
    for (int i = 0; i < 6; i += 2) *(u32x4*)(ob + i * 16) = pair16(o[i], o[i + 1]);
  }
  STAMP(ws, 3, 3);
}

extern "C" void kernel_launch(void* const* d_in, const int* in_sizes, int n_in, void* d_out, int out_size, void* d_ws,
                              size_t ws_size, hipStream_t stream) {
  const u16* x_img = (const u16*)d_in[0];
  const u16* x_txt = (const u16*)d_in[1];
  const u16* cvec = (const u16*)d_in[2];
  const u16* wqkv_img = (const u16*)d_in[3];
  const u16* wqkv_txt = (const u16*)d_in[4];
  const u16* wo_img = (const u16*)d_in[5];
  const u16* wo_txt = (const u16*)d_in[6];
  const u16* wmod_img = (const u16*)d_in[7];
  const u16* wmod_txt = (const u16*)d_in[8];
  char* ws = (char*)d_ws;
  k0_prep<<<K0_T1 + K0_T2 + K0_PAD + K0_T3, 512, 0, stream>>>(x_img, x_txt, cvec, wqkv_img, wo_img, wo_txt, wmod_img, wmod_txt, ws);
  k1_proj<<<252, 512, 0, stream>>>(wqkv_img, wqkv_txt, ws);
  k2_scores<<<254, 512, 0, stream>>>(wo_img, wo_txt, ws);
  k3_pv<<<224, 512, 0, stream>>>(x_img, x_txt, ws, (u16*)d_out);
}
```

```cpp
#include <hip/hip_runtime.h>
#include <hip/hip_bf16.h>
#include <stdint.h>
#include <type_traits>

typedef __attribute__((ext_vector_type(8))) __bf16 bf16x8;
typedef __attribute__((ext_vector_type(4))) __bf16 bf16x4;
typedef __attribute__((ext_vector_type(2))) __bf16 bf16x2;
typedef __attribute__((ext_vector_type(4))) float f32x4;
typedef __attribute__((ext_vector_type(2))) float f32x2;
typedef __attribute__((ext_vector_type(4))) unsigned u32x4;
typedef __attribute__((ext_vector_type(2))) unsigned u32x2;
#define LDS_AS __attribute__((address_space(3)))
typedef __amdgpu_buffer_rsrc_t rsrc_t;
typedef unsigned short u16;

constexpr int D = 768, NI = 1024, NTX = 32, NK = 1056;
constexpr int RT = 66;
constexpr int KD = 24;
constexpr int KK = 33;
constexpr int KSPL = 2;
constexpr int OOB = 0x7f000000;

constexpr unsigned SZ_TOK = 2u * RT * KD * 1024u;
constexpr unsigned SZ_VT = 2u * 48u * KK * 1024u;
constexpr unsigned OFF_XN = 0;
constexpr unsigned OFF_Q = OFF_XN + SZ_TOK;
constexpr unsigned OFF_K = OFF_Q + SZ_TOK;
constexpr unsigned OFF_VX = OFF_K + SZ_TOK;
constexpr unsigned OFF_VT1 = OFF_VX + 4u * KD * 1024u;
constexpr unsigned OFF_VT2 = OFF_VT1 + SZ_VT;
constexpr unsigned OFF_P = OFF_VT2 + SZ_VT;
constexpr unsigned OFF_WVO = OFF_P + 2u * RT * KK * 1024u;
constexpr unsigned OFF_MOD = OFF_WVO + 96u * KD * 1024u;
constexpr unsigned OFF_ST = OFF_MOD + KSPL * 4u * 2304u * 4u;
constexpr unsigned OFF_FLAG = OFF_ST + 2u * NK * 24u * 4u;
constexpr unsigned OFF_H = OFF_FLAG + 4096u;
constexpr unsigned WS_END = OFF_H + SZ_TOK;

#define WAIT_VM(n) asm volatile("s_waitcnt vmcnt(%0)" ::"n"(n) : "memory")
#define WAIT_LGKM(n) asm volatile("s_waitcnt lgkmcnt(%0)" ::"n"(n) : "memory")
#define BARRIER() __builtin_amdgcn_s_barrier()
#define TIE(x) asm volatile("" : "+v"(x))
#define SCHED_FENCE() __builtin_amdgcn_sched_barrier(0)

#define STAMP(ws, k, i) do { } while (0)
#define TRACE(ws, k, i) do { } while (0)

template <int I, int N, class F>
__device__ __forceinline__ void static_for(F&& f) {
  if constexpr (I < N) { f(std::integral_constant<int, I>{}); static_for<I + 1, N>(f); }
}
__device__ __forceinline__ rsrc_t make_rsrc(const void* p, unsigned bytes) {
  return __builtin_amdgcn_make_buffer_rsrc((void*)p, 0, (int)bytes, 0x00020000);
}
__device__ __forceinline__ u32x4 make_srd(const void* p, unsigned bytes) {
  unsigned long long a = (unsigned long long)p;
  u32x4 r;
  r[0] = (unsigned)a; r[1] = (unsigned)(a >> 32) & 0xffffu; r[2] = bytes; r[3] = 0x00020000u;
  return r;
}
template <int IMM>
__device__ __forceinline__ void dma(rsrc_t rs, LDS_AS char* lds, int voff, int soff) {
  __builtin_amdgcn_raw_ptr_buffer_load_lds(rs, (LDS_AS void*)lds, 16, voff, soff, IMM, 0);
}
__device__ __forceinline__ void dma_sc1(rsrc_t rs, LDS_AS char* lds, int voff, int soff) {
  __builtin_amdgcn_raw_ptr_buffer_load_lds(rs, (LDS_AS void*)lds, 16, voff, soff, 0, 16);
}
__device__ __forceinline__ u32x4 aload(const u32x4& srd, int voff, int soff) {
  u32x4 r;
  asm volatile("buffer_load_dwordx4 %0, %1, %2, %3 offen" : "=v"(r) : "v"(voff), "s"(srd), "s"(soff));
  return r;
}
template <int OFF>
__device__ __forceinline__ void rd_tr(bf16x4& lo, bf16x4& hi, unsigned addr) {
  asm volatile("ds_read_b64_tr_b16 %0, %2 offset:%c3\n\tds_read_b64_tr_b16 %1, %2 offset:%c4"
               : "=&v"(lo), "=&v"(hi) : "v"(addr), "i"(OFF), "i"(OFF + 512));
}
template <int OFF>
__device__ __forceinline__ void rd_128(bf16x8& r, unsigned addr) {
  asm volatile("ds_read_b128 %0, %1 offset:%c2" : "=v"(r) : "v"(addr), "i"(OFF));
}
template <int OFF>
__device__ __forceinline__ void rd_32(float& r, unsigned addr) {
  asm volatile("ds_read_b32 %0, %1 offset:%c2" : "=v"(r) : "v"(addr), "i"(OFF));
}
__device__ __forceinline__ void tie1(float& x) { TIE(x); }
__device__ __forceinline__ void tie4(f32x4& x) { TIE(x); }
__device__ __forceinline__ void tie8(bf16x8& x) { TIE(x); }
__device__ __forceinline__ void tie4h(bf16x4& x) { TIE(x); }
__device__ __forceinline__ bf16x8 cat(bf16x4 lo, bf16x4 hi) { return __builtin_shufflevector(lo, hi, 0, 1, 2, 3, 4, 5, 6, 7); }
__device__ __forceinline__ unsigned lds_addr(LDS_AS char* p) { return (unsigned)(size_t)p; }
__device__ __forceinline__ int wrapn(int v, int n) { return v >= n ? v - n : v; }
__device__ __forceinline__ f32x4 mfma(bf16x8 a, bf16x8 b, f32x4 c) {
  return __builtin_amdgcn_mfma_f32_16x16x32_bf16(a, b, c, 0, 0, 0);
}
__device__ __forceinline__ unsigned pack2(float a, float b) {
  f32x2 f = {a, b};
  bf16x2 h = __builtin_convertvector(f, bf16x2);
  return __builtin_bit_cast(unsigned, h);
}
__device__ __forceinline__ u32x2 pack4(f32x4 v) { return u32x2{pack2(v[0], v[1]), pack2(v[2], v[3])}; }
__device__ __forceinline__ float bflo(unsigned v) { return __uint_as_float(v << 16); }
__device__ __forceinline__ float bfhi(unsigned v) { return __uint_as_float(v & 0xffff0000u); }
__device__ __forceinline__ float red4_max(float x) {
  auto a = __builtin_amdgcn_permlane16_swap(__float_as_uint(x), __float_as_uint(x), false, false);
  x = fmaxf(__uint_as_float(a[0]), __uint_as_float(a[1]));
  auto b = __builtin_amdgcn_permlane32_swap(__float_as_uint(x), __float_as_uint(x), false, false);
  return fmaxf(__uint_as_float(b[0]), __uint_as_float(b[1]));
}
__device__ __forceinline__ float red4_sum(float x) {
  auto a = __builtin_amdgcn_permlane16_swap(__float_as_uint(x), __float_as_uint(x), false, false);
  x = __uint_as_float(a[0]) + __uint_as_float(a[1]);
  auto b = __builtin_amdgcn_permlane32_swap(__float_as_uint(x), __float_as_uint(x), false, false);
  return __uint_as_float(b[0]) + __uint_as_float(b[1]);
}
__device__ __forceinline__ unsigned tiled_lane(int lane) {
  int g = lane >> 4;
  return (unsigned)(((lane & 15) + 16 * (g >> 1)) * 16 + (g & 1) * 8);
}
__device__ __forceinline__ unsigned tiled_blk(int rowtile, int KS, int T16) {
  return (unsigned)((rowtile * KS + (T16 >> 1)) * 1024 + (T16 & 1) * 512);
}
__device__ __forceinline__ u32x4 pair16(f32x4 x, f32x4 y) {
  const u32x2 px = pack4(x), py = pack4(y);
  auto a = __builtin_amdgcn_permlane16_swap(px[0], py[0], false, false);
  auto b = __builtin_amdgcn_permlane16_swap(px[1], py[1], false, false);
  return u32x4{a[0], b[0], a[1], b[1]};
}
__device__ __forceinline__ unsigned tiled_lane16(int lane) { return (unsigned)(((lane & 15) + 16 * (lane >> 5)) * 16); }
template <int NTILE, class Blk, class Val>
__device__ __forceinline__ void store_tiles(char* __restrict__ base, int lane, Blk&& blk, Val&& val) {
  const bool odd = (lane >> 4) & 1;
  char* p16 = base + tiled_lane16(lane);
#pragma unroll
  for (int i = 0; i + 1 < NTILE; i += 2) *(u32x4*)(p16 + (odd ? blk(i + 1) : blk(i))) = pair16(val(i), val(i + 1));
  if (NTILE & 1) *(u32x2*)(base + tiled_lane(lane) + blk(NTILE - 1)) = pack4(val(NTILE - 1));
}
__device__ __forceinline__ int timg_src(int lane, int sigma, int pitch) {
  int r = lane >> 3, c = (lane & 7) ^ (2 * ((r >> 1) & 1) + 4 * sigma);
  return r * pitch + c * 16;
}
template <int NB>
__device__ __forceinline__ int timg_rd(int lane, int nt) {
  int g = lane >> 4, q = (lane & 15) >> 2, p = lane & 3;
  int f = 2 * ((q >> 1) & 1) + 4 * (g & 1);
  return (g * NB + (nt >> 2)) * 1024 + q * 128 + (((2 * (nt & 3)) ^ f) + (p >> 1)) * 16 + (p & 1) * 8;
}

template <int MT, int NT>
struct Frags { bf16x8 a[MT]; bf16x8 b[NT]; bf16x4 l[NT], h[NT]; };

template <int MT, int NT, bool BTR, int AM, int BM, int AO, int BO, int I>
__device__ __forceinline__ void rd_item(Frags<MT, NT>& n, unsigned ab, const unsigned (&bb)[NT]) {
  if constexpr (I < NT) {
    if constexpr (BTR) rd_tr<BO>(n.l[I], n.h[I], bb[I]);
    else rd_128<BO + I * BM>(n.b[I], bb[0]);
  } else if constexpr (I < NT + MT) {
    rd_128<AO + (I - NT) * AM>(n.a[I - NT], ab);
  }
}
template <int MT, int NT, bool BTR, int AM, int BM, int AO, int BO>
__device__ __forceinline__ void rd_set(Frags<MT, NT>& n, unsigned ab, const unsigned (&bb)[NT]) {
  static_for<0, MT + NT>([&](auto ic) { rd_item<MT, NT, BTR, AM, BM, AO, BO, decltype(ic)::value>(n, ab, bb); });
}
template <int MT, int NT, bool BTR, bool AH = false>
__device__ __forceinline__ void land(Frags<MT, NT>& c) {
  WAIT_LGKM(AH ? MT + (BTR ? 2 : 1) * NT : 0);
#pragma unroll
  for (int j = 0; j < NT; ++j) {
    if constexpr (BTR) { TIE(c.l[j]); TIE(c.h[j]); } else TIE(c.b[j]);
  }
#pragma unroll
  for (int m = 0; m < MT; ++m) TIE(c.a[m]);
  SCHED_FENCE();
}
template <int MT, int NT, bool BTR, bool SWAP, bool ZC, int AM, int BM, int AO, int BO, bool AH = false, class X>
__device__ __forceinline__ void kstep(f32x4 (&acc)[MT][NT], Frags<MT, NT>& c, Frags<MT, NT>& n, unsigned ab,
                                      const unsigned (&bb)[NT], X&& extra) {
  land<MT, NT, BTR, AH>(c);
  static_for<0, MT * NT>([&](auto ic) {
    constexpr int I = decltype(ic)::value, m = I / NT, j = I % NT;
    bf16x8 bf;
    if constexpr (BTR) bf = cat(c.l[j], c.h[j]); else bf = c.b[j];
    const f32x4 ci = ZC ? f32x4{0.f, 0.f, 0.f, 0.f} : acc[m][j];
    acc[m][j] = SWAP ? mfma(bf, c.a[m], ci) : mfma(c.a[m], bf, ci);
    tie4(acc[m][j]);
    rd_item<MT, NT, BTR, AM, BM, AO, BO, I>(n, ab, bb);
    extra(ic);
    SCHED_FENCE();
  });
  static_for<MT * NT, (MT + NT > MT * NT ? MT + NT : MT * NT)>(
      [&](auto ic) { rd_item<MT, NT, BTR, AM, BM, AO, BO, decltype(ic)::value>(n, ab, bb); });
}
struct NoExtra { template <class T> __device__ __forceinline__ void operator()(T) const {} };

struct NoHook { __device__ __forceinline__ void operator()() const {} };
struct NoTail { __device__ __forceinline__ void operator()(int) const {} };
template <int NST, int NS, int V, int TV = 0, class Issue, class Hook = NoHook, class Tail = NoTail>
__device__ __forceinline__ void loader_loop(Issue&& issue, Hook&& hook = NoHook{}, Tail&& tail = NoTail{}) {
#pragma unroll
  for (int s = 0; s < NS - 1; ++s) issue(s, s);
  WAIT_VM(V * (NS - 2));
  BARRIER();
  hook();
  int slot = NS - 1;
#pragma unroll 1
  for (int t = 0; t <= NST - NS; ++t) {
    WAIT_VM(V * (NS - 3));
    BARRIER();
    issue(t + NS - 1, slot);
    slot = wrapn(slot + 1, NS);
  }
  static_for<1, NS>([&](auto rc) {
    constexpr int R = decltype(rc)::value;
    WAIT_VM(R == NS - 1 ? 0 : V * (NS - 2 - R) + (R > 1 ? TV : 0));
    BARRIER();
    if constexpr (R == 1) tail(slot);
  });
}

template <int MW, int NW, int NS, bool A_ROWMAJOR>
__device__ __forceinline__ void small_gemm(LDS_AS char* smem, rsrc_t ra, int a_off, int a_pitch, rsrc_t rb, int b_off,
                                           int b_pitch, int rot, int wave8, int lane, f32x4 (&acc)[MW][NW]) {
  constexpr int NB = (2 * NW) / 4;
  constexpr int ABLK = 4 * MW;
  constexpr int STAGE = (ABLK + 8 * NB) * 1024;
  constexpr int AV = ABLK / 4, BV = 2 * NB;
  const int wave = wave8 & 3;
  if (wave8 >= 4) {
    int av[AV];
#pragma unroll
    for (int i = 0; i < AV; ++i) {
      const int u = wave * AV + i;
      if (A_ROWMAJOR) {
        const int r = u * 8 + (lane >> 3);
        av[i] = a_off + r * a_pitch + (((lane & 7) ^ ((r >> 1) & 7)) * 16);
      } else {
        av[i] = a_off + (u >> 1) * a_pitch + (u & 1) * 1024 + lane * 16;
      }
    }
    int bv[2];
    bv[0] = b_off + (wave * 16) * b_pitch + timg_src(lane, 0, b_pitch);
    bv[1] = b_off + (wave * 16 + 8) * b_pitch + timg_src(lane, 1, b_pitch);
    loader_loop<12, NS, AV + BV>([&](int t, int slot) {
      LDS_AS char* sb = smem + slot * STAGE;
      const int kt = wrapn(t + rot, 12);
      const int soa = kt * (A_ROWMAJOR ? 128 : 2048);
      const int sob = kt * 64 * b_pitch;
#pragma unroll
      for (int i = 0; i < AV; ++i) dma<0>(ra, sb + (wave * AV + i) * 1024, av[i], soa);
#pragma unroll
      for (int h = 0; h < 2; ++h)
#pragma unroll
        for (int nb = 0; nb < NB; ++nb) dma<0>(rb, sb + (ABLK + (2 * wave + h) * NB + nb) * 1024, bv[h] + nb * 128, sob);
    });
    return;
  }
  const int wm = wave >> 1, wn = wave & 1;
  unsigned a0, a1;
  if (A_ROWMAJOR) {
    const int s = (lane & 15) >> 1, g = lane >> 4;
    a0 = lds_addr(smem) + wm * MW * 2048 + (lane & 15) * 128 + ((g ^ s) * 16);
    a1 = lds_addr(smem) + wm * MW * 2048 + (lane & 15) * 128 + (((4 + g) ^ s) * 16);
  } else {
    a0 = lds_addr(smem) + wm * MW * 2048 + lane * 16;
    a1 = a0 + 1024;
  }
  unsigned brd[NW], bc[NW];
#pragma unroll
  for (int n = 0; n < NW; ++n) brd[n] = lds_addr(smem) + ABLK * 1024 + timg_rd<NB>(lane, wn * NW + n);
  Frags<MW, NW> x0, x1, y0, y1;
  BARRIER();
  rd_set<MW, NW, true, 2048, 0, 0, 0>(x0, a0, brd);
  rd_set<MW, NW, true, 2048, 0, 0, 4 * NB * 1024>(x1, a1, brd);
  int so = 0;
  auto stage = [&](Frags<MW, NW>& c0, Frags<MW, NW>& c1, Frags<MW, NW>& n0, Frags<MW, NW>& n1) {
    so = wrapn(so + STAGE, NS * STAGE);
#pragma unroll
    for (int n = 0; n < NW; ++n) bc[n] = brd[n] + so;
    BARRIER();
    kstep<MW, NW, true, false, false, 2048, 0, 0, 0, true>(acc, c0, n0, a0 + so, bc, NoExtra{});
    kstep<MW, NW, true, false, false, 2048, 0, 0, 4 * NB * 1024, true>(acc, c1, n1, a1 + so, bc, NoExtra{});
  };
#pragma unroll 1
  for (int t = 0; t < 12; t += 2) {
    stage(x0, x1, y0, y1);
    stage(y0, y1, x0, x1);
  }
  land<MW, NW, true>(x0);
  land<MW, NW, true>(x1);
}

constexpr int K0_T1 = 72 * KSPL, K0_T2 = 2 * RT, K0_T3 = 192, K0_PAD = 8 - (K0_T1 + K0_T2) % 8;
__global__ __launch_bounds__(512, 4) void k0_prep(const u16* __restrict__ x_img, const u16* __restrict__ x_txt,
                                                  const u16* __restrict__ cvec, const u16* __restrict__ wqkv_img,
                                                  const u16* __restrict__ wo_img, const u16* __restrict__ wo_txt,
                                                  const u16* __restrict__ wmod_img, const u16* __restrict__ wmod_txt,
                                                  char* __restrict__ ws) {
  __shared__ __attribute__((aligned(1024))) char smem_[81920];
  LDS_AS char* smem = (LDS_AS char*)smem_;
  const int bid = blockIdx.x, tid = threadIdx.x, lane = tid & 63;
  const int wave = __builtin_amdgcn_readfirstlane(tid >> 6);
  STAMP(ws, 0, 0);
  if (bid >= K0_T1 + K0_T2 + K0_PAD) {
    const int id = bid - (K0_T1 + K0_T2 + K0_PAD), x = id & 7, idx = id >> 3;
    const int mb = (x & 1) * 4 + (idx & 3), nbk = (x >> 1) * 6 + (idx >> 2);
    rsrc_t ra = make_rsrc(wqkv_img, D * 3 * D * 2);
    rsrc_t rb = make_rsrc(nbk < 12 ? wo_img : wo_txt, D * D * 2);
    f32x4 acc[3][2] = {};
    const int rot = ((idx & 3) * 3 + (idx >> 2) * 2) % 12;
    small_gemm<3, 2, 4, true>(smem, ra, mb * 96 * (3 * D * 2) + 2 * D * 2, 3 * D * 2, rb, (nbk % 12) * 128, D * 2, rot, wave, lane, acc);
    if (wave >= 4) return;
    STAMP(ws, 0, 2);
    const int wm = wave >> 1, wn = wave & 1;
    store_tiles<6>(ws + OFF_WVO, lane, [&](int i) { return tiled_blk(nbk * 4 + wn * 2 + (i & 1), KD, mb * 6 + wm * 3 + (i >> 1)); },
                   [&](int i) { return acc[i >> 1][i & 1]; });
  } else if (wave >= 4 || bid >= K0_T1 + K0_T2) {
    if (bid == K0_T1 + K0_T2 && tid < 256) *(u32x4*)(ws + OFF_FLAG + tid * 16) = u32x4{0u, 0u, 0u, 0u};
    return;
  } else if (bid < K0_T1) {
    constexpr int ROWS = D / KSPL, PER = ROWS / 32;
    const int i = bid, s = i / (36 * KSPL), rem = i % (36 * KSPL), cb = rem % 36, kh = rem / 36;
    const u16* w = (s ? wmod_txt : wmod_img) + (size_t)(kh * ROWS + (tid >> 3)) * (6 * D) + cb * 64 + (tid & 7) * 8;
    u16 cv[3];
#pragma unroll
    for (int e = 0; e < 3; ++e) { const int z = tid + e * 256; cv[e] = cvec[(z / ROWS) * D + kh * ROWS + z % ROWS]; }
    u32x4 wv[PER];
#pragma unroll
    for (int r = 0; r < PER; ++r) wv[r] = *(const u32x4*)(w + (size_t)r * 32 * (6 * D));
    LDS_AS float* sc = (LDS_AS float*)smem;
    LDS_AS float* red = (LDS_AS float*)(smem + 8192);
#pragma unroll
    for (int e = 0; e < 3; ++e) {
      const int z = tid + e * 256;
      const float v = __uint_as_float((unsigned)cv[e] << 16);
      sc[(z % ROWS) * 2 + z / ROWS] = v / (1.0f + __expf(-v));
    }
    __syncthreads();
    float a0[8] = {}, a1[8] = {};
#pragma unroll
    for (int r = 0; r < PER; ++r) {
      f32x2 sv = *(LDS_AS f32x2*)(sc + ((tid >> 3) + r * 32) * 2);
#pragma unroll
      for (int j = 0; j < 4; ++j) {
        float lo = bflo(wv[r][j]), hi = bfhi(wv[r][j]);
        a0[2 * j] = fmaf(sv[0], lo, a0[2 * j]); a0[2 * j + 1] = fmaf(sv[0], hi, a0[2 * j + 1]);
        a1[2 * j] = fmaf(sv[1], lo, a1[2 * j]); a1[2 * j + 1] = fmaf(sv[1], hi, a1[2 * j + 1]);
      }
    }
    LDS_AS float* rp = red + (tid >> 3) * 128 + (tid & 7) * 8;
    *(LDS_AS f32x4*)(rp) = f32x4{a0[0], a0[1], a0[2], a0[3]};
    *(LDS_AS f32x4*)(rp + 4) = f32x4{a0[4], a0[5], a0[6], a0[7]};
    *(LDS_AS f32x4*)(rp + 64) = f32x4{a1[0], a1[1], a1[2], a1[3]};
    *(LDS_AS f32x4*)(rp + 68) = f32x4{a1[4], a1[5], a1[6], a1[7]};
    __syncthreads();
    {
      const int o = tid >> 1, hf = tid & 1;
      float t0 = 0.f, t1 = 0.f;
#pragma unroll
      for (int g = 0; g < 16; g += 2) { t0 += red[(hf * 16 + g) * 128 + o]; t1 += red[(hf * 16 + g + 1) * 128 + o]; }
      float t = t0 + t1;
      t += __shfl_xor(t, 1);
      float* mod = (float*)(ws + OFF_MOD);
      if (hf == 0) mod[((kh * 2 + s) * 2 + (o >> 6)) * 2304 + cb * 64 + (o & 63)] = t;
    }
  } else {
    const int i = bid - K0_T1, b = i / RT, rt = i % RT;
    const u16* src = rt < 64 ? x_img + (size_t)(b * NI + rt * 16) * D : x_txt + (size_t)(b * NTX + (rt - 64) * 16) * D;
    src += (lane & 15) * D + wave * 192 + (lane >> 4) * 8;
    u32x4 v[6];
#pragma unroll
    for (int k = 0; k < 6; ++k) v[k] = *(const u32x4*)(src + k * 32);
    LDS_AS float* red = (LDS_AS float*)smem;
    float s0 = 0.f, s1 = 0.f;
#pragma unroll
    for (int k = 0; k < 6; ++k)
#pragma unroll
      for (int j = 0; j < 4; ++j) { s0 += bflo(v[k][j]); s1 += bfhi(v[k][j]); }
    const float s = red4_sum(s0 + s1);
    if (lane < 16) red[wave * 16 + lane] = s;
    __syncthreads();
    const int r = lane & 15;
    const float mean = ((red[r] + red[16 + r]) + (red[32 + r] + red[48 + r])) * (1.0f / D);
    float q0 = 0.f, q1 = 0.f;
#pragma unroll
    for (int k = 0; k < 6; ++k)
#pragma unroll
      for (int j = 0; j < 4; ++j) {
        float lo = bflo(v[k][j]) - mean, hi = bfhi(v[k][j]) - mean;
        q0 = fmaf(lo, lo, q0); q1 = fmaf(hi, hi, q1);
      }
    const float q = red4_sum(q0 + q1);
    if (lane < 16) red[64 + wave * 16 + lane] = q;
    __syncthreads();
    const float var = ((red[64 + r] + red[80 + r]) + (red[96 + r] + red[112 + r])) * (1.0f / D);
    const float rstd = 1.0f / sqrtf(var + 1e-5f);
    char* dst = ws + OFF_XN + (size_t)(i * KD + wave * 6) * 1024 + lane * 16;
#pragma unroll
    for (int k = 0; k < 6; ++k) {
      u32x4 o;
#pragma unroll
      for (int j = 0; j < 4; ++j) {
        typedef __attribute__((ext_vector_type(2))) _Float16 f16x2;
        const f16x2 h = {(_Float16)((bflo(v[k][j]) - mean) * rstd), (_Float16)((bfhi(v[k][j]) - mean) * rstd)};
        o[j] = __builtin_bit_cast(unsigned, h);
      }
      *(u32x4*)(dst + k * 1024) = o;
    }
  }
  STAMP(ws, 0, 3);
}

constexpr int K1_NS = 7, K1_STAGE = 21 * 1024, K1_GS = K1_NS * K1_STAGE, K1_LDS = K1_GS + 2 * 6144;

__device__ __forceinline__ void fmix_lo(float& d, unsigned v, float g, float s) {
  asm volatile("v_fma_mix_f32 %0, %1, %2, %3 op_sel_hi:[1,0,0]" : "=v"(d) : "v"(v), "v"(g), "v"(s));
}
__device__ __forceinline__ void fmix_hi(float& d, unsigned v, float g, float s) {
  asm volatile("v_fma_mix_f32 %0, %1, %2, %3 op_sel:[1,0,0] op_sel_hi:[1,0,0]" : "=v"(d) : "v"(v), "v"(g), "v"(s));
}
__device__ __forceinline__ void cvt2(unsigned& d, float a, float b) {
  asm volatile("v_cvt_pk_bf16_f32 %0, %1, %2" : "=v"(d) : "v"(a), "v"(b));
}
template <int OFF>
__device__ __forceinline__ void rd_u4(u32x4& r, unsigned addr) {
  asm volatile("ds_read_b128 %0, %1 offset:%c2" : "=v"(r) : "v"(addr), "i"(OFF));
}
template <int OFF>
__device__ __forceinline__ void rd_f4(f32x4& r, unsigned addr) {
  asm volatile("ds_read_b128 %0, %1 offset:%c2" : "=v"(r) : "v"(addr), "i"(OFF));
}
__device__ __forceinline__ void tieu(u32x4& x) { TIE(x); }
template <bool BIG>
constexpr int k1_nextra(int k) { k = (k + 12) % 12; return (k == 7 || k == 8) ? (BIG ? 1 : 0) : (k >= 4 && k <= 10 ? 1 : 0); }
template <bool BIG, int NBR>
constexpr int k1_bwait(int v) { int n = 0; for (int k = v - 5; k < v; ++k) n += k1_nextra<BIG>(k) + NBR; return n; }

template <int MT, bool VT>
__device__ __forceinline__ void k1_body(LDS_AS char* smem, char* __restrict__ ws, const u16* __restrict__ wsrc, int gA, int gB,
                                        int cb, int smod, int wave8, int lane, int tid) {
  constexpr bool BIG = MT == 9;
  constexpr int MH = BIG ? 9 : MT / 2;
  constexpr int WP = 3 * D * 2;
  constexpr int AV = (MT + 2) / 3;
  auto gtile = [&](int lt) { return lt < MH ? gA + lt : gB + (lt - MH); };

  if (wave8 == 7) {
    const u32x4 srd_ws = make_srd(ws, WS_END);
    constexpr int NI4 = BIG ? 6 : 12;
    u32x4 mp[NI4][KSPL];
#pragma unroll
    for (int h = 0; h < NI4; ++h) {
      const int b = BIG ? (gA >= RT ? 1 : 0) : h / 6;
#pragma unroll
      for (int k = 0; k < KSPL; ++k) mp[h][k] = aload(srd_ws, lane * 16, (int)OFF_MOD + (((k * 2 + smod) * 2 + b) * 2304 + (h % 6) * 256) * 4);
    }
    WAIT_VM(0);
#pragma unroll
    for (int h = 0; h < NI4; ++h) {
      TIE(mp[h][0]); TIE(mp[h][1]);
      f32x4 v = __builtin_bit_cast(f32x4, mp[h][0]) + __builtin_bit_cast(f32x4, mp[h][1]);
      if (h % 6 >= 3) v += 1.0f;
      *(LDS_AS f32x4*)(smem + K1_GS + h * 1024 + lane * 16) = v;
    }
    WAIT_LGKM(0);
#pragma unroll 1
    for (int t = 0; t < 25; ++t) BARRIER();
    return;
  }
  if (wave8 >= 4) {
    const int l = wave8 - 4;
    rsrc_t rs = make_rsrc(ws, WS_END);
    rsrc_t rb = VT ? rs : make_rsrc(wsrc, D * 3 * D * 2);
    int bv[2];
    bv[0] = VT ? lane * 16 : cb * 384 + l * 128 + timg_src(lane, 0, WP);
    bv[1] = VT ? lane * 16 : 8 * WP + cb * 384 + l * 128 + timg_src(lane, 1, WP);
    int ab[AV];
#pragma unroll
    for (int i = 0; i < AV; ++i) ab[i] = (l * AV + i < MT) ? (int)OFF_XN + gtile(l * AV + i) * KD * 1024 : OOB;
#pragma unroll
    for (int i = 0; i < AV; ++i) dma<0>(rs, smem + (K1_NS - 1) * K1_STAGE + (l * AV + i) * 1024, lane * 16, ab[i]);
    loader_loop<24, K1_NS, 4 + AV>([&](int t, int slot) {
      LDS_AS char* sb = smem + slot * K1_STAGE;
#pragma unroll
      for (int i = 0; i < 4; ++i) {
        if (VT) dma<0>(rb, sb + 9216 + (l * 4 + i) * 1024, bv[0], (int)OFF_WVO + (((cb - 8) * 12 + l * 4 + i) * KD + t) * 1024);
        else dma<0>(rb, sb + 9216 + (i * 3 + l) * 1024, bv[i & 1], (t * 32 + (i >> 1) * 16) * WP);
      }
#pragma unroll
      for (int i = 0; i < AV; ++i) dma<0>(rs, sb + (l * AV + i) * 1024, lane * 16, ab[i] + (t + 1) * 1024);
    });
    return;
  }

  const int c = wave8;
  constexpr int NH = BIG ? 3 : 1;
  constexpr int NBR = VT ? 1 : 2;
  constexpr int NPOS = BIG ? 27 : 12;
  const int own = BIG ? 2 * c : (MT == 4 ? c : (c & 1));
  const unsigned l0 = lds_addr(smem);
  unsigned bb[4];
#pragma unroll
  for (int r = 0; r < 4; ++r) bb[r] = l0 + 9216 + (VT ? (r ^ c) * 1024 + lane * 16 : timg_rd<3>(lane, r ^ c));
  unsigned xa = l0 + (K1_NS - 1) * K1_STAGE + own * 1024 + lane * 16, x8 = l0 + (K1_NS - 1) * K1_STAGE + 8192 + lane * 16;
  unsigned ga = l0 + K1_GS + (lane >> 4) * 32 + (BIG ? 0 : (own >= MH ? 6144 : 0));
  f32x4 acc[2][12] = {}, acc8[3] = {};
  bf16x8 fb[6]; bf16x4 fl[6], fh[6];
  u32x4 xr[NH], hA[NH], hB[NH];
  f32x4 gs[4];
  float tm[4];

  auto rdB = [&](auto vc) {
    constexpr int V = decltype(vc)::value;
    if constexpr (VT) rd_128<(V >> 2) * 4096>(fb[V % 6], bb[V & 3]); else rd_tr<(V >> 2) * 1024>(fl[V % 6], fh[V % 6], bb[V & 3]);
  };
  auto extra = [&](auto kc) {
    constexpr int K = decltype(kc)::value;
    if constexpr (K == 4) rd_u4<0>(xr[0], xa);
    if constexpr (K == 5) rd_f4<0>(gs[0], ga);
    if constexpr (K == 6) rd_f4<3072>(gs[2], ga);
    if constexpr (K == 7 && BIG) rd_u4<1024>(xr[BIG ? 1 : 0], xa);
    if constexpr (K == 8 && BIG) rd_u4<0>(xr[BIG ? 2 : 0], x8);
    if constexpr (K == 9) rd_f4<16>(gs[1], ga);
    if constexpr (K == 10) rd_f4<3088>(gs[3], ga);
  };
  auto valu = [&](auto wc, u32x4 (&hn)[NH]) {
    constexpr int W = decltype(wc)::value, q = W / 6, o = W % 6;
    constexpr int blk = q % NH, d = (q / NH) * 2 + (o < 4 ? (o >> 1) : (o - 4));
    if constexpr (o < 4) {
      const float g = gs[2 + (d >> 1)][(d & 1) * 2 + (o & 1)], sh = gs[d >> 1][(d & 1) * 2 + (o & 1)];
      if constexpr (o & 1) fmix_hi(tm[o], xr[blk][d], g, sh); else fmix_lo(tm[o], xr[blk][d], g, sh);
    } else {
      unsigned r;
      cvt2(r, tm[(o - 4) * 2], tm[(o - 4) * 2 + 1]);
      hn[blk][d] = r;
    }
  };
  int aslot = K1_NS - 1, bslot = 0, gj = 0;
  auto advA = [&]() {
    const int ds = aslot == K1_NS - 1 ? -(K1_NS - 1) * K1_STAGE : K1_STAGE;
    aslot = aslot == K1_NS - 1 ? 0 : aslot + 1;
    xa += ds; x8 += ds;
    ga += gj < 23 ? 128 : 0;
    gj += 1;
  };
  auto advB = [&]() {
    const int ds = bslot == K1_NS - 1 ? -(K1_NS - 1) * K1_STAGE : K1_STAGE;
    bslot = bslot == K1_NS - 1 ? 0 : bslot + 1;
#pragma unroll
    for (int r = 0; r < 4; ++r) bb[r] += ds;
  };
  auto tie_side = [&]() {
#pragma unroll
    for (int i = 0; i < NH; ++i) tieu(xr[i]);
#pragma unroll
    for (int i = 0; i < 4; ++i) tie4(gs[i]);
  };
  auto step = [&](u32x4 (&hc)[NH], u32x4 (&hn)[NH]) {
    BARRIER();
    static_for<0, NPOS>([&](auto pc) {
      constexpr int P = decltype(pc)::value;
      constexpr int v = BIG ? (P / 9) * 4 + (P % 9 < 3 ? 0 : (P % 9 - 1) / 2) : P;
      constexpr int w = BIG ? (P % 9 < 3 ? P % 9 : (P % 9 - 1) % 2) : 0;
      constexpr bool first = w == 0, last = BIG ? (w == ((v & 3) == 0 ? 2 : 1)) : true;
      if constexpr (first) {
        WAIT_LGKM((k1_bwait<BIG, NBR>(v)));
        if constexpr (VT) tie8(fb[v % 6]); else { tie4h(fl[v % 6]); tie4h(fh[v % 6]); }
        if constexpr (v == 0) { tieu(xr[0]); tie4(gs[0]); tie4(gs[2]); }
        if constexpr (v == 1 && BIG) tieu(xr[BIG ? 1 : 0]);
        if constexpr (v == 2 && BIG) tieu(xr[BIG ? 2 : 0]);
        if constexpr (v == 4) { tie4(gs[1]); tie4(gs[3]); }
      }
      bf16x8 bf;
      if constexpr (VT) bf = fb[v % 6]; else bf = cat(fl[v % 6], fh[v % 6]);
      const bf16x8 af = __builtin_bit_cast(bf16x8, hc[w]);
      f32x4& d = w == 2 ? acc8[v >> 2] : acc[w & 1][v];
      d = VT ? mfma(af, bf, d) : mfma(bf, af, d);
      tie4(d);
      constexpr int W0 = BIG ? 2 * P : (P == 0 ? 0 : (P == 4 ? 6 : 12));
      constexpr int WN = BIG ? 2 : 6;
      static_for<0, WN>([&](auto ic) {
        constexpr int W = W0 + decltype(ic)::value;
        if constexpr (W < 12 * NH) valu(std::integral_constant<int, W>{}, hn);
      });
      if constexpr (last) {
        if constexpr (v == 4) advA();
        extra(std::integral_constant<int, v>{});
        if constexpr (v == 6) advB();
        rdB(std::integral_constant<int, (v + 6) % 12>{});
      }
      SCHED_FENCE();
    });
  };

  BARRIER();
  STAMP(ws, 1, 1);
  static_for<4, 11>(extra);
  WAIT_LGKM(0);
  tie_side();
  static_for<0, 12 * NH>([&](auto wc) { valu(wc, hA); });
  advA();
  static_for<4, 12>([&](auto kc) {
    extra(kc);
    if constexpr (decltype(kc)::value >= 6) rdB(std::integral_constant<int, decltype(kc)::value - 6>{});
  });
#pragma unroll 1
  for (int t = 0; t < 12; ++t) {
    step(hA, hB);
    step(hB, hA);
  }
  WAIT_LGKM(0);
  tie_side();
#pragma unroll
  for (int i = 0; i < 6; ++i) {
    if constexpr (VT) tie8(fb[i]); else { tie4h(fl[i]); tie4h(fh[i]); }
  }
  SCHED_FENCE();
  STAMP(ws, 1, 2);

  constexpr int NTILE = BIG ? 27 : 12;
  const int sec = cb >> 2;
  const int nt0 = (cb & 3) * 12;
  auto val = [&](int i) { return i < 24 ? acc[i / 12][i % 12] : acc8[i - 24]; };
  auto lt_of = [&](int i) { return i < 24 ? own + i / 12 : 8; };
  auto nt_of = [&](int i) { return nt0 + ((i < 24 ? i % 12 : 4 * (i - 24)) ^ c); };
  if (MT == 2 && c >= 2) return;
  if (VT) {
    store_tiles<NTILE>(ws + (sec == 2 ? OFF_VT1 : OFF_VT2), lane, [&](int i) {
      const int gt = gtile(lt_of(i)), b = gt >= RT ? 1 : 0;
      return tiled_blk(b * 48 + nt_of(i), KK, gt - b * RT);
    }, val);
  } else {
    store_tiles<NTILE>(ws + (sec == 0 ? OFF_Q : (sec == 1 ? OFF_K : OFF_VX)), lane, [&](int i) {
      return tiled_blk(sec == 2 ? lt_of(i) : gtile(lt_of(i)), KD, nt_of(i));
    }, val);
  }
  STAMP(ws, 1, 3);
}

template <bool VT>
__device__ __forceinline__ void k1_big(LDS_AS char* smem, char* __restrict__ ws, const u16* __restrict__ wsrc, int gA, int grp,
                                       int cb, int wave8, int lane) {
  constexpr int WP = 3 * D * 2;
  const int hb = (int)OFF_H + gA * KD * 1024;
  const u32x4 srd_ws = make_srd(ws, WS_END);
  rsrc_t rs = make_rsrc(ws, WS_END);
  if (wave8 == 7) {
    u32x4 mp[6][KSPL];
#pragma unroll
    for (int h = 0; h < 6; ++h)
#pragma unroll
      for (int k = 0; k < KSPL; ++k) mp[h][k] = aload(srd_ws, lane * 16, (int)OFF_MOD + ((k * 4 + (gA >= RT ? 1 : 0)) * 2304 + h * 256) * 4);
    WAIT_VM(0);
#pragma unroll
    for (int h = 0; h < 6; ++h) {
      TIE(mp[h][0]); TIE(mp[h][1]);
      f32x4 v = __builtin_bit_cast(f32x4, mp[h][0]) + __builtin_bit_cast(f32x4, mp[h][1]);
      if (h >= 3) v += 1.0f;
      *(LDS_AS f32x4*)(smem + K1_GS + h * 1024 + lane * 16) = v;
    }
    WAIT_LGKM(0);
    BARRIER();
    BARRIER();
    if (lane == 0) __hip_atomic_store((unsigned*)(ws + OFF_FLAG + grp * 256) + cb, 1u, __ATOMIC_RELAXED, __HIP_MEMORY_SCOPE_AGENT);
#pragma unroll 1
    for (int t = 0; t < 24; ++t) BARRIER();
    return;
  }
  if (wave8 >= 4) {
    const int l = wave8 - 4;
    rsrc_t rb = VT ? rs : make_rsrc(wsrc, D * 3 * D * 2);
    const int bv0 = VT ? lane * 16 : cb * 384 + l * 128 + timg_src(lane, 0, WP);
    const int bv1 = VT ? lane * 16 : 8 * WP + cb * 384 + l * 128 + timg_src(lane, 1, WP);
    auto issueB = [&](int s, int slot) {
      const int kk = s;
      LDS_AS char* sb = smem + slot * K1_STAGE + 9216;
#pragma unroll
      for (int i = 0; i < 4; ++i) {
        if (VT) dma<0>(rb, sb + (l * 4 + i) * 1024, bv0, (int)OFF_WVO + (((cb - 8) * 12 + l * 4 + i) * KD + kk) * 1024);
        else dma<0>(rb, sb + (i * 3 + l) * 1024, (i & 1) ? bv1 : bv0, (kk * 32 + (i >> 1) * 16) * WP);
      }
    };
    auto issueA = [&](int s, int slot) {
      const int kk = s;
#pragma unroll
      for (int i = 0; i < 3; ++i) dma_sc1(rs, smem + slot * K1_STAGE + (l * 3 + i) * 1024, lane * 16, hb + ((l * 3 + i) * KD + kk) * 1024);
    };
    issueB(0, 0); issueB(1, 1);
    BARRIER();
    issueB(2, 2); issueB(3, 3); issueB(4, 4); issueB(5, 5);
    WAIT_VM(20); BARRIER();
    WAIT_VM(16); BARRIER(); issueB(6, 6);
    WAIT_VM(16); BARRIER(); issueB(7, 0);
    WAIT_VM(16); BARRIER(); issueB(8, 1);
    {
      const int fo = (int)OFF_FLAG + grp * 256;
      #pragma unroll 1
      for (int spin = 0; spin < (1 << 20); ++spin) {
        unsigned f;
        asm volatile("buffer_load_dword %0, %1, %2, %3 offen sc1\n\ts_waitcnt vmcnt(0)" : "=v"(f) : "v"((lane & 15) * 4), "s"(srd_ws), "s"(fo) : "memory");
        if (__builtin_amdgcn_ballot_w64(f != 1u) == 0) break;
        __builtin_amdgcn_s_sleep(1);
      }
    }
    issueA(6, 6); issueA(7, 0); issueA(8, 1);
    BARRIER(); issueB(9, 2); issueA(9, 2);
    BARRIER(); issueB(10, 3); issueA(10, 3);
    WAIT_VM(20); BARRIER(); issueB(11, 4); issueA(11, 4);
    WAIT_VM(24); BARRIER(); issueB(12, 5); issueA(12, 5);
    int slot = 6;
#pragma unroll 1
    for (int t = 7; t < 18; ++t) {
      WAIT_VM(28); BARRIER();
      issueB(t + 6, slot); issueA(t + 6, slot);
      slot = wrapn(slot + 1, K1_NS);
    }
    WAIT_VM(28); BARRIER();
    WAIT_VM(21); BARRIER();
    WAIT_VM(14); BARRIER();
    WAIT_VM(7); BARRIER();
    WAIT_VM(0); BARRIER();
    BARRIER();
    return;
  }
  const int c = wave8;
  {
    const int W = min(cb * 4 + c, 53), ksp = 6 + W / 3, tp = (W % 3) * 3;
    const int ks2 = 4 + (c >> 1), t2 = (c & 1) * 5;
    auto tile_of = [&](int i) { return i < 3 ? tp + i : (i < 12 ? i - 3 : min(t2 + i - 12, 8)); };
    auto ks_of = [&](int i) { return i < 3 ? ksp : (i < 12 ? c : ks2); };
    u32x4 xr[17];
#pragma unroll
    for (int i = 0; i < 17; ++i) xr[i] = aload(srd_ws, lane * 16, (int)OFF_XN + ((gA + tile_of(i)) * KD + ks_of(i)) * 1024);
    BARRIER();
    f32x4 gs[3][4];
#pragma unroll
    for (int z = 0; z < 3; ++z) {
      const unsigned gp = lds_addr(smem) + K1_GS + (z == 0 ? ksp : (z == 1 ? c : ks2)) * 128 + (lane >> 4) * 32;
      rd_f4<0>(gs[z][0], gp); rd_f4<16>(gs[z][1], gp); rd_f4<3072>(gs[z][2], gp); rd_f4<3088>(gs[z][3], gp);
    }
    WAIT_VM(0);
    WAIT_LGKM(0);
#pragma unroll
    for (int z = 0; z < 3; ++z)
#pragma unroll
      for (int i = 0; i < 4; ++i) TIE(gs[z][i]);
#pragma unroll
    for (int i = 0; i < 17; ++i) {
      const int z = i < 3 ? 0 : (i < 12 ? 1 : 2);
      TIE(xr[i]);
      float t[8];
#pragma unroll
      for (int d = 0; d < 4; ++d) {
        fmix_lo(t[2 * d], xr[i][d], gs[z][2 + (d >> 1)][(d & 1) * 2], gs[z][d >> 1][(d & 1) * 2]);
        fmix_hi(t[2 * d + 1], xr[i][d], gs[z][2 + (d >> 1)][(d & 1) * 2 + 1], gs[z][d >> 1][(d & 1) * 2 + 1]);
      }
      const u32x4 o = {pack2(t[0], t[1]), pack2(t[2], t[3]), pack2(t[4], t[5]), pack2(t[6], t[7])};
      if (i < 3) {
        if (cb * 4 + c < 54) __builtin_amdgcn_raw_buffer_store_b128(o, rs, lane * 16, hb + (tile_of(i) * KD + ksp) * 1024, 16);
      } else {
        *(LDS_AS u32x4*)(smem + ks_of(i) * K1_STAGE + tile_of(i) * 1024 + lane * 16) = o;
      }
    }
    WAIT_VM(0);
    WAIT_LGKM(0);
  }
  const unsigned ard = lds_addr(smem) + lane * 16;
  unsigned brd[3], bc[3];
#pragma unroll
  for (int n = 0; n < 3; ++n) brd[n] = lds_addr(smem) + 9216 + (VT ? c * 3072 + lane * 16 : timg_rd<3>(lane, c * 3 + n));
  f32x4 acc[9][3] = {};
  Frags<9, 3> f0, f1;
  BARRIER();
  STAMP(ws, 1, 1);
  rd_set<9, 3, !VT, 1024, 1024, 0, 0>(f0, ard, brd);
  int so = 0;
#pragma unroll 1
  for (int t = 0; t < 12; ++t) {
    so = wrapn(so + K1_STAGE, K1_NS * K1_STAGE);
#pragma unroll
    for (int n = 0; n < 3; ++n) bc[n] = brd[n] + so;
    BARRIER();
    TRACE(ws, 1, 2 * t);
    kstep<9, 3, !VT, !VT, false, 1024, 1024, 0, 0>(acc, f0, f1, ard + so, bc, NoExtra{});
    so = wrapn(so + K1_STAGE, K1_NS * K1_STAGE);
#pragma unroll
    for (int n = 0; n < 3; ++n) bc[n] = brd[n] + so;
    BARRIER();
    TRACE(ws, 1, 2 * t + 1);
    kstep<9, 3, !VT, !VT, false, 1024, 1024, 0, 0>(acc, f1, f0, ard + so, bc, NoExtra{});
  }
  land<9, 3, !VT>(f0);
  STAMP(ws, 1, 2);
  const int sec = cb >> 2, nt0 = (cb & 3) * 12 + c * 3;
  if (VT) {
    const int b = gA >= RT ? 1 : 0;
    store_tiles<27>(ws + (sec == 2 ? OFF_VT1 : OFF_VT2), lane, [&](int i) { return tiled_blk(b * 48 + nt0 + i % 3, KK, gA - b * RT + i / 3); },
                    [&](int i) { return acc[i / 3][i % 3]; });
  } else {
    store_tiles<27>(ws + (sec == 0 ? OFF_Q : OFF_K), lane, [&](int i) { return tiled_blk(gA + i / 3, KD, nt0 + i % 3); },
                    [&](int i) { return acc[i / 3][i % 3]; });
  }
  STAMP(ws, 1, 3);
}

__global__ __launch_bounds__(512, 2) void k1_proj(const u16* __restrict__ wqkv_img, const u16* __restrict__ wqkv_txt,
                                                  char* __restrict__ ws) {
  __shared__ __attribute__((aligned(1024))) char smem_[K1_LDS];
  LDS_AS char* smem = (LDS_AS char*)smem_;
  const int bid = blockIdx.x, tid = threadIdx.x, lane = tid & 63;
  const int wave = __builtin_amdgcn_readfirstlane(tid >> 6);
  STAMP(ws, 1, 0);
  if (bid < 224) {
    const int x = bid & 7, idx = bid >> 3;
    const int gA = (x & 1) * RT + (idx % 7) * 9, cb = (x >> 1) * 4 + idx / 7, grp = (x & 1) * 7 + idx % 7;
    if (cb < 8) k1_big<false>(smem, ws, wqkv_img, gA, grp, cb, wave, lane);
    else k1_big<true>(smem, ws, wqkv_img, gA, grp, cb, wave, lane);
  } else if (bid < 240) {
    const int x = bid & 7, cb = (x >> 1) * 4 + (x & 1) * 2 + ((bid - 224) >> 3);
    if (cb < 8) k1_body<2, false>(smem, ws, wqkv_img, 63, RT + 63, cb, 0, wave, lane, tid);
    else k1_body<2, true>(smem, ws, wqkv_img, 63, RT + 63, cb, 0, wave, lane, tid);
  } else {
    k1_body<4, false>(smem, ws, wqkv_txt, 64, RT + 64, bid - 240, 1, wave, lane, tid);
  }
}

constexpr int K2_NS = 4, K2_STAGE = 36 * 1024;
__global__ __launch_bounds__(512) void k2_scores(const u16* __restrict__ wo_img, const u16* __restrict__ wo_txt,
                                                 char* __restrict__ ws) {
  __shared__ __attribute__((aligned(1024))) char smem_[K2_NS * K2_STAGE];
  LDS_AS char* smem = (LDS_AS char*)smem_;
  const int bid = blockIdx.x, tid = threadIdx.x, lane = tid & 63;
  const int wave8 = __builtin_amdgcn_readfirstlane(tid >> 6), wave = wave8 & 3;
  const int x = bid & 7, idx = bid >> 3;
  STAMP(ws, 2, 0);
  if (idx < 30 || (idx == 30 && x < 2)) {
    const int b = x & 1, qd = x >> 1;
    int ti, tj;
    if (idx == 30) { ti = 5; tj = 5; }
    else if (qd == 0) { ti = idx / 6; tj = idx % 6; }
    else if (qd == 1) { ti = idx / 5; tj = 6 + idx % 5; }
    else if (qd == 2) { ti = 6 + idx / 6; tj = 5 + idx % 6; }
    else { ti = 5 + idx / 5; tj = idx % 5; }
    if (wave8 >= 6) {
      rsrc_t rs = make_rsrc(ws, WS_END);
      const int sbase = wave8 == 6 ? (int)OFF_K + (b * RT + tj * 6) * KD * 1024 : (int)OFF_Q + (b * RT + ti * 6) * KD * 1024;
      loader_loop<8, K2_NS, 18>([&](int t, int slot) {
        LDS_AS char* sb = smem + slot * K2_STAGE + (wave8 - 6) * 18432;
#pragma unroll
        for (int i = 0; i < 6; ++i) {
          const int so = sbase + i * KD * 1024 + t * 3072;
          dma<0>(rs, sb + i * 3072, lane * 16, so);
          dma<1024>(rs, sb + i * 3072, lane * 16, so);
          dma<2048>(rs, sb + i * 3072, lane * 16, so);
        }
      });
      return;
    }
    const unsigned krd = lds_addr(smem) + lane * 16, qrd = lds_addr(smem) + 18432 + wave8 * 3072 + lane * 16;
    f32x4 acc[6][1] = {};
    Frags<6, 1> f0, f1, f2;
    unsigned bc[1] = {qrd};
    BARRIER();
    rd_set<6, 1, false, 3072, 0, 0, 0>(f0, krd, bc);
    rd_set<6, 1, false, 3072, 0, 1024, 1024>(f1, krd, bc);
    int so = 0;
#pragma unroll 1
    for (int t = 0; t < 8; ++t) {
      BARRIER();
      TRACE(ws, 2, t);
      kstep<6, 1, false, false, false, 3072, 0, 2048, 2048, true>(acc, f0, f2, krd + so, bc, NoExtra{});
      so = wrapn(so + K2_STAGE, K2_NS * K2_STAGE);
      bc[0] = qrd + so;
      kstep<6, 1, false, false, false, 3072, 0, 0, 0, true>(acc, f1, f0, krd + so, bc, NoExtra{});
      kstep<6, 1, false, false, false, 3072, 0, 1024, 1024, true>(acc, f2, f1, krd + so, bc, NoExtra{});
    }
    land<6, 1, false>(f1);
    land<6, 1, false>(f0);
    STAMP(ws, 2, 2);
    constexpr float SC = 0.036084391824351615f * 1.4426950408889634f;
    float mi[6];
#pragma unroll
    for (int i = 0; i < 6; ++i) mi[i] = fmaxf(fmaxf(acc[i][0][0], acc[i][0][1]), fmaxf(acc[i][0][2], acc[i][0][3]));
    const float mx = red4_max(fmaxf(fmaxf(fmaxf(mi[0], mi[1]), fmaxf(mi[2], mi[3])), fmaxf(mi[4], mi[5]))) * SC;
#pragma unroll
    for (int i = 0; i < 6; ++i) {
#pragma unroll
      for (int r = 0; r < 4; ++r) acc[i][0][r] = __builtin_amdgcn_exp2f(fmaf(acc[i][0][r], SC, -mx));
      mi[i] = (acc[i][0][0] + acc[i][0][1]) + (acc[i][0][2] + acc[i][0][3]);
    }
    store_tiles<6>(ws + OFF_P, lane, [&](int i) { return tiled_blk(b * RT + ti * 6 + wave8, KK, tj * 6 + i); },
                   [&](int i) { return acc[i][0]; });
    const float ls = red4_sum(((mi[0] + mi[1]) + (mi[2] + mi[3])) + (mi[4] + mi[5]));
    if (lane < 32) ((float*)(ws + OFF_ST))[(b * NK + ti * 96 + wave8 * 16 + (lane & 15)) * 24 + (lane >> 4) * 12 + tj] = lane < 16 ? mx : ls;
  } else {
    const int e = (idx - 30) * 8 + x - 2;
    rsrc_t ra = make_rsrc(ws + OFF_VX, 4 * KD * 1024);
    rsrc_t rb = make_rsrc(e < 6 ? wo_img : wo_txt, D * D * 2);
    f32x4 acc[2][4] = {};
    small_gemm<2, 4, 6, false>(smem, ra, 0, KD * 1024, rb, (e % 6) * 256, D * 2, e, wave8, lane, acc);
    if (wave8 >= 4) return;
    STAMP(ws, 2, 2);
    const int wm = wave >> 1, wn = wave & 1;
    store_tiles<8>(ws + (e < 6 ? OFF_VT1 : OFF_VT2), lane, [&](int i) { return tiled_blk(wm * 48 + (e % 6) * 8 + wn * 4 + (i & 3), KK, 64 + (i >> 2)); },
                   [&](int i) { return acc[i >> 2][i & 3]; });
  }
  STAMP(ws, 2, 3);
}

constexpr int K3_NS = 4, K3_STAGE = 33 * 1024;
__global__ __launch_bounds__(512) void k3_pv(const u16* __restrict__ x_img, const u16* __restrict__ x_txt, char* __restrict__ ws,
                                             u16* __restrict__ out) {
  __shared__ __attribute__((aligned(1024))) char smem_[K3_NS * K3_STAGE + 12 * 80 * 4];
  LDS_AS char* smem = (LDS_AS char*)smem_;
  const int bid = blockIdx.x, tid = threadIdx.x, lane = tid & 63;
  const int wave8 = __builtin_amdgcn_readfirstlane(tid >> 6);
  STAMP(ws, 3, 0);
  const int x = bid & 7, idx = bid >> 3, b = x & 1;
  int rt0, nrt, nb, txt;
  if (idx < 26) {
    const int u = (x >> 1) * 26 + idx, rb = (u % 52) >> 2;
    nb = (u / 52) * 4 + (u & 3); rt0 = rb * 5; nrt = rb == 12 ? 4 : 5; txt = 0;
  } else {
    nb = (x >> 1) * 2 + idx - 26; rt0 = 64; nrt = 2; txt = 1;
  }
  if (wave8 >= 5) {
    const int lw = wave8 - 5;
    rsrc_t rs = make_rsrc(ws, WS_END);
    const int vb = (int)(txt ? OFF_VT2 : OFF_VT1) + (b * 48 + nb * 6) * KK * 1024, pb = (int)OFF_P + (b * RT + rt0) * KK * 1024;
    const int vo = lw * 1024 + lane * 16;
    const u32x4 srd_ws = make_srd(ws, WS_END);
    const int ltid = tid - 320;
    u32x4 st[6];
    if (lw < 2) {
      const int vs = (int)OFF_ST + (b * NK + rt0 * 16 + min(ltid, nrt * 16 - 1)) * 96;
#pragma unroll
      for (int i = 0; i < 6; ++i) st[i] = aload(srd_ws, vs, i * 16);
    }
    loader_loop<11, K3_NS, 11, 6>([&](int t, int slot) {
      LDS_AS char* sb = smem + slot * K3_STAGE + lw * 1024;
#pragma unroll
      for (int u = 0; u < 6; ++u) dma<0>(rs, sb + u * 3072, vo, vb + u * KK * 1024 + t * 3072);
#pragma unroll
      for (int u = 0; u < 5; ++u) dma<0>(rs, sb + (6 + u) * 3072, vo, u < nrt ? pb + u * KK * 1024 + t * 3072 : OOB);
    }, [&]() {
      if (lw < 2) {
#pragma unroll
        for (int i = 0; i < 6; ++i) TIE(st[i]);
        float m[11];
#pragma unroll
        for (int t = 0; t < 11; ++t) m[t] = __uint_as_float(st[t >> 2][t & 3]);
        float M = m[0];
#pragma unroll
        for (int t = 1; t < 11; ++t) M = fmaxf(M, m[t]);
        float L = 0.f;
#pragma unroll
        for (int t = 0; t < 11; ++t) {
          m[t] = __builtin_amdgcn_exp2f(m[t] - M);
          L = fmaf(m[t], __uint_as_float(st[3 + (t >> 2)][t & 3]), L);
        }
        const float inv = 1.0f / L;
        LDS_AS float* fw = (LDS_AS float*)(smem + K3_NS * K3_STAGE);
        if (ltid < 80) {
          fw[ltid] = 0.f;
#pragma unroll
          for (int t = 0; t < 11; ++t) fw[(1 + t) * 80 + ltid] = m[t] * inv;
        }
      }
      WAIT_LGKM(0);
    }, [&](int slot) {
      const u16* xres = txt ? x_txt + (size_t)b * NTX * D : x_img + (size_t)(b * NI + rt0 * 16) * D;
      rsrc_t rx = make_rsrc(xres, nrt * 16 * D * 2);
      LDS_AS char* sb = smem + slot * K3_STAGE + lw * 5120;
#pragma unroll
      for (int i = 0; i < 5; ++i) {
        const int c = (lw * 5 + i) * 64 + lane;
        dma<0>(rx, sb + i * 1024, (c / 12) * (D * 2) + nb * 192 + (c % 12) * 16, 0);
      }
      dma<0>(rs, smem + slot * K3_STAGE + 15360 + lw * 1024,
             (lw == 0 && lane < 24 * KSPL) ? (int)OFF_MOD + ((((lane / 24) * 2 + txt) * 2 + b) * 2304 + 1536 + nb * 96 + (lane % 24) * 4) * 4 : OOB, 0);
    });
    return;
  }
  const unsigned vrd = lds_addr(smem) + lane * 16, prd = lds_addr(smem) + 18432 + wave8 * 3072 + lane * 16;
  const unsigned fs = lds_addr(smem) + K3_NS * K3_STAGE + (wave8 * 16 + (lane & 15)) * 4;
  f32x4 acc[6][1] = {}, t0[6][1], t1[6][1] = {};
  Frags<6, 1> f0, f1, f2;
  unsigned bc[1] = {prd};
  BARRIER();
  rd_set<6, 1, false, 3072, 0, 0, 0>(f0, vrd, bc);
  rd_set<6, 1, false, 3072, 0, 1024, 1024>(f1, vrd, bc);
  int so = 0;
  auto stage = [&](int t, f32x4 (&cur)[6][1], f32x4 (&prv)[6][1]) {
    BARRIER();
    TRACE(ws, 3, t);
    float fp;
    rd_32<0>(fp, fs + t * 320);
    kstep<6, 1, false, false, true, 3072, 0, 2048, 2048, true>(cur, f0, f2, vrd + so, bc, NoExtra{});
    so = wrapn(so + K3_STAGE, K3_NS * K3_STAGE);
    bc[0] = prd + so;
    kstep<6, 1, false, false, false, 3072, 0, 0, 0, true>(cur, f1, f0, vrd + so, bc, [&](auto ic) {
      constexpr int I = decltype(ic)::value;
      if constexpr (I == 0) tie1(fp);
      acc[I][0] += prv[I][0] * fp;
      tie4(acc[I][0]);
    });
    kstep<6, 1, false, false, false, 3072, 0, 1024, 1024, true>(cur, f2, f1, vrd + so, bc, NoExtra{});
  };
#pragma unroll 1
  for (int t = 0; t < 10; t += 2) {
    stage(t, t0, t1);
    stage(t + 1, t1, t0);
  }
  stage(10, t0, t1);
  float fp;
  rd_32<0>(fp, fs + 11 * 320);
  land<6, 1, false>(f0);
  land<6, 1, false>(f1);
  TIE(fp);
  STAMP(ws, 3, 2);
  if (wave8 < nrt) {
    LDS_AS char* xs = smem + 3 * K3_STAGE;
    const int cw = (lane >> 4) * 4;
    f32x4 o[6];
#pragma unroll
    for (int i = 0; i < 6; ++i) {
      const f32x4 gate = *(LDS_AS f32x4*)(xs + 15360 + (cw + i * 16) * 4) + *(LDS_AS f32x4*)(xs + 15360 + 384 + (cw + i * 16) * 4);
      const u32x2 xv = *(LDS_AS u32x2*)(xs + (wave8 * 16 + (lane & 15)) * 192 + (cw + i * 16) * 2);
      const f32x4 a = acc[i][0] + t0[i][0] * fp;
      o[i] = f32x4{fmaf(gate[0], a[0], bflo(xv[0])), fmaf(gate[1], a[1], bfhi(xv[0])),
                   fmaf(gate[2], a[2], bflo(xv[1])), fmaf(gate[3], a[3], bfhi(xv[1]))};
    }
    u16* outp = txt ? out + (size_t)2 * NI * D + (size_t)b * NTX * D : out + (size_t)(b * NI + rt0 * 16) * D;
    u16* ob = outp + (size_t)(wave8 * 16 + (lane & 15)) * D + nb * 96 + ((lane >> 4) & 1) * 16 + (lane >> 5) * 8;
#pragma unroll
    for (int i = 0; i < 6; i += 2) *(u32x4*)(ob + i * 16) = pair16(o[i], o[i + 1]);
  }
  STAMP(ws, 3, 3);
}

extern "C" void kernel_launch(void* const* d_in, const int* in_sizes, int n_in, void* d_out, int out_size, void* d_ws,
                              size_t ws_size, hipStream_t stream) {
  const u16* x_img = (const u16*)d_in[0];
  const u16* x_txt = (const u16*)d_in[1];
  const u16* cvec = (const u16*)d_in[2];
  const u16* wqkv_img = (const u16*)d_in[3];
  const u16* wqkv_txt = (const u16*)d_in[4];
  const u16* wo_img = (const u16*)d_in[5];
  const u16* wo_txt = (const u16*)d_in[6];
  const u16* wmod_img = (const u16*)d_in[7];
  const u16* wmod_txt = (const u16*)d_in[8];
  char* ws = (char*)d_ws;
  k0_prep<<<K0_T1 + K0_T2 + K0_PAD + K0_T3, 512, 0, stream>>>(x_img, x_txt, cvec, wqkv_img, wo_img, wo_txt, wmod_img, wmod_txt, ws);
  k1_proj<<<252, 512, 0, stream>>>(wqkv_img, wqkv_txt, ws);
  k2_scores<<<254, 512, 0, stream>>>(wo_img, wo_txt, ws);
  k3_pv<<<224, 512, 0, stream>>>(x_img, x_txt, ws, (u16*)d_out);
}
```

```cpp
#include <hip/hip_runtime.h>
#include <hip/hip_bf16.h>
#include <stdint.h>
#include <type_traits>

typedef __attribute__((ext_vector_type(8))) __bf16 bf16x8;
typedef __attribute__((ext_vector_type(4))) __bf16 bf16x4;
typedef __attribute__((ext_vector_type(2))) __bf16 bf16x2;
typedef __attribute__((ext_vector_type(4))) float f32x4;
typedef __attribute__((ext_vector_type(2))) float f32x2;
typedef __attribute__((ext_vector_type(4))) unsigned u32x4;
typedef __attribute__((ext_vector_type(2))) unsigned u32x2;
#define LDS_AS __attribute__((address_space(3)))
typedef __amdgpu_buffer_rsrc_t rsrc_t;
typedef unsigned short u16;

constexpr int D = 768, NI = 1024, NTX = 32, NK = 1056;
constexpr int RT = 66;
constexpr int KD = 24;
constexpr int KK = 33;
constexpr int KSPL = 2;
constexpr int OOB = 0x7f000000;

constexpr unsigned SZ_TOK = 2u * RT * KD * 1024u;
constexpr unsigned SZ_VT = 2u * 48u * KK * 1024u;
constexpr unsigned OFF_XN = 0;
constexpr unsigned OFF_Q = OFF_XN + SZ_TOK;
constexpr unsigned OFF_K = OFF_Q + SZ_TOK;
constexpr unsigned OFF_VX = OFF_K + SZ_TOK;
constexpr unsigned OFF_VT1 = OFF_VX + 4u * KD * 1024u;
constexpr unsigned OFF_VT2 = OFF_VT1 + SZ_VT;
constexpr unsigned OFF_P = OFF_VT2 + SZ_VT;
constexpr unsigned OFF_WVO = OFF_P + 2u * RT * KK * 1024u;
constexpr unsigned OFF_MOD = OFF_WVO + 96u * KD * 1024u;
constexpr unsigned OFF_ST = OFF_MOD + KSPL * 4u * 2304u * 4u;
constexpr unsigned OFF_CNT = OFF_ST + 2u * RT * 11u * 256u;
constexpr unsigned OFF_H = OFF_CNT + 20480u;
constexpr unsigned WS_END = OFF_H + SZ_TOK;
constexpr int C_H = 0;
constexpr int C_QK = 14;
constexpr int C_T = 46;
constexpr int C_V = 49;
constexpr int C_X = 81;
constexpr int C_P = 93;
constexpr int C_W = 115;
constexpr unsigned SZ_CNT = 20480u;

#define WAIT_VM(n) asm volatile("s_waitcnt vmcnt(%0)" ::"n"(n) : "memory")
#define WAIT_LGKM(n) asm volatile("s_waitcnt lgkmcnt(%0)" ::"n"(n) : "memory")
#define BARRIER() __builtin_amdgcn_s_barrier()
#define TIE(x) asm volatile("" : "+v"(x))
#define SCHED_FENCE() __builtin_amdgcn_sched_barrier(0)

#define STAMP(ws, k, i) do { } while (0)
#define TRACE(ws, k, i) do { } while (0)

template <int I, int N, class F>
__device__ __forceinline__ void static_for(F&& f) {
  if constexpr (I < N) { f(std::integral_constant<int, I>{}); static_for<I + 1, N>(f); }
}
__device__ __forceinline__ rsrc_t make_rsrc(const void* p, unsigned bytes) {
  return __builtin_amdgcn_make_buffer_rsrc((void*)p, 0, (int)bytes, 0x00020000);
}
__device__ __forceinline__ u32x4 make_srd(const void* p, unsigned bytes) {
  unsigned long long a = (unsigned long long)p;
  u32x4 r;
  r[0] = (unsigned)a; r[1] = (unsigned)(a >> 32) & 0xffffu; r[2] = bytes; r[3] = 0x00020000u;
  return r;
}
template <int IMM>
__device__ __forceinline__ void dma(rsrc_t rs, LDS_AS char* lds, int voff, int soff) {
  __builtin_amdgcn_raw_ptr_buffer_load_lds(rs, (LDS_AS void*)lds, 16, voff, soff, IMM, 0);
}
template <int IMM = 0>
__device__ __forceinline__ void dma_sc1(rsrc_t rs, LDS_AS char* lds, int voff, int soff) {
  __builtin_amdgcn_raw_ptr_buffer_load_lds(rs, (LDS_AS void*)lds, 16, voff, soff, IMM, 16);
}
__device__ __forceinline__ u32x4 aload(const u32x4& srd, int voff, int soff) {
  u32x4 r;
  asm volatile("buffer_load_dwordx4 %0, %1, %2, %3 offen" : "=v"(r) : "v"(voff), "s"(srd), "s"(soff));
  return r;
}
__device__ __forceinline__ u32x4 aload_sc1(const u32x4& srd, int voff, int soff) {
  u32x4 r;
  asm volatile("buffer_load_dwordx4 %0, %1, %2, %3 offen sc1" : "=v"(r) : "v"(voff), "s"(srd), "s"(soff));
  return r;
}
__device__ __forceinline__ void signal(char* ws, int idx) {
  __hip_atomic_fetch_add((unsigned*)(ws + OFF_CNT + idx * 128), 1u, __ATOMIC_RELAXED, __HIP_MEMORY_SCOPE_AGENT);
}
__device__ __forceinline__ bool last_wave(LDS_AS unsigned* word, unsigned n, int lane) {
  WAIT_VM(0);
  unsigned old = 0;
  if (lane == 0) old = __hip_atomic_fetch_add(word, 1u, __ATOMIC_RELAXED, __HIP_MEMORY_SCOPE_WORKGROUP);
  return lane == 0 && old == n - 1;
}
struct Pending { int i0, i1; LDS_AS unsigned* arr; };
__device__ __forceinline__ void flush(char* ws, const Pending& p, int lane) {
  if (p.i0 >= 0 && last_wave(p.arr, 4, lane)) { signal(ws, p.i0); if (p.i1 >= 0) signal(ws, p.i1); }
}
__device__ __forceinline__ void poll(const u32x4& srd, int idx, unsigned target) {
#pragma unroll 1
  for (int spin = 0; spin < (1 << 20); ++spin) {
    unsigned f;
    asm volatile("buffer_load_dword %0, %1, %2, 0 offen sc1\n\ts_waitcnt vmcnt(0)" : "=v"(f) : "v"((int)OFF_CNT + idx * 128), "s"(srd) : "memory");
    if (__builtin_amdgcn_ballot_w64(f < target) == 0) break;
    __builtin_amdgcn_s_sleep(1);
  }
}
__device__ __forceinline__ unsigned peek(const u32x4& srd, int idx) {
  unsigned f;
  asm volatile("buffer_load_dword %0, %1, %2, 0 offen sc1" : "=v"(f) : "v"((int)OFF_CNT + idx * 128), "s"(srd) : "memory");
  return f;
}
__device__ __forceinline__ void check(unsigned f, const u32x4& srd, int idx, unsigned target) {
  WAIT_VM(0);
  TIE(f);
  if (__builtin_amdgcn_ballot_w64(f < target) != 0) poll(srd, idx, target);
}
__device__ __forceinline__ void barriers(int n) {
#pragma unroll 1
  for (int t = 0; t < n; ++t) BARRIER();
}
template <int OFF>
__device__ __forceinline__ void rd_tr(bf16x4& lo, bf16x4& hi, unsigned addr) {
  asm volatile("ds_read_b64_tr_b16 %0, %2 offset:%c3\n\tds_read_b64_tr_b16 %1, %2 offset:%c4"
               : "=&v"(lo), "=&v"(hi) : "v"(addr), "i"(OFF), "i"(OFF + 512));
}
template <int OFF>
__device__ __forceinline__ void rd_128(bf16x8& r, unsigned addr) {
  asm volatile("ds_read_b128 %0, %1 offset:%c2" : "=v"(r) : "v"(addr), "i"(OFF));
}
template <int OFF>
__device__ __forceinline__ void rd_32(float& r, unsigned addr) {
  asm volatile("ds_read_b32 %0, %1 offset:%c2" : "=v"(r) : "v"(addr), "i"(OFF));
}
__device__ __forceinline__ void tie1(float& x) { TIE(x); }
__device__ __forceinline__ void tie4(f32x4& x) { TIE(x); }
__device__ __forceinline__ void tie8(bf16x8& x) { TIE(x); }
__device__ __forceinline__ void tie4h(bf16x4& x) { TIE(x); }
__device__ __forceinline__ bf16x8 cat(bf16x4 lo, bf16x4 hi) { return __builtin_shufflevector(lo, hi, 0, 1, 2, 3, 4, 5, 6, 7); }
__device__ __forceinline__ unsigned lds_addr(LDS_AS char* p) { return (unsigned)(size_t)p; }
__device__ __forceinline__ int wrapn(int v, int n) { return v >= n ? v - n : v; }
__device__ __forceinline__ f32x4 mfma(bf16x8 a, bf16x8 b, f32x4 c) {
  return __builtin_amdgcn_mfma_f32_16x16x32_bf16(a, b, c, 0, 0, 0);
}
__device__ __forceinline__ unsigned pack2(float a, float b) {
  f32x2 f = {a, b};
  bf16x2 h = __builtin_convertvector(f, bf16x2);
  return __builtin_bit_cast(unsigned, h);
}
__device__ __forceinline__ u32x2 pack4(f32x4 v) { return u32x2{pack2(v[0], v[1]), pack2(v[2], v[3])}; }
__device__ __forceinline__ float bflo(unsigned v) { return __uint_as_float(v << 16); }
__device__ __forceinline__ float bfhi(unsigned v) { return __uint_as_float(v & 0xffff0000u); }
__device__ __forceinline__ float red4_max(float x) {
  auto a = __builtin_amdgcn_permlane16_swap(__float_as_uint(x), __float_as_uint(x), false, false);
  x = fmaxf(__uint_as_float(a[0]), __uint_as_float(a[1]));
  auto b = __builtin_amdgcn_permlane32_swap(__float_as_uint(x), __float_as_uint(x), false, false);
  return fmaxf(__uint_as_float(b[0]), __uint_as_float(b[1]));
}
__device__ __forceinline__ float red4_sum(float x) {
  auto a = __builtin_amdgcn_permlane16_swap(__float_as_uint(x), __float_as_uint(x), false, false);
  x = __uint_as_float(a[0]) + __uint_as_float(a[1]);
  auto b = __builtin_amdgcn_permlane32_swap(__float_as_uint(x), __float_as_uint(x), false, false);
  return __uint_as_float(b[0]) + __uint_as_float(b[1]);
}
__device__ __forceinline__ unsigned tiled_lane(int lane) {
  int g = lane >> 4;
  return (unsigned)(((lane & 15) + 16 * (g >> 1)) * 16 + (g & 1) * 8);
}
__device__ __forceinline__ unsigned tiled_blk(int rowtile, int KS, int T16) {
  return (unsigned)((rowtile * KS + (T16 >> 1)) * 1024 + (T16 & 1) * 512);
}
__device__ __forceinline__ u32x4 pair16(f32x4 x, f32x4 y) {
  const u32x2 px = pack4(x), py = pack4(y);
  auto a = __builtin_amdgcn_permlane16_swap(px[0], py[0], false, false);
  auto b = __builtin_amdgcn_permlane16_swap(px[1], py[1], false, false);
  return u32x4{a[0], b[0], a[1], b[1]};
}
__device__ __forceinline__ unsigned tiled_lane16(int lane) { return (unsigned)(((lane & 15) + 16 * (lane >> 5)) * 16); }
template <int NTILE, int AUX, class Blk, class Val>
__device__ __forceinline__ void store_tiles(rsrc_t rs, int base, int lane, Blk&& blk, Val&& val) {
  const bool odd = (lane >> 4) & 1;
  const int l16 = (int)tiled_lane16(lane);
#pragma unroll
  for (int i = 0; i + 1 < NTILE; i += 2)
    __builtin_amdgcn_raw_buffer_store_b128(pair16(val(i), val(i + 1)), rs, l16 + (odd ? (int)(blk(i + 1) - blk(i)) + 0x100000 : 0x100000),
                                           base - 0x100000 + (int)blk(i), AUX);
  if (NTILE & 1) __builtin_amdgcn_raw_buffer_store_b64(pack4(val(NTILE - 1)), rs, (int)tiled_lane(lane), base + (int)blk(NTILE - 1), AUX);
}
__device__ __forceinline__ int timg_src(int lane, int sigma, int pitch) {
  int r = lane >> 3, c = (lane & 7) ^ (2 * ((r >> 1) & 1) + 4 * sigma);
  return r * pitch + c * 16;
}
template <int NB>
__device__ __forceinline__ int timg_rd(int lane, int nt) {
  int g = lane >> 4, q = (lane & 15) >> 2, p = lane & 3;
  int f = 2 * ((q >> 1) & 1) + 4 * (g & 1);
  return (g * NB + (nt >> 2)) * 1024 + q * 128 + (((2 * (nt & 3)) ^ f) + (p >> 1)) * 16 + (p & 1) * 8;
}

template <int MT, int NT>
struct Frags { bf16x8 a[MT]; bf16x8 b[NT]; bf16x4 l[NT], h[NT]; };

template <int MT, int NT, bool BTR, int AM, int BM, int AO, int BO, int I>
__device__ __forceinline__ void rd_item(Frags<MT, NT>& n, unsigned ab, const unsigned (&bb)[NT]) {
  if constexpr (I < NT) {
    if constexpr (BTR) rd_tr<BO>(n.l[I], n.h[I], bb[I]);
    else rd_128<BO + I * BM>(n.b[I], bb[0]);
  } else if constexpr (I < NT + MT) {
    rd_128<AO + (I - NT) * AM>(n.a[I - NT], ab);
  }
}
template <int MT, int NT, bool BTR, int AM, int BM, int AO, int BO>
__device__ __forceinline__ void rd_set(Frags<MT, NT>& n, unsigned ab, const unsigned (&bb)[NT]) {
  static_for<0, MT + NT>([&](auto ic) { rd_item<MT, NT, BTR, AM, BM, AO, BO, decltype(ic)::value>(n, ab, bb); });
}
template <int MT, int NT, bool BTR, bool AH = false>
__device__ __forceinline__ void land(Frags<MT, NT>& c) {
  WAIT_LGKM(AH ? MT + (BTR ? 2 : 1) * NT : 0);
#pragma unroll
  for (int j = 0; j < NT; ++j) {
    if constexpr (BTR) { TIE(c.l[j]); TIE(c.h[j]); } else TIE(c.b[j]);
  }
#pragma unroll
  for (int m = 0; m < MT; ++m) TIE(c.a[m]);
  SCHED_FENCE();
}
template <int MT, int NT, bool BTR, bool SWAP, bool ZC, int AM, int BM, int AO, int BO, bool AH = false, class X>
__device__ __forceinline__ void kstep(f32x4 (&acc)[MT][NT], Frags<MT, NT>& c, Frags<MT, NT>& n, unsigned ab,
                                      const unsigned (&bb)[NT], X&& extra) {
  land<MT, NT, BTR, AH>(c);
  static_for<0, MT * NT>([&](auto ic) {
    constexpr int I = decltype(ic)::value, m = I / NT, j = I % NT;
    bf16x8 bf;
    if constexpr (BTR) bf = cat(c.l[j], c.h[j]); else bf = c.b[j];
    const f32x4 ci = ZC ? f32x4{0.f, 0.f, 0.f, 0.f} : acc[m][j];
    acc[m][j] = SWAP ? mfma(bf, c.a[m], ci) : mfma(c.a[m], bf, ci);
    tie4(acc[m][j]);
    rd_item<MT, NT, BTR, AM, BM, AO, BO, I>(n, ab, bb);
    extra(ic);
    SCHED_FENCE();
  });
  static_for<MT * NT, (MT + NT > MT * NT ? MT + NT : MT * NT)>(
      [&](auto ic) { rd_item<MT, NT, BTR, AM, BM, AO, BO, decltype(ic)::value>(n, ab, bb); });
}
struct NoExtra { template <class T> __device__ __forceinline__ void operator()(T) const {} };

struct NoHook { __device__ __forceinline__ void operator()() const {} };
struct NoTail { __device__ __forceinline__ void operator()(int) const {} };
template <int NST, int NS, int V, int TV = 0, bool XB = false, class Issue, class Hook = NoHook, class Tail = NoTail>
__device__ __forceinline__ void loader_loop(Issue&& issue, Hook&& hook = NoHook{}, Tail&& tail = NoTail{}) {
#pragma unroll
  for (int s = 0; s < NS - 1; ++s) issue(s, s);
  if (XB) BARRIER();
  WAIT_VM(V * (NS - 2));
  BARRIER();
  hook();
  int slot = NS - 1;
#pragma unroll 1
  for (int t = 0; t <= NST - NS; ++t) {
    WAIT_VM(V * (NS - 3));
    BARRIER();
    issue(t + NS - 1, slot);
    slot = wrapn(slot + 1, NS);
  }
  static_for<1, NS>([&](auto rc) {
    constexpr int R = decltype(rc)::value;
    WAIT_VM(R == NS - 1 ? 0 : V * (NS - 2 - R) + (R > 1 ? TV : 0));
    BARRIER();
    if constexpr (R == 1) tail(slot);
  });
}

template <int VP, int V, class Issue>
__device__ __forceinline__ void stream12(int& slot, Issue&& issue) {
  WAIT_VM(2 * VP); BARRIER(); issue(0, slot); slot = wrapn(slot + 1, 5);
  WAIT_VM(VP + V); BARRIER(); issue(1, slot); slot = wrapn(slot + 1, 5);
#pragma unroll 1
  for (int t = 2; t < 12; ++t) {
    WAIT_VM(2 * V); BARRIER(); issue(t, slot); slot = wrapn(slot + 1, 5);
  }
}

template <int MW, int NW, bool BTR, bool SWAP, int BM, int BKS, int SLOT, int ASTEP, int FT>
__device__ __forceinline__ void gemm12(f32x4 (&acc)[MW][NW], unsigned a0, unsigned a1, const unsigned (&brd)[NW], int so,
                                       char* __restrict__ ws, const Pending& pend, int lane) {
  Frags<MW, NW> x0, x1, y0, y1;
  unsigned bc[NW];
#pragma unroll
  for (int n = 0; n < NW; ++n) bc[n] = brd[n] + so;
  rd_set<MW, NW, BTR, 2048, BM, 0, 0>(x0, ASTEP ? a0 : a0 + so, bc);
  rd_set<MW, NW, BTR, 2048, BM, 0, BKS>(x1, ASTEP ? a1 : a1 + so, bc);
  auto stage = [&](Frags<MW, NW>& c0, Frags<MW, NW>& c1, Frags<MW, NW>& n0, Frags<MW, NW>& n1) {
    so = wrapn(so + SLOT, 5 * SLOT);
    a0 += ASTEP; a1 += ASTEP;
#pragma unroll
    for (int n = 0; n < NW; ++n) bc[n] = brd[n] + so;
    BARRIER();
    kstep<MW, NW, BTR, SWAP, false, 2048, BM, 0, 0, true>(acc, c0, n0, ASTEP ? a0 : a0 + so, bc, NoExtra{});
    kstep<MW, NW, BTR, SWAP, false, 2048, BM, 0, BKS, true>(acc, c1, n1, ASTEP ? a1 : a1 + so, bc, NoExtra{});
  };
#pragma unroll 1
  for (int t = 0; t < 12; t += 2) {
    if (t == FT) flush(ws, pend, lane);
    stage(x0, x1, y0, y1);
    stage(y0, y1, x0, x1);
  }
  land<MW, NW, BTR>(x0);
  land<MW, NW, BTR>(x1);
}
__device__ __forceinline__ int t96_src(int pc, int lane, int pitch, int odd) {
  const int ks = pc / 6, r = pc % 6, g1 = (lane >> 4) & 1, r16 = 8 * g1 + 4 * (lane >> 5) + ((lane >> 2) & 3);
  return r < 4 ? (ks * 32 + r * 8) * pitch + odd * 64 + timg_src(lane, r & 1, pitch)
               : (ks * 32 + (r - 4) * 16 + r16) * pitch + (odd ? 0 : 128) + (((lane & 3) ^ (2 * g1)) * 16);
}
__device__ __forceinline__ int t96_rd(int lane, int nt, int odd) {
  const int g = lane >> 4, q = (lane & 15) >> 2, p = lane & 3;
  const bool narrow = odd ? nt < 2 : nt >= 4;
  return narrow ? 4096 + (g >> 1) * 1024 + (g & 1) * 256 + q * 64 + (((2 * (nt & 1) + (p >> 1)) ^ (2 * (g & 1))) * 16) + (p & 1) * 8
                : timg_rd<1>(lane, odd ? nt - 2 : nt);
}

#define K0_PFN 144
constexpr int K0_T1 = 72 * KSPL, K0_T2 = 2 * RT, K0_Z = 2, K0_PF = K0_PFN;
__global__ __launch_bounds__(256) void k0_prep(const u16* __restrict__ x_img, const u16* __restrict__ x_txt,
                                               const u16* __restrict__ cvec, const u16* __restrict__ wmod_img,
                                               const u16* __restrict__ wmod_txt, const u16* __restrict__ wqkv_img,
                                               const u16* __restrict__ wqkv_txt, const u16* __restrict__ wo_img,
                                               const u16* __restrict__ wo_txt, char* __restrict__ ws) {
  __shared__ __attribute__((aligned(1024))) char smem_[24576];
  LDS_AS char* smem = (LDS_AS char*)smem_;
  const int bid = blockIdx.x, tid = threadIdx.x, lane = tid & 63;
  const int wave = __builtin_amdgcn_readfirstlane(tid >> 6);
  STAMP(ws, 0, 0);
  if (bid >= K0_T1 + K0_T2 + K0_Z) {
    const int i = bid - (K0_T1 + K0_T2 + K0_Z);
    const char* p = i < 18 ? (const char*)wo_img + i * 65536 : (i < 36 ? (const char*)wo_txt + (i - 18) * 65536
                  : (i < 90 ? (const char*)wqkv_img + (i - 36) * 65536 : (const char*)wqkv_txt + (i - 90) * 65536));
    u32x4 v[16];
#pragma unroll
    for (int k = 0; k < 16; ++k) v[k] = *(const u32x4*)(p + k * 4096 + tid * 16);
#pragma unroll
    for (int k = 0; k < 16; ++k) asm volatile("" ::"v"(v[k]));
    return;
  } else if (bid >= K0_T1 + K0_T2) {
    for (unsigned o = ((bid - K0_T1 - K0_T2) * 256 + tid) * 16; o < SZ_CNT; o += K0_Z * 4096) *(u32x4*)(ws + OFF_CNT + o) = u32x4{0u, 0u, 0u, 0u};
    return;
  } else if (bid < K0_T1) {
    constexpr int ROWS = D / KSPL, PER = ROWS / 32;
    const int i = bid, s = i / (36 * KSPL), rem = i % (36 * KSPL), cb = rem % 36, kh = rem / 36;
    const u16* w = (s ? wmod_txt : wmod_img) + (size_t)(kh * ROWS + (tid >> 3)) * (6 * D) + cb * 64 + (tid & 7) * 8;
    u16 cv[3];
#pragma unroll
    for (int e = 0; e < 3; ++e) { const int z = tid + e * 256; cv[e] = cvec[(z / ROWS) * D + kh * ROWS + z % ROWS]; }
    u32x4 wv[PER];
#pragma unroll
    for (int r = 0; r < PER; ++r) wv[r] = *(const u32x4*)(w + (size_t)r * 32 * (6 * D));
    LDS_AS float* sc = (LDS_AS float*)smem;
    LDS_AS float* red = (LDS_AS float*)(smem + 8192);
#pragma unroll
    for (int e = 0; e < 3; ++e) {
      const int z = tid + e * 256;
      const float v = __uint_as_float((unsigned)cv[e] << 16);
      sc[(z % ROWS) * 2 + z / ROWS] = v / (1.0f + __expf(-v));
    }
    __syncthreads();
    float a0[8] = {}, a1[8] = {};
#pragma unroll
    for (int r = 0; r < PER; ++r) {
      f32x2 sv = *(LDS_AS f32x2*)(sc + ((tid >> 3) + r * 32) * 2);
#pragma unroll
      for (int j = 0; j < 4; ++j) {
        float lo = bflo(wv[r][j]), hi = bfhi(wv[r][j]);
        a0[2 * j] = fmaf(sv[0], lo, a0[2 * j]); a0[2 * j + 1] = fmaf(sv[0], hi, a0[2 * j + 1]);
        a1[2 * j] = fmaf(sv[1], lo, a1[2 * j]); a1[2 * j + 1] = fmaf(sv[1], hi, a1[2 * j + 1]);
      }
    }
    LDS_AS float* rp = red + (tid >> 3) * 128 + (tid & 7) * 8;
    *(LDS_AS f32x4*)(rp) = f32x4{a0[0], a0[1], a0[2], a0[3]};
    *(LDS_AS f32x4*)(rp + 4) = f32x4{a0[4], a0[5], a0[6], a0[7]};
    *(LDS_AS f32x4*)(rp + 64) = f32x4{a1[0], a1[1], a1[2], a1[3]};
    *(LDS_AS f32x4*)(rp + 68) = f32x4{a1[4], a1[5], a1[6], a1[7]};
    __syncthreads();
    {
      const int o = tid >> 1, hf = tid & 1;
      float t0 = 0.f, t1 = 0.f;
#pragma unroll
      for (int g = 0; g < 16; g += 2) { t0 += red[(hf * 16 + g) * 128 + o]; t1 += red[(hf * 16 + g + 1) * 128 + o]; }
      float t = t0 + t1;
      t += __shfl_xor(t, 1);
      float* mod = (float*)(ws + OFF_MOD);
      if (hf == 0) mod[((kh * 2 + s) * 2 + (o >> 6)) * 2304 + cb * 64 + (o & 63)] = t;
    }
  } else {
    const int i = bid - K0_T1, b = i / RT, rt = i % RT;
    const u16* src = rt < 64 ? x_img + (size_t)(b * NI + rt * 16) * D : x_txt + (size_t)(b * NTX + (rt - 64) * 16) * D;
    src += (lane & 15) * D + wave * 192 + (lane >> 4) * 8;
    u32x4 v[6];
#pragma unroll
    for (int k = 0; k < 6; ++k) v[k] = *(const u32x4*)(src + k * 32);
    LDS_AS float* red = (LDS_AS float*)smem;
    float s0 = 0.f, s1 = 0.f;
#pragma unroll
    for (int k = 0; k < 6; ++k)
#pragma unroll
      for (int j = 0; j < 4; ++j) { s0 += bflo(v[k][j]); s1 += bfhi(v[k][j]); }
    const float s = red4_sum(s0 + s1);
    if (lane < 16) red[wave * 16 + lane] = s;
    __syncthreads();
    const int r = lane & 15;
    const float mean = ((red[r] + red[16 + r]) + (red[32 + r] + red[48 + r])) * (1.0f / D);
    float q0 = 0.f, q1 = 0.f;
#pragma unroll
    for (int k = 0; k < 6; ++k)
#pragma unroll
      for (int j = 0; j < 4; ++j) {
        float lo = bflo(v[k][j]) - mean, hi = bfhi(v[k][j]) - mean;
        q0 = fmaf(lo, lo, q0); q1 = fmaf(hi, hi, q1);
      }
    const float q = red4_sum(q0 + q1);
    if (lane < 16) red[64 + wave * 16 + lane] = q;
    __syncthreads();
    const float var = ((red[64 + r] + red[80 + r]) + (red[96 + r] + red[112 + r])) * (1.0f / D);
    const float rstd = 1.0f / sqrtf(var + 1e-5f);
    char* dst = ws + OFF_XN + (size_t)(i * KD + wave * 6) * 1024 + lane * 16;
#pragma unroll
    for (int k = 0; k < 6; ++k) {
      u32x4 o;
#pragma unroll
      for (int j = 0; j < 4; ++j) {
        typedef __attribute__((ext_vector_type(2))) _Float16 f16x2;
        const f16x2 h = {(_Float16)((bflo(v[k][j]) - mean) * rstd), (_Float16)((bfhi(v[k][j]) - mean) * rstd)};
        o[j] = __builtin_bit_cast(unsigned, h);
      }
      *(u32x4*)(dst + k * 1024) = o;
    }
  }
  STAMP(ws, 0, 3);
}

constexpr int SLOT = 30 * 1024;
constexpr int P1_SB = 98304, P1_SGS = P1_SB + 4 * 12288;
constexpr int RED = P1_SB + 5 * 12288, ARR = RED + 1024, MAIN_LDS = ARR + 32;
constexpr int WP = 3 * D * 2;

__device__ __forceinline__ void fmix_lo(float& d, unsigned v, float g, float s) {
  asm volatile("v_fma_mix_f32 %0, %1, %2, %3 op_sel_hi:[1,0,0]" : "=v"(d) : "v"(v), "v"(g), "v"(s));
}
__device__ __forceinline__ void fmix_hi(float& d, unsigned v, float g, float s) {
  asm volatile("v_fma_mix_f32 %0, %1, %2, %3 op_sel:[1,0,0] op_sel_hi:[1,0,0]" : "=v"(d) : "v"(v), "v"(g), "v"(s));
}
template <int OFF>
__device__ __forceinline__ void rd_f4(f32x4& r, unsigned addr) {
  asm volatile("ds_read_b128 %0, %1 offset:%c2" : "=v"(r) : "v"(addr), "i"(OFF));
}
__device__ __forceinline__ u32x4 modulate(u32x4 x, const f32x4 (&g)[4]) {
  float t[8];
#pragma unroll
  for (int d = 0; d < 4; ++d) {
    fmix_lo(t[2 * d], x[d], g[2 + (d >> 1)][(d & 1) * 2], g[d >> 1][(d & 1) * 2]);
    fmix_hi(t[2 * d + 1], x[d], g[2 + (d >> 1)][(d & 1) * 2 + 1], g[d >> 1][(d & 1) * 2 + 1]);
  }
  return u32x4{pack2(t[0], t[1]), pack2(t[2], t[3]), pack2(t[4], t[5]), pack2(t[6], t[7])};
}

struct P2Tile { int b, ti, tj; };
__device__ __forceinline__ P2Tile p2_tile(int bid) {
  const int x = bid & 7, idx = bid >> 3, qd = x >> 1;
  P2Tile t;
  t.b = x & 1;
  if (idx == 30) { t.ti = 5; t.tj = 5; }
  else if (qd == 0) { t.ti = idx / 6; t.tj = idx % 6; }
  else if (qd == 1) { t.ti = idx / 5; t.tj = 6 + idx % 5; }
  else if (qd == 2) { t.ti = 6 + idx / 6; t.tj = 5 + idx % 6; }
  else { t.ti = 5 + idx / 5; t.tj = idx % 5; }
  return t;
}
__device__ __forceinline__ int p2_counter(const P2Tile& t, int lane) {
  const int u = min(lane, 11), sec = u < 6 ? 1 : 0, rt = (u < 6 ? t.tj * 6 + u : t.ti * 6 + u - 6);
  return rt < 64 ? C_QK + (t.b * 2 + sec) * 8 + rt / 9 : C_T + sec;
}
__device__ __forceinline__ void p2_issue(LDS_AS char* smem, rsrc_t rs, const P2Tile& t, int l, int lane, int s, int slot) {
#pragma unroll
  for (int i = 0; i < 4; ++i) {
    const int u = l * 4 + i;
    const int so = (u < 6 ? (int)OFF_K + (t.b * RT + t.tj * 6 + u) * KD * 1024 : (int)OFF_Q + (t.b * RT + t.ti * 6 + u - 6) * KD * 1024) + s * 2048;
    dma_sc1<0>(rs, smem + slot * SLOT + u * 2048, lane * 16, so);
    dma_sc1<1024>(rs, smem + slot * SLOT + u * 2048, lane * 16, so);
  }
}
__device__ __forceinline__ void p2_mfma(LDS_AS char* smem, char* __restrict__ ws, const P2Tile& t, int so, int wave, int lane,
                                        LDS_AS unsigned* arr, const Pending& pend) {
  rsrc_t rs = make_rsrc(ws, WS_END);
  const int wk = wave >> 1, wq = wave & 1, b = t.b, ti = t.ti, tj = t.tj;
  const unsigned krd = lds_addr(smem) + wk * 6144 + lane * 16;
  const unsigned qrd[3] = {lds_addr(smem) + 12288 + wq * 6144 + lane * 16, 0, 0};
  f32x4 acc[3][3] = {};
  gemm12<3, 3, false, false, 2048, 1024, SLOT, 0, 4>(acc, krd, krd + 1024, qrd, so, ws, pend, lane);
  STAMP(ws, 2, 2);
  constexpr float SC = 0.036084391824351615f * 1.4426950408889634f;
  LDS_AS float* red = (LDS_AS float*)(smem + RED);
  const int g = lane >> 4;
  float mx[3], ls[3];
#pragma unroll
  for (int j = 0; j < 3; ++j) {
    float m0 = fmaxf(fmaxf(acc[0][j][0], acc[0][j][1]), fmaxf(acc[0][j][2], acc[0][j][3]));
    float m1 = fmaxf(fmaxf(acc[1][j][0], acc[1][j][1]), fmaxf(acc[1][j][2], acc[1][j][3]));
    float m2 = fmaxf(fmaxf(acc[2][j][0], acc[2][j][1]), fmaxf(acc[2][j][2], acc[2][j][3]));
    mx[j] = red4_max(fmaxf(fmaxf(m0, m1), m2));
  }
  if (lane < 48) red[wk * 96 + wq * 48 + lane] = g == 0 ? mx[0] : (g == 1 ? mx[1] : mx[2]);
  WAIT_LGKM(0);
  BARRIER();
#pragma unroll
  for (int j = 0; j < 3; ++j) mx[j] = fmaxf(mx[j], red[(wk ^ 1) * 96 + wq * 48 + j * 16 + (lane & 15)]) * SC;
#pragma unroll
  for (int j = 0; j < 3; ++j) {
    float l = 0.f;
#pragma unroll
    for (int i = 0; i < 3; ++i) {
#pragma unroll
      for (int r = 0; r < 4; ++r) acc[i][j][r] = __builtin_amdgcn_exp2f(fmaf(acc[i][j][r], SC, -mx[j]));
      l += (acc[i][j][0] + acc[i][j][1]) + (acc[i][j][2] + acc[i][j][3]);
    }
    ls[j] = l;
  }
  store_tiles<9, 16>(rs, (int)OFF_P, lane, [&](int i) { return tiled_blk(b * RT + ti * 6 + wq * 3 + i / 3, KK, tj * 6 + wk * 3 + i % 3); },
                     [&](int i) { return acc[i % 3][i / 3]; });
#pragma unroll
  for (int j = 0; j < 3; ++j) ls[j] = red4_sum(ls[j]);
  if (lane < 48) {
    const u32x2 v = {__float_as_uint(g == 0 ? mx[0] : (g == 1 ? mx[1] : mx[2])), __float_as_uint(g == 0 ? ls[0] : (g == 1 ? ls[1] : ls[2]))};
    __builtin_amdgcn_raw_buffer_store_b64(v, rs, g * 2816 + (lane & 15) * 16, (int)OFF_ST + ((b * RT + ti * 6 + wq * 3) * 11 + tj) * 256 + wk * 8, 16);
  }
  if (last_wave(arr, 4, lane)) signal(ws, C_P + b * 11 + ti);
  STAMP(ws, 2, 3);
}

template <int MW>
__device__ __forceinline__ void big_p1(LDS_AS char* smem, char* __restrict__ ws, int gA, int g, int col, int m0, int wn, int lane,
                                       LDS_AS unsigned* arr, const Pending& pw, Pending& pv) {
  rsrc_t rs = make_rsrc(ws, WS_END);
  const unsigned l0 = lds_addr(smem), aa = l0 + m0 * 2048 + lane * 16;
  const int b = gA >= RT ? 1 : 0, sec = col >> 3, nt0 = (col & 7) * 6 + wn * 3;
  {
    unsigned brd[3];
#pragma unroll
    for (int n = 0; n < 3; ++n) brd[n] = l0 + 18432 + t96_rd(lane, wn * 3 + n, col & 1);
    f32x4 acc[MW][3] = {};
    gemm12<MW, 3, true, true, 0, 6144, SLOT, 0, 4>(acc, aa, aa + 1024, brd, 2 * SLOT, ws, pw, lane);
    STAMP(ws, 1, 2);
    store_tiles<MW * 3, 16>(rs, (int)(sec ? OFF_K : OFF_Q), lane, [&](int i) { return tiled_blk(gA + m0 + i / 3, KD, nt0 + i % 3); },
                            [&](int i) { return acc[i / 3][i % 3]; });
    STAMP(ws, 1, 3);
  }
  {
    const unsigned brd[3] = {l0 + 18432 + wn * 3072 + lane * 16, 0, 0};
    f32x4 acc[MW][3] = {};
    gemm12<MW, 3, false, false, 1024, 6144, SLOT, 0, 4>(acc, aa, aa + 1024, brd, 4 * SLOT, ws,
                                                         Pending{C_QK + (b * 2 + sec) * 8 + g, -1, arr + 2}, lane);
    STAMP(ws, 4, 2);
    store_tiles<MW * 3, 16>(rs, (int)(sec ? OFF_VT2 : OFF_VT1), lane,
                            [&](int i) { return tiled_blk(b * 48 + nt0 + i % 3, KK, gA - b * RT + m0 + i / 3); },
                            [&](int i) { return acc[i / 3][i % 3]; });
    pv = Pending{C_V + (b * 2 + sec) * 8 + (col & 7), -1, arr + 3};
    STAMP(ws, 4, 3);
  }
}

__device__ __forceinline__ void big_wg(LDS_AS char* smem, char* __restrict__ ws, const u16* __restrict__ wqkv_img,
                                       const u16* __restrict__ wo_img, const u16* __restrict__ wo_txt, int bid, int wave8, int lane) {
  const int x = bid & 7, idx = bid >> 3, b = x & 1, g = idx % 7, col = (x >> 1) * 4 + idx / 7;
  const int gA = b * RT + g * 9, grp = b * 7 + g, hb = (int)OFF_H + gA * KD * 1024;
  const int wi = idx < 24 ? idx : idx - 4, wmb = b * 6 + wi % 6, wcol = (x >> 1) * 4 + wi / 6;
  const P2Tile t2 = p2_tile(bid);
  const u32x4 srd_ws = make_srd(ws, WS_END);
  rsrc_t rs = make_rsrc(ws, WS_END);
  LDS_AS unsigned* arr = (LDS_AS unsigned*)(smem + ARR);
  if (wave8 == 7) {
    const int c2 = p2_counter(t2, lane);
    barriers(7);
    unsigned f = peek(srd_ws, C_H + grp);
    barriers(2);
    check(f, srd_ws, C_H + grp, 12);
    barriers(10);
    f = peek(srd_ws, C_W + col);
    barriers(2);
    check(f, srd_ws, C_W + col, 12);
    barriers(10);
    f = peek(srd_ws, c2);
    barriers(2);
    check(f, srd_ws, c2, 8);
    barriers(17);
    return;
  }
  if (wave8 >= 4) {
    const int l = wave8 - 4;
    rsrc_t rw = make_rsrc(wqkv_img, D * 3 * D * 2);
    rsrc_t ro = make_rsrc(wcol < 8 ? wo_img : wo_txt, D * D * 2);
    int bo[4], bw[4], av[3];
#pragma unroll
    for (int i = 0; i < 4; ++i) {
      bo[i] = (wcol & 7) * 192 + t96_src(l * 4 + i, lane, D * 2, wcol & 1);
      bw[i] = col * 192 + t96_src(l * 4 + i, lane, WP, col & 1);
    }
#pragma unroll
    for (int i = 0; i < 3; ++i) {
      const int u = min(l * 3 + i, 7), r = u * 8 + (lane >> 3);
      av[i] = (wmb * 64 + r) * WP + 2 * D * 2 + (((lane & 7) ^ ((r >> 1) & 7)) * 16);
    }
    auto issueW = [&](int s, int slot) {
#pragma unroll
      for (int i = 0; i < 3; ++i) dma<0>(rw, smem + slot * SLOT + min(l * 3 + i, 7) * 1024, av[i], s * 128);
#pragma unroll
      for (int i = 0; i < 4; ++i) dma<0>(ro, smem + slot * SLOT + 18432 + (l * 4 + i) * 1024, bo[i], s * 64 * (D * 2));
    };
    auto issueA = [&](int s, int slot) {
#pragma unroll
      for (int i = 0; i < 3; ++i) {
        LDS_AS char* sa = smem + slot * SLOT + (l * 3 + i) * 2048;
        dma_sc1<0>(rs, sa, lane * 16, hb + ((l * 3 + i) * KD + 2 * s) * 1024);
        dma_sc1<1024>(rs, sa, lane * 16, hb + ((l * 3 + i) * KD + 2 * s) * 1024);
      }
    };
#pragma unroll
    for (int s = 0; s < 4; ++s) issueW(s, s);
    WAIT_VM(21); BARRIER();
    int slot = 4;
#pragma unroll 1
    for (int s = 4; s < 12; ++s) {
      WAIT_VM(14); BARRIER(); issueW(s, slot); slot = wrapn(slot + 1, 5);
    }
    stream12<7, 10>(slot, [&](int s, int sl) {
#pragma unroll
      for (int i = 0; i < 4; ++i) dma<0>(rw, smem + sl * SLOT + 18432 + (l * 4 + i) * 1024, bw[i], s * 64 * WP);
      issueA(s, sl);
    });
    stream12<10, 10>(slot, [&](int s, int sl) {
#pragma unroll
      for (int i = 0; i < 4; ++i) {
        const int pc = l * 4 + i;
        dma_sc1(rs, smem + sl * SLOT + 18432 + pc * 1024, lane * 16, (int)OFF_WVO + ((col * 6 + pc % 6) * KD + 2 * s + pc / 6) * 1024);
      }
      issueA(s, sl);
    });
    stream12<10, 8>(slot, [&](int s, int sl) { p2_issue(smem, rs, t2, l, lane, s, sl); });
    WAIT_VM(16); BARRIER();
    WAIT_VM(8); BARRIER();
    WAIT_VM(0); BARRIER();
    BARRIER();
    BARRIER();
    return;
  }
  const int c = wave8, wm = c >> 1, wn = c & 1;
  Pending ph{-1, -1, arr};
  if (col < 12) {
    const int W = col * 4 + c, ks = W >> 1, t0 = (W & 1) * 5;
    u32x4 xr[5], mp[4][KSPL];
#pragma unroll
    for (int i = 0; i < 4; ++i)
#pragma unroll
      for (int k = 0; k < KSPL; ++k)
        mp[i][k] = aload(srd_ws, (lane >> 4) * 32, (int)OFF_MOD + ((k * 4 + b) * 2304 + (i >> 1) * 768 + ks * 32 + (i & 1) * 4) * 4);
#pragma unroll
    for (int i = 0; i < 5; ++i) xr[i] = aload(srd_ws, lane * 16, (int)OFF_XN + ((gA + min(t0 + i, 8)) * KD + ks) * 1024);
    WAIT_VM(0);
    f32x4 gs[4];
#pragma unroll
    for (int i = 0; i < 4; ++i) {
      TIE(mp[i][0]); TIE(mp[i][1]);
      gs[i] = __builtin_bit_cast(f32x4, mp[i][0]) + __builtin_bit_cast(f32x4, mp[i][1]);
      if (i >= 2) gs[i] += 1.0f;
    }
#pragma unroll
    for (int i = 0; i < 5; ++i) {
      TIE(xr[i]);
      __builtin_amdgcn_raw_buffer_store_b128(modulate(xr[i], gs), rs, lane * 16, hb + (min(t0 + i, 8) * KD + ks) * 1024, 16);
    }
    ph.i0 = C_H + grp;
  }
  Pending pw{-1, -1, arr + 1};
  {
    const int sw = (lane & 15) >> 1, gq = lane >> 4;
    const unsigned ab = lds_addr(smem) + wm * 4096 + (lane & 15) * 128;
    unsigned brd[3];
#pragma unroll
    for (int n = 0; n < 3; ++n) brd[n] = lds_addr(smem) + 18432 + t96_rd(lane, wn * 3 + n, wcol & 1);
    f32x4 acc[2][3] = {};
    BARRIER();
    STAMP(ws, 5, 1);
    gemm12<2, 3, true, false, 0, 6144, SLOT, 0, 2>(acc, ab + ((gq ^ sw) * 16), ab + (((4 + gq) ^ sw) * 16), brd, 0, ws, ph, lane);
    STAMP(ws, 5, 2);
    if (idx < 24) {
      store_tiles<6, 16>(rs, (int)OFF_WVO, lane, [&](int i) { return tiled_blk(wcol * 6 + wn * 3 + i % 3, KD, wmb * 4 + wm * 2 + i / 3); },
                         [&](int i) { return acc[i / 3][i % 3]; });
      pw.i0 = C_W + wcol;
    }
    STAMP(ws, 5, 3);
  }
  Pending pv;
  if (wm == 0) big_p1<5>(smem, ws, gA, g, col, 0, wn, lane, arr, pw, pv);
  else big_p1<4>(smem, ws, gA, g, col, 5, wn, lane, arr, pw, pv);
  p2_mfma(smem, ws, t2, 1 * SLOT, c, lane, arr + 4, pv);
}

template <int MT, bool VT, bool FIRST>
__device__ __forceinline__ void small_p1(LDS_AS char* smem, char* __restrict__ ws, const u16* __restrict__ wsrc, int wcol, int obase,
                                         int gA, int col, int smod, int wave8, int lane, const Pending& pend) {
  constexpr int MH = MT / 2;
  const u32x4 srd_ws = make_srd(ws, WS_END);
  rsrc_t rs = make_rsrc(ws, WS_END);
  auto gtile = [&](int lt) { return lt < MH ? gA + lt : gA + RT + (lt - MH); };
  if (wave8 == 7) {
    if (FIRST) {
      u32x4 mp[12][KSPL];
#pragma unroll
      for (int h = 0; h < 12; ++h)
#pragma unroll
        for (int k = 0; k < KSPL; ++k) mp[h][k] = aload(srd_ws, lane * 16, (int)OFF_MOD + (((k * 2 + smod) * 2 + h / 6) * 2304 + (h % 6) * 256) * 4);
      WAIT_VM(0);
#pragma unroll
      for (int h = 0; h < 12; ++h) {
        TIE(mp[h][0]); TIE(mp[h][1]);
        f32x4 v = __builtin_bit_cast(f32x4, mp[h][0]) + __builtin_bit_cast(f32x4, mp[h][1]);
        if (h % 6 >= 3) v += 1.0f;
        *(LDS_AS f32x4*)(smem + P1_SGS + h * 1024 + lane * 16) = v;
      }
      WAIT_LGKM(0);
    }
    barriers(FIRST ? 15 : 14);
    return;
  }
  if (wave8 >= 4) {
    const int l = wave8 - 4;
    rsrc_t rb = VT ? rs : make_rsrc(wsrc, D * 3 * D * 2);
    int bv[4];
#pragma unroll
    for (int i = 0; i < 4; ++i) {
      const int pc = l * 4 + i;
      bv[i] = VT ? ((col * 6 + pc % 6) * KD + pc / 6) * 1024 + lane * 16 : wcol + t96_src(pc, lane, WP, col & 1);
    }
    if (VT) poll(srd_ws, C_W + col, 12);
    loader_loop<12, 5, 4, 0, FIRST>([&](int s, int slot) {
#pragma unroll
      for (int i = 0; i < 4; ++i) {
        if (VT) dma_sc1(rb, smem + P1_SB + slot * 12288 + (l * 4 + i) * 1024, bv[i], (int)OFF_WVO + s * 2048);
        else dma<0>(rb, smem + P1_SB + slot * 12288 + (l * 4 + i) * 1024, bv[i], s * 64 * WP);
      }
    });
    BARRIER();
    return;
  }
  const int c = wave8, wm = c >> 1, wn = c & 1;
  auto aoff = [&](int st) { return st * (MT * 2048); };
  if (FIRST) {
    u32x4 xr[6][MT];
#pragma unroll
    for (int k = 0; k < 6; ++k)
#pragma unroll
      for (int i = 0; i < MT; ++i) xr[k][i] = aload(srd_ws, lane * 16, (int)OFF_XN + (gtile(i) * KD + c * 6 + k) * 1024);
    BARRIER();
    WAIT_VM(0);
#pragma unroll
    for (int k = 0; k < 6; ++k) {
      f32x4 gs[2][4];
#pragma unroll
      for (int z = 0; z < 2; ++z) {
        const unsigned gp = lds_addr(smem) + P1_SGS + z * 6144 + (c * 6 + k) * 128 + (lane >> 4) * 32;
        rd_f4<0>(gs[z][0], gp); rd_f4<16>(gs[z][1], gp); rd_f4<3072>(gs[z][2], gp); rd_f4<3088>(gs[z][3], gp);
      }
      WAIT_LGKM(0);
#pragma unroll
      for (int z = 0; z < 2; ++z)
#pragma unroll
        for (int i = 0; i < 4; ++i) TIE(gs[z][i]);
#pragma unroll
      for (int i = 0; i < MT; ++i) {
        TIE(xr[k][i]);
        *(LDS_AS u32x4*)(smem + aoff(c * 3 + (k >> 1)) + i * 2048 + (k & 1) * 1024 + lane * 16) = modulate(xr[k][i], gs[i < MH ? 0 : 1]);
      }
    }
    WAIT_LGKM(0);
  }
  const unsigned aa = lds_addr(smem) + wm * MH * 2048 + lane * 16;
  unsigned brd[3];
#pragma unroll
  for (int n = 0; n < 3; ++n) brd[n] = lds_addr(smem) + P1_SB + (VT ? wn * 3072 + lane * 16 : t96_rd(lane, wn * 3 + n, col & 1));
  f32x4 acc[MH][3] = {};
  BARRIER();
  gemm12<MH, 3, !VT, !VT, 1024, 6144, 12288, MT * 2048, 4>(acc, aa, aa + 1024, brd, 0, ws, pend, lane);
  BARRIER();
  STAMP(ws, FIRST ? 1 : 4, 2);
  const int nt0 = (col & 7) * 6 + wn * 3;
  if (VT) store_tiles<MH * 3, 16>(rs, obase, lane, [&](int i) { return tiled_blk(wm * 48 + nt0 + i % 3, KK, gA + i / 3); },
                                  [&](int i) { return acc[i / 3][i % 3]; });
  else store_tiles<MH * 3, 16>(rs, obase, lane, [&](int i) { return tiled_blk((FIRST ? gA + wm * RT : wm * MH) + i / 3, KD, nt0 + i % 3); },
                               [&](int i) { return acc[i / 3][i % 3]; });
  STAMP(ws, FIRST ? 1 : 4, 3);
}

__device__ __forceinline__ void small_wg(LDS_AS char* smem, char* __restrict__ ws, const u16* __restrict__ wqkv_img,
                                         const u16* __restrict__ wqkv_txt, const u16* __restrict__ wo_img,
                                         const u16* __restrict__ wo_txt, int bid, int wave8, int lane) {
  const int x = bid & 7, idx = bid >> 3;
  const u32x4 srd_ws = make_srd(ws, WS_END);
  rsrc_t rs = make_rsrc(ws, WS_END);
  LDS_AS unsigned* arr = (LDS_AS unsigned*)(smem + ARR);
  Pending pv{-1, -1, arr + 1};
  if (bid < 240) {
    const int col = (x >> 1) * 4 + (x & 1) * 2 + (idx - 28), sec = col >> 3;
    small_p1<2, false, true>(smem, ws, wqkv_img, col * 192, (int)(sec ? OFF_K : OFF_Q), 63, col, 0, wave8, lane, Pending{-1, -1, arr});
    small_p1<2, true, false>(smem, ws, wqkv_img, 0, (int)(sec ? OFF_VT2 : OFF_VT1), 63, col, 0, wave8, lane,
                             Pending{C_QK + sec * 8 + 7, C_QK + 16 + sec * 8 + 7, arr});
    pv.i0 = C_V + sec * 8 + (col & 7); pv.i1 = pv.i0 + 16;
  } else {
    const int col = bid - 240;
    small_p1<4, false, true>(smem, ws, wqkv_txt, col * 192, (int)(col >> 3 ? OFF_K : OFF_Q), 64, col, 1, wave8, lane, Pending{-1, -1, arr});
    const Pending pq{C_T + (col >> 3), -1, arr};
    if (col < 8) {
      small_p1<4, false, false>(smem, ws, wqkv_txt, 3072 + col * 192, (int)OFF_VX, 64, col, 1, wave8, lane, pq);
      pv.i0 = C_T + 2;
    } else if (wave8 < 4) {
      flush(ws, pq, lane);
    }
  }
  if (wave8 < 4) flush(ws, pv, lane);
  if (bid < 242) {
    const P2Tile t2 = p2_tile(bid);
    if (wave8 == 7) barriers(14);
    else if (wave8 >= 4) {
      poll(srd_ws, p2_counter(t2, lane), 8);
      loader_loop<12, 5, 8>([&](int s, int slot) { p2_issue(smem, rs, t2, wave8 - 4, lane, s, slot); });
      BARRIER();
    } else {
      BARRIER();
      p2_mfma(smem, ws, t2, 0, wave8, lane, arr + 4, Pending{-1, -1, arr});
    }
  } else if (bid < 254) {
    const int e = bid - 242, wave = wave8 & 3;
    if (wave8 >= 4) {
      rsrc_t rb = make_rsrc(e < 6 ? wo_img : wo_txt, D * D * 2);
      const int bv0 = (e % 6) * 256 + wave * 16 * (D * 2) + timg_src(lane, 0, D * 2);
      const int bv1 = (e % 6) * 256 + (wave * 16 + 8) * (D * 2) + timg_src(lane, 1, D * 2);
      poll(srd_ws, C_T + 2, 8);
      loader_loop<12, 5, 6>([&](int s, int slot) {
        LDS_AS char* sb = smem + slot * SLOT;
        dma_sc1<0>(rs, sb + wave * 2048, lane * 16, (int)OFF_VX + (wave * KD + 2 * s) * 1024);
        dma_sc1<1024>(rs, sb + wave * 2048, lane * 16, (int)OFF_VX + (wave * KD + 2 * s) * 1024);
#pragma unroll
        for (int i = 0; i < 4; ++i) dma<0>(rb, sb + 8192 + (wave * 4 + i) * 1024, ((i >> 1) ? bv1 : bv0) + (i & 1) * 128, s * 64 * (D * 2));
      });
    } else {
      const int wm = wave >> 1, wn = wave & 1;
      const unsigned aa = lds_addr(smem) + wm * 4096 + lane * 16;
      unsigned brd[4];
#pragma unroll
      for (int n = 0; n < 4; ++n) brd[n] = lds_addr(smem) + 8192 + timg_rd<2>(lane, wn * 4 + n);
      f32x4 acc[2][4] = {};
      BARRIER();
      gemm12<2, 4, true, false, 0, 8192, SLOT, 0, 4>(acc, aa, aa + 1024, brd, 0, ws, Pending{-1, -1, arr}, lane);
      STAMP(ws, 2, 2);
      store_tiles<8, 16>(rs, (int)(e < 6 ? OFF_VT1 : OFF_VT2), lane,
                         [&](int i) { return tiled_blk(wm * 48 + (e % 6) * 8 + wn * 4 + (i & 3), KK, 64 + (i >> 2)); },
                         [&](int i) { return acc[i >> 2][i & 3]; });
      if (last_wave(arr + 4, 4, lane)) signal(ws, C_X + e);
      STAMP(ws, 2, 3);
    }
  }
}

constexpr int K3_NS = 4, K3_STAGE = 33 * 1024, K3_F = K3_NS * K3_STAGE;
template <int QT>
__device__ __forceinline__ void k3_mfma(LDS_AS char* smem, char* __restrict__ ws, int wn, int qoff, int nb, int lane, u16* __restrict__ outp) {
  const unsigned vrd = lds_addr(smem) + wn * 9216 + lane * 16, prd = lds_addr(smem) + 18432 + qoff * 3072 + lane * 16;
  const unsigned fs = lds_addr(smem) + K3_F + (qoff * 16 + (lane & 15)) * 4;
  auto rdf = [&](float (&f)[QT], int t) {
    rd_32<0>(f[0], fs + t * 320);
    if constexpr (QT > 1) rd_32<64>(f[QT > 1 ? 1 : 0], fs + t * 320);
    if constexpr (QT > 2) rd_32<128>(f[QT > 2 ? 2 : 0], fs + t * 320);
  };
  f32x4 acc[3][QT] = {}, t0[3][QT], t1[3][QT] = {};
  Frags<3, QT> f0, f1, f2;
  unsigned bc[QT];
  bc[0] = prd;
  BARRIER();
  STAMP(ws, 3, 1);
  rd_set<3, QT, false, 3072, 3072, 0, 0>(f0, vrd, bc);
  rd_set<3, QT, false, 3072, 3072, 1024, 1024>(f1, vrd, bc);
  int so = 0;
  auto stage = [&](int t, f32x4 (&cur)[3][QT], f32x4 (&prv)[3][QT]) {
    BARRIER();
    float fp[QT];
    rdf(fp, t);
    kstep<3, QT, false, false, true, 3072, 3072, 2048, 2048, true>(cur, f0, f2, vrd + so, bc, NoExtra{});
    so = wrapn(so + K3_STAGE, K3_NS * K3_STAGE);
    bc[0] = prd + so;
    kstep<3, QT, false, false, false, 3072, 3072, 0, 0, true>(cur, f1, f0, vrd + so, bc, [&](auto ic) {
      constexpr int I = decltype(ic)::value;
      if constexpr (I < QT) tie1(fp[I]);
      acc[I / QT][I % QT] += prv[I / QT][I % QT] * fp[I % QT];
      tie4(acc[I / QT][I % QT]);
    });
    kstep<3, QT, false, false, false, 3072, 3072, 1024, 1024, true>(cur, f2, f1, vrd + so, bc, NoExtra{});
  };
#pragma unroll 1
  for (int t = 0; t < 10; t += 2) {
    stage(t, t0, t1);
    stage(t + 1, t1, t0);
  }
  stage(10, t0, t1);
  float fp[QT];
  rdf(fp, 11);
  land<3, QT, false>(f0);
  land<3, QT, false>(f1);
#pragma unroll
  for (int j = 0; j < QT; ++j) TIE(fp[j]);
  STAMP(ws, 3, 2);
  LDS_AS char* xs = smem + 3 * K3_STAGE;
  const int cw = wn * 48 + (lane >> 4) * 4;
  f32x4 o[QT * 3];
#pragma unroll
  for (int i = 0; i < 3; ++i) {
    const f32x4 gate = *(LDS_AS f32x4*)(xs + 15360 + (cw + i * 16) * 4) + *(LDS_AS f32x4*)(xs + 15360 + 384 + (cw + i * 16) * 4);
#pragma unroll
    for (int j = 0; j < QT; ++j) {
      const u32x2 xv = *(LDS_AS u32x2*)(xs + ((qoff + j) * 16 + (lane & 15)) * 192 + (cw + i * 16) * 2);
      const f32x4 a = acc[i][j] + t0[i][j] * fp[j];
      o[j * 3 + i] = f32x4{fmaf(gate[0], a[0], bflo(xv[0])), fmaf(gate[1], a[1], bfhi(xv[0])),
                           fmaf(gate[2], a[2], bflo(xv[1])), fmaf(gate[3], a[3], bfhi(xv[1]))};
    }
  }
  const bool odd = (lane >> 4) & 1;
  u16* ob = outp + (size_t)(qoff * 16 + (lane & 15)) * D + nb * 96 + wn * 48;
  auto toff = [&](int i) { return (i / 3) * 16 * D + (i % 3) * 16; };
#pragma unroll
  for (int i = 0; i + 1 < QT * 3; i += 2) *(u32x4*)(ob + (odd ? toff(i + 1) : toff(i)) + (lane >> 5) * 8) = pair16(o[i], o[i + 1]);
  if (QT & 1) *(u32x2*)(ob + toff(QT * 3 - 1) + (lane >> 4) * 4) = pack4(o[QT * 3 - 1]);
  STAMP(ws, 3, 3);
}

__device__ __forceinline__ void phase3(LDS_AS char* smem, char* __restrict__ ws, const u16* __restrict__ x_img,
                                       const u16* __restrict__ x_txt, u16* __restrict__ out, int bid, int wave8, int lane) {
  if (bid >= 224) return;
  const int wave = wave8 & 3, x = bid & 7, idx = bid >> 3, b = x & 1;
  int rt0, nrt, nb, txt;
  if (idx < 26) {
    const int u = (x >> 1) * 26 + idx, rb = (u % 52) >> 2;
    nb = (u / 52) * 4 + (u & 3); rt0 = rb * 5; nrt = rb == 12 ? 4 : 5; txt = 0;
  } else {
    nb = (x >> 1) * 2 + idx - 26; rt0 = 64; nrt = 2; txt = 1;
  }
  if (wave8 >= 4) {
    rsrc_t rs = make_rsrc(ws, WS_END);
    const u32x4 srd_ws = make_srd(ws, WS_END);
    auto tail = [&](int slot) {
      const u16* xres = txt ? x_txt + (size_t)b * NTX * D : x_img + (size_t)(b * NI + rt0 * 16) * D;
      rsrc_t rx = make_rsrc(xres, nrt * 16 * D * 2);
      LDS_AS char* sb = smem + slot * K3_STAGE + wave * 4096;
#pragma unroll
      for (int i = 0; i < 4; ++i) {
        const int c = (wave * 4 + i) * 64 + lane;
        if (wave * 4 + i < 15) dma<0>(rx, sb + i * 1024, (c / 12) * (D * 2) + nb * 192 + (c % 12) * 16, 0);
        else dma<0>(rs, sb + i * 1024, lane < 24 * KSPL ? (int)OFF_MOD + ((((lane / 24) * 2 + txt) * 2 + b) * 2304 + 1536 + nb * 96 + (lane % 24) * 4) * 4 : OOB, 0);
      }
    };
    if (wave < 2) {
      poll(srd_ws, C_V + (b * 2 + txt) * 8 + nb, 8);
      const int vb = (int)(txt ? OFF_VT2 : OFF_VT1) + (b * 48 + nb * 6 + wave * 3) * KK * 1024;
      loader_loop<11, K3_NS, 9, 4>([&](int t, int slot) {
        if (t == 10) poll(srd_ws, C_X + txt * 6 + (nb * 96 + (lane & 1) * 95) / 128, 1);
        LDS_AS char* sb = smem + slot * K3_STAGE + wave * 9216;
#pragma unroll
        for (int i = 0; i < 3; ++i) {
          const int so = vb + i * KK * 1024 + t * 3072;
          dma_sc1<0>(rs, sb + i * 3072, lane * 16, so);
          dma_sc1<1024>(rs, sb + i * 3072, lane * 16, so);
          dma_sc1<2048>(rs, sb + i * 3072, lane * 16, so);
        }
      }, NoHook{}, tail);
    } else {
      poll(srd_ws, C_P + b * 11 + (rt0 * 16 + (lane & 1) * (nrt * 16 - 1)) / 96, 11);
      const int ltid = (wave - 2) * 64 + lane;
      u32x4 st[11];
      {
        const int r = min(ltid, nrt * 16 - 1), vs = (int)OFF_ST + (b * RT + rt0 + (r >> 4)) * 2816 + (r & 15) * 16;
#pragma unroll
        for (int i = 0; i < 11; ++i) st[i] = aload_sc1(srd_ws, vs, i * 256);
      }
      const int pb = (int)OFF_P + (b * RT + rt0) * KK * 1024;
      loader_loop<11, K3_NS, 8, 4>([&](int t, int slot) {
#pragma unroll
        for (int i = 0; i < 8; ++i) {
          const int pc = (wave - 2) * 8 + i, u = pc / 3, ks = pc % 3;
          dma_sc1(rs, smem + slot * K3_STAGE + 18432 + min(pc, 14) * 1024, lane * 16,
                  u < nrt ? pb + (u * KK + t * 3 + ks) * 1024 : (pc == 15 ? pb + ((nrt - 1) * KK + t * 3 + 2) * 1024 + (nrt == 5 ? 0 : OOB) : OOB));
        }
      }, [&]() {
        float m[11];
#pragma unroll
        for (int t = 0; t < 11; ++t) { TIE(st[t]); m[t] = __uint_as_float(st[t][0]); }
        float M = m[0];
#pragma unroll
        for (int t = 1; t < 11; ++t) M = fmaxf(M, m[t]);
        float L = 0.f;
#pragma unroll
        for (int t = 0; t < 11; ++t) {
          m[t] = __builtin_amdgcn_exp2f(m[t] - M);
          L = fmaf(m[t], __uint_as_float(st[t][1]) + __uint_as_float(st[t][3]), L);
        }
        const float inv = 1.0f / L;
        LDS_AS float* fw = (LDS_AS float*)(smem + K3_F);
        if (ltid < 80) {
          fw[ltid] = 0.f;
#pragma unroll
          for (int t = 0; t < 11; ++t) fw[(1 + t) * 80 + ltid] = m[t] * inv;
        }
        WAIT_LGKM(0);
      }, tail);
    }
    return;
  }
  u16* outp = txt ? out + (size_t)2 * NI * D + (size_t)b * NTX * D : out + (size_t)(b * NI + rt0 * 16) * D;
  const int wq = wave >> 1, q0 = (nrt + 1) >> 1;
  const int qt = wq ? nrt - q0 : q0, qoff = wq ? q0 : 0;
  if (qt == 3) k3_mfma<3>(smem, ws, wave & 1, qoff, nb, lane, outp);
  else if (qt == 2) k3_mfma<2>(smem, ws, wave & 1, qoff, nb, lane, outp);
  else k3_mfma<1>(smem, ws, wave & 1, qoff, nb, lane, outp);
}

__global__ __launch_bounds__(512, 2) void kmain(const u16* __restrict__ x_img, const u16* __restrict__ x_txt,
                                                const u16* __restrict__ wqkv_img, const u16* __restrict__ wqkv_txt,
                                                const u16* __restrict__ wo_img, const u16* __restrict__ wo_txt,
                                                char* __restrict__ ws, u16* __restrict__ out) {
  __shared__ __attribute__((aligned(1024))) char smem_[MAIN_LDS];
  LDS_AS char* smem = (LDS_AS char*)smem_;
  const int bid = blockIdx.x, tid = threadIdx.x, lane = tid & 63;
  const int wave = __builtin_amdgcn_readfirstlane(tid >> 6);
  if (tid < 8) ((LDS_AS unsigned*)(smem + ARR))[tid] = 0u;
  WAIT_LGKM(0);
  STAMP(ws, 5, 0);
  if (bid < 224) {
    big_wg(smem, ws, wqkv_img, wo_img, wo_txt, bid, wave, lane);
    STAMP(ws, 3, 0);
    phase3(smem, ws, x_img, x_txt, out, bid, wave, lane);
  } else {
    small_wg(smem, ws, wqkv_img, wqkv_txt, wo_img, wo_txt, bid, wave, lane);
  }
}

extern "C" void kernel_launch(void* const* d_in, const int* in_sizes, int n_in, void* d_out, int out_size, void* d_ws,
                              size_t ws_size, hipStream_t stream) {
  const u16* x_img = (const u16*)d_in[0];
  const u16* x_txt = (const u16*)d_in[1];
  const u16* cvec = (const u16*)d_in[2];
  const u16* wqkv_img = (const u16*)d_in[3];
  const u16* wqkv_txt = (const u16*)d_in[4];
  const u16* wo_img = (const u16*)d_in[5];
  const u16* wo_txt = (const u16*)d_in[6];
  const u16* wmod_img = (const u16*)d_in[7];
  const u16* wmod_txt = (const u16*)d_in[8];
  char* ws = (char*)d_ws;
  k0_prep<<<K0_T1 + K0_T2 + K0_Z + K0_PF, 256, 0, stream>>>(x_img, x_txt, cvec, wmod_img, wmod_txt, wqkv_img, wqkv_txt, wo_img, wo_txt, ws);
  kmain<<<256, 512, 0, stream>>>(x_img, x_txt, wqkv_img, wqkv_txt, wo_img, wo_txt, ws, (u16*)d_out);
}
```

```cpp
#include <hip/hip_runtime.h>
#include <hip/hip_bf16.h>
#include <stdint.h>
#include <type_traits>

typedef __attribute__((ext_vector_type(8))) __bf16 bf16x8;
typedef __attribute__((ext_vector_type(4))) __bf16 bf16x4;
typedef __attribute__((ext_vector_type(2))) __bf16 bf16x2;
typedef __attribute__((ext_vector_type(4))) float f32x4;
typedef __attribute__((ext_vector_type(2))) float f32x2;
typedef __attribute__((ext_vector_type(4))) unsigned u32x4;
typedef __attribute__((ext_vector_type(2))) unsigned u32x2;
#define LDS_AS __attribute__((address_space(3)))
typedef __amdgpu_buffer_rsrc_t rsrc_t;
typedef unsigned short u16;

constexpr int D = 768, NI = 1024, NTX = 32, NK = 1056;
constexpr int RT = 66;
constexpr int KD = 24;
constexpr int KK = 33;
constexpr int KSPL = 2;
constexpr int OOB = 0x7f000000;

constexpr unsigned SZ_TOK = 2u * RT * KD * 1024u;
constexpr unsigned SZ_VT = 2u * 48u * KK * 1024u;
constexpr unsigned OFF_XN = 0;
constexpr unsigned OFF_Q = OFF_XN + SZ_TOK;
constexpr unsigned OFF_K = OFF_Q + SZ_TOK;
constexpr unsigned OFF_VX = OFF_K + SZ_TOK;
constexpr unsigned OFF_VT1 = OFF_VX + 4u * KD * 1024u;
constexpr unsigned OFF_VT2 = OFF_VT1 + SZ_VT;
constexpr unsigned OFF_P = OFF_VT2 + SZ_VT;
constexpr unsigned OFF_WVO = OFF_P + 2u * RT * KK * 1024u;
constexpr unsigned OFF_MOD = OFF_WVO + 96u * KD * 1024u;
constexpr unsigned OFF_ST = OFF_MOD + KSPL * 4u * 2304u * 4u;
constexpr unsigned OFF_CNT = OFF_ST + 2u * RT * 11u * 256u;
constexpr unsigned OFF_H = OFF_CNT + 20480u;
constexpr unsigned WS_END = OFF_H + SZ_TOK;
constexpr int C_H = 0;
constexpr int C_QK = 14;
constexpr int C_T = 46;
constexpr int C_V = 49;
constexpr int C_X = 81;
constexpr int C_P = 93;
constexpr int C_W = 115;
constexpr unsigned SZ_CNT = 20480u;

#define WAIT_VM(n) asm volatile("s_waitcnt vmcnt(%0)" ::"n"(n) : "memory")
#define WAIT_LGKM(n) asm volatile("s_waitcnt lgkmcnt(%0)" ::"n"(n) : "memory")
#define BARRIER() __builtin_amdgcn_s_barrier()
#define TIE(x) asm volatile("" : "+v"(x))
#define SCHED_FENCE() __builtin_amdgcn_sched_barrier(0)

#define STAMP(ws, k, i) do { } while (0)
#define TRACE(ws, k, i) do { } while (0)

template <int I, int N, class F>
__device__ __forceinline__ void static_for(F&& f) {
  if constexpr (I < N) { f(std::integral_constant<int, I>{}); static_for<I + 1, N>(f); }
}
__device__ __forceinline__ rsrc_t make_rsrc(const void* p, unsigned bytes) {
  return __builtin_amdgcn_make_buffer_rsrc((void*)p, 0, (int)bytes, 0x00020000);
}
__device__ __forceinline__ u32x4 make_srd(const void* p, unsigned bytes) {
  unsigned long long a = (unsigned long long)p;
  u32x4 r;
  r[0] = (unsigned)a; r[1] = (unsigned)(a >> 32) & 0xffffu; r[2] = bytes; r[3] = 0x00020000u;
  return r;
}
template <int IMM>
__device__ __forceinline__ void dma(rsrc_t rs, LDS_AS char* lds, int voff, int soff) {
  __builtin_amdgcn_raw_ptr_buffer_load_lds(rs, (LDS_AS void*)lds, 16, voff, soff, IMM, 0);
}
template <int IMM = 0>
__device__ __forceinline__ void dma_sc1(rsrc_t rs, LDS_AS char* lds, int voff, int soff) {
  __builtin_amdgcn_raw_ptr_buffer_load_lds(rs, (LDS_AS void*)lds, 16, voff, soff, IMM, 16);
}
__device__ __forceinline__ u32x4 aload(const u32x4& srd, int voff, int soff) {
  u32x4 r;
  asm volatile("buffer_load_dwordx4 %0, %1, %2, %3 offen" : "=v"(r) : "v"(voff), "s"(srd), "s"(soff));
  return r;
}
__device__ __forceinline__ u32x4 aload_sc1(const u32x4& srd, int voff, int soff) {
  u32x4 r;
  asm volatile("buffer_load_dwordx4 %0, %1, %2, %3 offen sc1" : "=v"(r) : "v"(voff), "s"(srd), "s"(soff));
  return r;
}
__device__ __forceinline__ void signal(char* ws, int idx) {
  __hip_atomic_fetch_add((unsigned*)(ws + OFF_CNT + idx * 128), 1u, __ATOMIC_RELAXED, __HIP_MEMORY_SCOPE_AGENT);
}
__device__ __forceinline__ bool last_wave(LDS_AS unsigned* word, unsigned n, int lane) {
  WAIT_VM(0);
  unsigned old = 0;
  if (lane == 0) old = __hip_atomic_fetch_add(word, 1u, __ATOMIC_RELAXED, __HIP_MEMORY_SCOPE_WORKGROUP);
  return lane == 0 && old == n - 1;
}
struct Pending { int i0, i1; LDS_AS unsigned* arr; };
__device__ __forceinline__ void flush(char* ws, const Pending& p, int lane) {
  if (p.i0 >= 0 && last_wave(p.arr, 4, lane)) { signal(ws, p.i0); if (p.i1 >= 0) signal(ws, p.i1); }
}
__device__ __forceinline__ void poll(const u32x4& srd, int idx, unsigned target) {
#pragma unroll 1
  for (int spin = 0; spin < (1 << 20); ++spin) {
    unsigned f;
    asm volatile("buffer_load_dword %0, %1, %2, 0 offen sc1\n\ts_waitcnt vmcnt(0)" : "=v"(f) : "v"((int)OFF_CNT + idx * 128), "s"(srd) : "memory");
    if (__builtin_amdgcn_ballot_w64(f < target) == 0) break;
    __builtin_amdgcn_s_sleep(1);
  }
}
__device__ __forceinline__ unsigned peek(const u32x4& srd, int idx) {
  unsigned f;
  asm volatile("buffer_load_dword %0, %1, %2, 0 offen sc1" : "=v"(f) : "v"((int)OFF_CNT + idx * 128), "s"(srd) : "memory");
  return f;
}
__device__ __forceinline__ void check(unsigned f, const u32x4& srd, int idx, unsigned target) {
  WAIT_VM(0);
  TIE(f);
  if (__builtin_amdgcn_ballot_w64(f < target) != 0) poll(srd, idx, target);
}
__device__ __forceinline__ void barriers(int n) {
#pragma unroll 1
  for (int t = 0; t < n; ++t) BARRIER();
}
template <int OFF>
__device__ __forceinline__ void rd_tr(bf16x4& lo, bf16x4& hi, unsigned addr) {
  asm volatile("ds_read_b64_tr_b16 %0, %2 offset:%c3\n\tds_read_b64_tr_b16 %1, %2 offset:%c4"
               : "=&v"(lo), "=&v"(hi) : "v"(addr), "i"(OFF), "i"(OFF + 512));
}
template <int OFF>
__device__ __forceinline__ void rd_128(bf16x8& r, unsigned addr) {
  asm volatile("ds_read_b128 %0, %1 offset:%c2" : "=v"(r) : "v"(addr), "i"(OFF));
}
template <int OFF>
__device__ __forceinline__ void rd_32(float& r, unsigned addr) {
  asm volatile("ds_read_b32 %0, %1 offset:%c2" : "=v"(r) : "v"(addr), "i"(OFF));
}
__device__ __forceinline__ void tie1(float& x) { TIE(x); }
__device__ __forceinline__ void tie4(f32x4& x) { TIE(x); }
__device__ __forceinline__ void tie8(bf16x8& x) { TIE(x); }
__device__ __forceinline__ void tie4h(bf16x4& x) { TIE(x); }
__device__ __forceinline__ bf16x8 cat(bf16x4 lo, bf16x4 hi) { return __builtin_shufflevector(lo, hi, 0, 1, 2, 3, 4, 5, 6, 7); }
__device__ __forceinline__ unsigned lds_addr(LDS_AS char* p) { return (unsigned)(size_t)p; }
__device__ __forceinline__ int wrapn(int v, int n) { return v >= n ? v - n : v; }
__device__ __forceinline__ f32x4 mfma(bf16x8 a, bf16x8 b, f32x4 c) {
  return __builtin_amdgcn_mfma_f32_16x16x32_bf16(a, b, c, 0, 0, 0);
}
__device__ __forceinline__ unsigned pack2(float a, float b) {
  f32x2 f = {a, b};
  bf16x2 h = __builtin_convertvector(f, bf16x2);
  return __builtin_bit_cast(unsigned, h);
}
__device__ __forceinline__ u32x2 pack4(f32x4 v) { return u32x2{pack2(v[0], v[1]), pack2(v[2], v[3])}; }
__device__ __forceinline__ float bflo(unsigned v) { return __uint_as_float(v << 16); }
__device__ __forceinline__ float bfhi(unsigned v) { return __uint_as_float(v & 0xffff0000u); }
__device__ __forceinline__ float red4_max(float x) {
  auto a = __builtin_amdgcn_permlane16_swap(__float_as_uint(x), __float_as_uint(x), false, false);
  x = fmaxf(__uint_as_float(a[0]), __uint_as_float(a[1]));
  auto b = __builtin_amdgcn_permlane32_swap(__float_as_uint(x), __float_as_uint(x), false, false);
  return fmaxf(__uint_as_float(b[0]), __uint_as_float(b[1]));
}
__device__ __forceinline__ float red4_sum(float x) {
  auto a = __builtin_amdgcn_permlane16_swap(__float_as_uint(x), __float_as_uint(x), false, false);
  x = __uint_as_float(a[0]) + __uint_as_float(a[1]);
  auto b = __builtin_amdgcn_permlane32_swap(__float_as_uint(x), __float_as_uint(x), false, false);
  return __uint_as_float(b[0]) + __uint_as_float(b[1]);
}
__device__ __forceinline__ unsigned tiled_lane(int lane) {
  int g = lane >> 4;
  return (unsigned)(((lane & 15) + 16 * (g >> 1)) * 16 + (g & 1) * 8);
}
__device__ __forceinline__ unsigned tiled_blk(int rowtile, int KS, int T16) {
  return (unsigned)((rowtile * KS + (T16 >> 1)) * 1024 + (T16 & 1) * 512);
}
__device__ __forceinline__ u32x4 pair16(f32x4 x, f32x4 y) {
  const u32x2 px = pack4(x), py = pack4(y);
  auto a = __builtin_amdgcn_permlane16_swap(px[0], py[0], false, false);
  auto b = __builtin_amdgcn_permlane16_swap(px[1], py[1], false, false);
  return u32x4{a[0], b[0], a[1], b[1]};
}
__device__ __forceinline__ unsigned tiled_lane16(int lane) { return (unsigned)(((lane & 15) + 16 * (lane >> 5)) * 16); }
template <int NTILE, int AUX, class Blk, class Val>
__device__ __forceinline__ void store_tiles(rsrc_t rs, int base, int lane, Blk&& blk, Val&& val) {
  const bool odd = (lane >> 4) & 1;
  const int l16 = (int)tiled_lane16(lane);
#pragma unroll
  for (int i = 0; i + 1 < NTILE; i += 2)
    __builtin_amdgcn_raw_buffer_store_b128(pair16(val(i), val(i + 1)), rs, l16 + (odd ? (int)(blk(i + 1) - blk(i)) + 0x100000 : 0x100000),
                                           base - 0x100000 + (int)blk(i), AUX);
  if (NTILE & 1) __builtin_amdgcn_raw_buffer_store_b64(pack4(val(NTILE - 1)), rs, (int)tiled_lane(lane), base + (int)blk(NTILE - 1), AUX);
}
__device__ __forceinline__ int timg_src(int lane, int sigma, int pitch) {
  int r = lane >> 3, c = (lane & 7) ^ (2 * ((r >> 1) & 1) + 4 * sigma);
  return r * pitch + c * 16;
}
template <int NB>
__device__ __forceinline__ int timg_rd(int lane, int nt) {
  int g = lane >> 4, q = (lane & 15) >> 2, p = lane & 3;
  int f = 2 * ((q >> 1) & 1) + 4 * (g & 1);
  return (g * NB + (nt >> 2)) * 1024 + q * 128 + (((2 * (nt & 3)) ^ f) + (p >> 1)) * 16 + (p & 1) * 8;
}

template <int MT, int NT>
struct Frags { bf16x8 a[MT]; bf16x8 b[NT]; bf16x4 l[NT], h[NT]; };

template <int MT, int NT, bool BTR, int AM, int BM, int AO, int BO, int I>
__device__ __forceinline__ void rd_item(Frags<MT, NT>& n, unsigned ab, const unsigned (&bb)[NT]) {
  if constexpr (I < NT) {
    if constexpr (BTR) rd_tr<BO>(n.l[I], n.h[I], bb[I]);
    else rd_128<BO + I * BM>(n.b[I], bb[0]);
  } else if constexpr (I < NT + MT) {
    rd_128<AO + (I - NT) * AM>(n.a[I - NT], ab);
  }
}
template <int MT, int NT, bool BTR, int AM, int BM, int AO, int BO>
__device__ __forceinline__ void rd_set(Frags<MT, NT>& n, unsigned ab, const unsigned (&bb)[NT]) {
  static_for<0, MT + NT>([&](auto ic) { rd_item<MT, NT, BTR, AM, BM, AO, BO, decltype(ic)::value>(n, ab, bb); });
}
template <int MT, int NT, bool BTR, bool AH = false>
__device__ __forceinline__ void land(Frags<MT, NT>& c) {
  WAIT_LGKM(AH ? MT + (BTR ? 2 : 1) * NT : 0);
#pragma unroll
  for (int j = 0; j < NT; ++j) {
    if constexpr (BTR) { TIE(c.l[j]); TIE(c.h[j]); } else TIE(c.b[j]);
  }
#pragma unroll
  for (int m = 0; m < MT; ++m) TIE(c.a[m]);
  SCHED_FENCE();
}
template <int MT, int NT, bool BTR, bool SWAP, bool ZC, int AM, int BM, int AO, int BO, bool AH = false, class X>
__device__ __forceinline__ void kstep(f32x4 (&acc)[MT][NT], Frags<MT, NT>& c, Frags<MT, NT>& n, unsigned ab,
                                      const unsigned (&bb)[NT], X&& extra) {
  land<MT, NT, BTR, AH>(c);
  static_for<0, MT * NT>([&](auto ic) {
    constexpr int I = decltype(ic)::value, m = I / NT, j = I % NT;
    bf16x8 bf;
    if constexpr (BTR) bf = cat(c.l[j], c.h[j]); else bf = c.b[j];
    const f32x4 ci = ZC ? f32x4{0.f, 0.f, 0.f, 0.f} : acc[m][j];
    acc[m][j] = SWAP ? mfma(bf, c.a[m], ci) : mfma(c.a[m], bf, ci);
    tie4(acc[m][j]);
    rd_item<MT, NT, BTR, AM, BM, AO, BO, I>(n, ab, bb);
    extra(ic);
    SCHED_FENCE();
  });
  static_for<MT * NT, (MT + NT > MT * NT ? MT + NT : MT * NT)>(
      [&](auto ic) { rd_item<MT, NT, BTR, AM, BM, AO, BO, decltype(ic)::value>(n, ab, bb); });
}
struct NoExtra { template <class T> __device__ __forceinline__ void operator()(T) const {} };

struct NoHook { __device__ __forceinline__ void operator()() const {} };
struct NoTail { __device__ __forceinline__ void operator()(int) const {} };
template <int NST, int NS, int V, int TV = 0, bool XB = false, class Issue, class Hook = NoHook, class Tail = NoTail>
__device__ __forceinline__ void loader_loop(Issue&& issue, Hook&& hook = NoHook{}, Tail&& tail = NoTail{}) {
#pragma unroll
  for (int s = 0; s < NS - 1; ++s) issue(s, s);
  if (XB) BARRIER();
  WAIT_VM(V * (NS - 2));
  BARRIER();
  hook();
  int slot = NS - 1;
#pragma unroll 1
  for (int t = 0; t <= NST - NS; ++t) {
    WAIT_VM(V * (NS - 3));
    BARRIER();
    issue(t + NS - 1, slot);
    slot = wrapn(slot + 1, NS);
  }
  static_for<1, NS>([&](auto rc) {
    constexpr int R = decltype(rc)::value;
    WAIT_VM(R == NS - 1 ? 0 : V * (NS - 2 - R) + (R > 1 ? TV : 0));
    BARRIER();
    if constexpr (R == 1) tail(slot);
  });
}

template <int VP, int V, class Issue>
__device__ __forceinline__ void stream12(int& slot, Issue&& issue) {
  WAIT_VM(2 * VP); BARRIER(); issue(0, slot); slot = wrapn(slot + 1, 5);
  WAIT_VM(VP + V); BARRIER(); issue(1, slot); slot = wrapn(slot + 1, 5);
#pragma unroll 1
  for (int t = 2; t < 12; ++t) {
    WAIT_VM(2 * V); BARRIER(); issue(t, slot); slot = wrapn(slot + 1, 5);
  }
}

template <int MW, int NW, bool BTR, bool SWAP, int BM, int BKS, int SLOT, int ASTEP, int FT>
__device__ __forceinline__ void gemm12(f32x4 (&acc)[MW][NW], unsigned a0, unsigned a1, const unsigned (&brd)[NW], int so,
                                       char* __restrict__ ws, const Pending& pend, int lane) {
  Frags<MW, NW> x0, x1, y0, y1;
  unsigned bc[NW];
#pragma unroll
  for (int n = 0; n < NW; ++n) bc[n] = brd[n] + so;
  rd_set<MW, NW, BTR, 2048, BM, 0, 0>(x0, ASTEP ? a0 : a0 + so, bc);
  rd_set<MW, NW, BTR, 2048, BM, 0, BKS>(x1, ASTEP ? a1 : a1 + so, bc);
  auto stage = [&](Frags<MW, NW>& c0, Frags<MW, NW>& c1, Frags<MW, NW>& n0, Frags<MW, NW>& n1) {
    so = wrapn(so + SLOT, 5 * SLOT);
    a0 += ASTEP; a1 += ASTEP;
#pragma unroll
    for (int n = 0; n < NW; ++n) bc[n] = brd[n] + so;
    BARRIER();
    kstep<MW, NW, BTR, SWAP, false, 2048, BM, 0, 0, true>(acc, c0, n0, ASTEP ? a0 : a0 + so, bc, NoExtra{});
    kstep<MW, NW, BTR, SWAP, false, 2048, BM, 0, BKS, true>(acc, c1, n1, ASTEP ? a1 : a1 + so, bc, NoExtra{});
  };
#pragma unroll 1
  for (int t = 0; t < 12; t += 2) {
    if (t == FT) flush(ws, pend, lane);
    stage(x0, x1, y0, y1);
    stage(y0, y1, x0, x1);
  }
  land<MW, NW, BTR>(x0);
  land<MW, NW, BTR>(x1);
}
__device__ __forceinline__ int t96_src(int pc, int lane, int pitch, int odd) {
  const int ks = pc / 6, r = pc % 6, g1 = (lane >> 4) & 1, r16 = 8 * g1 + 4 * (lane >> 5) + ((lane >> 2) & 3);
  return r < 4 ? (ks * 32 + r * 8) * pitch + odd * 64 + timg_src(lane, r & 1, pitch)
               : (ks * 32 + (r - 4) * 16 + r16) * pitch + (odd ? 0 : 128) + (((lane & 3) ^ (2 * g1)) * 16);
}
__device__ __forceinline__ int t96_rd(int lane, int nt, int odd) {
  const int g = lane >> 4, q = (lane & 15) >> 2, p = lane & 3;
  const bool narrow = odd ? nt < 2 : nt >= 4;
  return narrow ? 4096 + (g >> 1) * 1024 + (g & 1) * 256 + q * 64 + (((2 * (nt & 1) + (p >> 1)) ^ (2 * (g & 1))) * 16) + (p & 1) * 8
                : timg_rd<1>(lane, odd ? nt - 2 : nt);
}

#define K0_PFN 144
constexpr int K0_T1 = 72 * KSPL, K0_T2 = 2 * RT, K0_Z = 8, K0_PF = K0_PFN;
__global__ __launch_bounds__(256) void k0_prep(const u16* __restrict__ x_img, const u16* __restrict__ x_txt,
                                               const u16* __restrict__ cvec, const u16* __restrict__ wmod_img,
                                               const u16* __restrict__ wmod_txt, const u16* __restrict__ wqkv_img,
                                               const u16* __restrict__ wqkv_txt, const u16* __restrict__ wo_img,
                                               const u16* __restrict__ wo_txt, char* __restrict__ ws) {
  __shared__ __attribute__((aligned(1024))) char smem_[24576];
  LDS_AS char* smem = (LDS_AS char*)smem_;
  const int bid = blockIdx.x, tid = threadIdx.x, lane = tid & 63;
  const int wave = __builtin_amdgcn_readfirstlane(tid >> 6);
  STAMP(ws, 0, 0);
  if (bid >= K0_T1 + K0_T2 + K0_Z) {
    const int i = bid - (K0_T1 + K0_T2 + K0_Z);
    const char* p = i < 18 ? (const char*)wo_img + i * 65536 : (i < 36 ? (const char*)wo_txt + (i - 18) * 65536
                  : (i < 90 ? (const char*)wqkv_img + (i - 36) * 65536 : (const char*)wqkv_txt + (i - 90) * 65536));
    u32x4 v[16];
#pragma unroll
    for (int k = 0; k < 16; ++k) v[k] = *(const u32x4*)(p + k * 4096 + tid * 16);
#pragma unroll
    for (int k = 0; k < 16; ++k) asm volatile("" ::"v"(v[k]));
    return;
  } else if (bid >= K0_T1 + K0_T2) {
    for (unsigned o = ((bid - K0_T1 - K0_T2) * 256 + tid) * 16; o < SZ_CNT; o += K0_Z * 4096) *(u32x4*)(ws + OFF_CNT + o) = u32x4{0u, 0u, 0u, 0u};
    unsigned long long pc;
    int delta, len;
    asm volatile("s_getpc_b64 %0\n.Lmmdit_k0_pc:\n\ts_mov_b32 %1, .Lmmdit_main_begin-.Lmmdit_k0_pc\n\ts_mov_b32 %2, .Lmmdit_main_end-.Lmmdit_main_begin"
                 : "=s"(pc), "=s"(delta), "=s"(len));
    pc += delta;
    unsigned acc = 0;
    for (int o = tid * 128; o < len; o += 256 * 128) acc ^= *(const unsigned*)((const char*)pc + o);
    asm volatile("" ::"v"(acc));
    return;
  } else if (bid < K0_T1) {
    constexpr int ROWS = D / KSPL, PER = ROWS / 32;
    const int i = bid, s = i / (36 * KSPL), rem = i % (36 * KSPL), cb = rem % 36, kh = rem / 36;
    const u16* w = (s ? wmod_txt : wmod_img) + (size_t)(kh * ROWS + (tid >> 3)) * (6 * D) + cb * 64 + (tid & 7) * 8;
    u16 cv[3];
#pragma unroll
    for (int e = 0; e < 3; ++e) { const int z = tid + e * 256; cv[e] = cvec[(z / ROWS) * D + kh * ROWS + z % ROWS]; }
    u32x4 wv[PER];
#pragma unroll
    for (int r = 0; r < PER; ++r) wv[r] = *(const u32x4*)(w + (size_t)r * 32 * (6 * D));
    LDS_AS float* sc = (LDS_AS float*)smem;
    LDS_AS float* red = (LDS_AS float*)(smem + 8192);
#pragma unroll
    for (int e = 0; e < 3; ++e) {
      const int z = tid + e * 256;
      const float v = __uint_as_float((unsigned)cv[e] << 16);
      sc[(z % ROWS) * 2 + z / ROWS] = v / (1.0f + __expf(-v));
    }
    __syncthreads();
    float a0[8] = {}, a1[8] = {};
#pragma unroll
    for (int r = 0; r < PER; ++r) {
      f32x2 sv = *(LDS_AS f32x2*)(sc + ((tid >> 3) + r * 32) * 2);
#pragma unroll
      for (int j = 0; j < 4; ++j) {
        float lo = bflo(wv[r][j]), hi = bfhi(wv[r][j]);
        a0[2 * j] = fmaf(sv[0], lo, a0[2 * j]); a0[2 * j + 1] = fmaf(sv[0], hi, a0[2 * j + 1]);
        a1[2 * j] = fmaf(sv[1], lo, a1[2 * j]); a1[2 * j + 1] = fmaf(sv[1], hi, a1[2 * j + 1]);
      }
    }
    LDS_AS float* rp = red + (tid >> 3) * 128 + (tid & 7) * 8;
    *(LDS_AS f32x4*)(rp) = f32x4{a0[0], a0[1], a0[2], a0[3]};
    *(LDS_AS f32x4*)(rp + 4) = f32x4{a0[4], a0[5], a0[6], a0[7]};
    *(LDS_AS f32x4*)(rp + 64) = f32x4{a1[0], a1[1], a1[2], a1[3]};
    *(LDS_AS f32x4*)(rp + 68) = f32x4{a1[4], a1[5], a1[6], a1[7]};
    __syncthreads();
    {
      const int o = tid >> 1, hf = tid & 1;
      float t0 = 0.f, t1 = 0.f;
#pragma unroll
      for (int g = 0; g < 16; g += 2) { t0 += red[(hf * 16 + g) * 128 + o]; t1 += red[(hf * 16 + g + 1) * 128 + o]; }
      float t = t0 + t1;
      t += __shfl_xor(t, 1);
      float* mod = (float*)(ws + OFF_MOD);
      if (hf == 0) mod[((kh * 2 + s) * 2 + (o >> 6)) * 2304 + cb * 64 + (o & 63)] = t;
    }
  } else {
    const int i = bid - K0_T1, b = i / RT, rt = i % RT;
    const u16* src = rt < 64 ? x_img + (size_t)(b * NI + rt * 16) * D : x_txt + (size_t)(b * NTX + (rt - 64) * 16) * D;
    src += (lane & 15) * D + wave * 192 + (lane >> 4) * 8;
    u32x4 v[6];
#pragma unroll
    for (int k = 0; k < 6; ++k) v[k] = *(const u32x4*)(src + k * 32);
    LDS_AS float* red = (LDS_AS float*)smem;
    float s0 = 0.f, s1 = 0.f;
#pragma unroll
    for (int k = 0; k < 6; ++k)
#pragma unroll
      for (int j = 0; j < 4; ++j) { s0 += bflo(v[k][j]); s1 += bfhi(v[k][j]); }
    const float s = red4_sum(s0 + s1);
    if (lane < 16) red[wave * 16 + lane] = s;
    __syncthreads();
    const int r = lane & 15;
    const float mean = ((red[r] + red[16 + r]) + (red[32 + r] + red[48 + r])) * (1.0f / D);
    float q0 = 0.f, q1 = 0.f;
#pragma unroll
    for (int k = 0; k < 6; ++k)
#pragma unroll
      for (int j = 0; j < 4; ++j) {
        float lo = bflo(v[k][j]) - mean, hi = bfhi(v[k][j]) - mean;
        q0 = fmaf(lo, lo, q0); q1 = fmaf(hi, hi, q1);
      }
    const float q = red4_sum(q0 + q1);
    if (lane < 16) red[64 + wave * 16 + lane] = q;
    __syncthreads();
    const float var = ((red[64 + r] + red[80 + r]) + (red[96 + r] + red[112 + r])) * (1.0f / D);
    const float rstd = 1.0f / sqrtf(var + 1e-5f);
    char* dst = ws + OFF_XN + (size_t)(i * KD + wave * 6) * 1024 + lane * 16;
#pragma unroll
    for (int k = 0; k < 6; ++k) {
      u32x4 o;
#pragma unroll
      for (int j = 0; j < 4; ++j) {
        typedef __attribute__((ext_vector_type(2))) _Float16 f16x2;
        const f16x2 h = {(_Float16)((bflo(v[k][j]) - mean) * rstd), (_Float16)((bfhi(v[k][j]) - mean) * rstd)};
        o[j] = __builtin_bit_cast(unsigned, h);
      }
      *(u32x4*)(dst + k * 1024) = o;
    }
  }
  STAMP(ws, 0, 3);
}

constexpr int SLOT = 30 * 1024;
constexpr int P1_SB = 98304, P1_SGS = P1_SB + 4 * 12288;
constexpr int RED = P1_SB + 5 * 12288, ARR = RED + 1024, MAIN_LDS = ARR + 32;
constexpr int WP = 3 * D * 2;

__device__ __forceinline__ void fmix_lo(float& d, unsigned v, float g, float s) {
  asm volatile("v_fma_mix_f32 %0, %1, %2, %3 op_sel_hi:[1,0,0]" : "=v"(d) : "v"(v), "v"(g), "v"(s));
}
__device__ __forceinline__ void fmix_hi(float& d, unsigned v, float g, float s) {
  asm volatile("v_fma_mix_f32 %0, %1, %2, %3 op_sel:[1,0,0] op_sel_hi:[1,0,0]" : "=v"(d) : "v"(v), "v"(g), "v"(s));
}
template <int OFF>
__device__ __forceinline__ void rd_f4(f32x4& r, unsigned addr) {
  asm volatile("ds_read_b128 %0, %1 offset:%c2" : "=v"(r) : "v"(addr), "i"(OFF));
}
__device__ __forceinline__ u32x4 modulate(u32x4 x, const f32x4 (&g)[4]) {
  float t[8];
#pragma unroll
  for (int d = 0; d < 4; ++d) {
    fmix_lo(t[2 * d], x[d], g[2 + (d >> 1)][(d & 1) * 2], g[d >> 1][(d & 1) * 2]);
    fmix_hi(t[2 * d + 1], x[d], g[2 + (d >> 1)][(d & 1) * 2 + 1], g[d >> 1][(d & 1) * 2 + 1]);
  }
  return u32x4{pack2(t[0], t[1]), pack2(t[2], t[3]), pack2(t[4], t[5]), pack2(t[6], t[7])};
}

struct P2Tile { int b, ti, tj; };
__device__ __forceinline__ P2Tile p2_tile(int bid) {
  const int x = bid & 7, idx = bid >> 3, qd = x >> 1;
  P2Tile t;
  t.b = x & 1;
  if (idx == 30) { t.ti = 5; t.tj = 5; }
  else if (qd == 0) { t.ti = idx / 6; t.tj = idx % 6; }
  else if (qd == 1) { t.ti = idx / 5; t.tj = 6 + idx % 5; }
  else if (qd == 2) { t.ti = 6 + idx / 6; t.tj = 5 + idx % 6; }
  else { t.ti = 5 + idx / 5; t.tj = idx % 5; }
  return t;
}
__device__ __forceinline__ int p2_counter(const P2Tile& t, int lane) {
  const int u = min(lane, 11), sec = u < 6 ? 1 : 0, rt = (u < 6 ? t.tj * 6 + u : t.ti * 6 + u - 6);
  return rt < 64 ? C_QK + (t.b * 2 + sec) * 8 + rt / 9 : C_T + sec;
}
__device__ __forceinline__ void p2_issue(LDS_AS char* smem, rsrc_t rs, const P2Tile& t, int l, int lane, int s, int slot) {
#pragma unroll
  for (int i = 0; i < 4; ++i) {
    const int u = l * 4 + i;
    const int so = (u < 6 ? (int)OFF_K + (t.b * RT + t.tj * 6 + u) * KD * 1024 : (int)OFF_Q + (t.b * RT + t.ti * 6 + u - 6) * KD * 1024) + s * 2048;
    dma_sc1<0>(rs, smem + slot * SLOT + u * 2048, lane * 16, so);
    dma_sc1<1024>(rs, smem + slot * SLOT + u * 2048, lane * 16, so);
  }
}
__device__ __forceinline__ void p2_mfma(LDS_AS char* smem, char* __restrict__ ws, const P2Tile& t, int so, int wave, int lane,
                                        LDS_AS unsigned* arr, const Pending& pend) {
  rsrc_t rs = make_rsrc(ws, WS_END);
  const int wk = wave >> 1, wq = wave & 1, b = t.b, ti = t.ti, tj = t.tj;
  const unsigned krd = lds_addr(smem) + wk * 6144 + lane * 16;
  const unsigned qrd[3] = {lds_addr(smem) + 12288 + wq * 6144 + lane * 16, 0, 0};
  f32x4 acc[3][3] = {};
  gemm12<3, 3, false, false, 2048, 1024, SLOT, 0, 4>(acc, krd, krd + 1024, qrd, so, ws, pend, lane);
  STAMP(ws, 2, 2);
  constexpr float SC = 0.036084391824351615f * 1.4426950408889634f;
  LDS_AS float* red = (LDS_AS float*)(smem + RED);
  const int g = lane >> 4;
  float mx[3], ls[3];
#pragma unroll
  for (int j = 0; j < 3; ++j) {
    float m0 = fmaxf(fmaxf(acc[0][j][0], acc[0][j][1]), fmaxf(acc[0][j][2], acc[0][j][3]));
    float m1 = fmaxf(fmaxf(acc[1][j][0], acc[1][j][1]), fmaxf(acc[1][j][2], acc[1][j][3]));
    float m2 = fmaxf(fmaxf(acc[2][j][0], acc[2][j][1]), fmaxf(acc[2][j][2], acc[2][j][3]));
    mx[j] = red4_max(fmaxf(fmaxf(m0, m1), m2));
  }
  if (lane < 48) red[wk * 96 + wq * 48 + lane] = g == 0 ? mx[0] : (g == 1 ? mx[1] : mx[2]);
  WAIT_LGKM(0);
  BARRIER();
#pragma unroll
  for (int j = 0; j < 3; ++j) mx[j] = fmaxf(mx[j], red[(wk ^ 1) * 96 + wq * 48 + j * 16 + (lane & 15)]) * SC;
#pragma unroll
  for (int j = 0; j < 3; ++j) {
    float l = 0.f;
#pragma unroll
    for (int i = 0; i < 3; ++i) {
#pragma unroll
      for (int r = 0; r < 4; ++r) acc[i][j][r] = __builtin_amdgcn_exp2f(fmaf(acc[i][j][r], SC, -mx[j]));
      l += (acc[i][j][0] + acc[i][j][1]) + (acc[i][j][2] + acc[i][j][3]);
    }
    ls[j] = l;
  }
  store_tiles<9, 16>(rs, (int)OFF_P, lane, [&](int i) { return tiled_blk(b * RT + ti * 6 + wq * 3 + i / 3, KK, tj * 6 + wk * 3 + i % 3); },
                     [&](int i) { return acc[i % 3][i / 3]; });
#pragma unroll
  for (int j = 0; j < 3; ++j) ls[j] = red4_sum(ls[j]);
  if (lane < 48) {
    const u32x2 v = {__float_as_uint(g == 0 ? mx[0] : (g == 1 ? mx[1] : mx[2])), __float_as_uint(g == 0 ? ls[0] : (g == 1 ? ls[1] : ls[2]))};
    __builtin_amdgcn_raw_buffer_store_b64(v, rs, g * 2816 + (lane & 15) * 16, (int)OFF_ST + ((b * RT + ti * 6 + wq * 3) * 11 + tj) * 256 + wk * 8, 16);
  }
  if (last_wave(arr, 4, lane)) signal(ws, C_P + b * 11 + ti);
  STAMP(ws, 2, 3);
}

template <int MW>
__device__ __forceinline__ void big_p1(LDS_AS char* smem, char* __restrict__ ws, int gA, int g, int col, int m0, int wn, int lane,
                                       LDS_AS unsigned* arr, const Pending& pw, Pending& pv) {
  rsrc_t rs = make_rsrc(ws, WS_END);
  const unsigned l0 = lds_addr(smem), aa = l0 + m0 * 2048 + lane * 16;
  const int b = gA >= RT ? 1 : 0, sec = col >> 3, nt0 = (col & 7) * 6 + wn * 3;
  {
    unsigned brd[3];
#pragma unroll
    for (int n = 0; n < 3; ++n) brd[n] = l0 + 18432 + t96_rd(lane, wn * 3 + n, col & 1);
    f32x4 acc[MW][3] = {};
    gemm12<MW, 3, true, true, 0, 6144, SLOT, 0, 4>(acc, aa, aa + 1024, brd, 2 * SLOT, ws, pw, lane);
    STAMP(ws, 1, 2);
    store_tiles<MW * 3, 16>(rs, (int)(sec ? OFF_K : OFF_Q), lane, [&](int i) { return tiled_blk(gA + m0 + i / 3, KD, nt0 + i % 3); },
                            [&](int i) { return acc[i / 3][i % 3]; });
    STAMP(ws, 1, 3);
  }
  {
    const unsigned brd[3] = {l0 + 18432 + wn * 3072 + lane * 16, 0, 0};
    f32x4 acc[MW][3] = {};
    gemm12<MW, 3, false, false, 1024, 6144, SLOT, 0, 4>(acc, aa, aa + 1024, brd, 4 * SLOT, ws,
                                                         Pending{C_QK + (b * 2 + sec) * 8 + g, -1, arr + 2}, lane);
    STAMP(ws, 4, 2);
    store_tiles<MW * 3, 16>(rs, (int)(sec ? OFF_VT2 : OFF_VT1), lane,
                            [&](int i) { return tiled_blk(b * 48 + nt0 + i % 3, KK, gA - b * RT + m0 + i / 3); },
                            [&](int i) { return acc[i / 3][i % 3]; });
    pv = Pending{C_V + (b * 2 + sec) * 8 + (col & 7), -1, arr + 3};
    STAMP(ws, 4, 3);
  }
}

__device__ __forceinline__ void big_wg(LDS_AS char* smem, char* __restrict__ ws, const u16* __restrict__ wqkv_img,
                                       const u16* __restrict__ wo_img, const u16* __restrict__ wo_txt, int bid, int wave8, int lane) {
  const int x = bid & 7, idx = bid >> 3, b = x & 1, g = idx % 7, col = (x >> 1) * 4 + idx / 7;
  const int gA = b * RT + g * 9, grp = b * 7 + g, hb = (int)OFF_H + gA * KD * 1024;
  const int wi = idx < 24 ? idx : idx - 4, wmb = b * 6 + wi % 6, wcol = (x >> 1) * 4 + wi / 6;
  const P2Tile t2 = p2_tile(bid);
  const u32x4 srd_ws = make_srd(ws, WS_END);
  rsrc_t rs = make_rsrc(ws, WS_END);
  LDS_AS unsigned* arr = (LDS_AS unsigned*)(smem + ARR);
  if (wave8 == 7) {
    const int c2 = p2_counter(t2, lane);
    barriers(7);
    unsigned f = peek(srd_ws, C_H + grp);
    barriers(2);
    check(f, srd_ws, C_H + grp, 12);
    barriers(10);
    f = peek(srd_ws, C_W + col);
    barriers(2);
    check(f, srd_ws, C_W + col, 12);
    barriers(10);
    f = peek(srd_ws, c2);
    barriers(2);
    check(f, srd_ws, c2, 8);
    barriers(17);
    return;
  }
  if (wave8 >= 4) {
    const int l = wave8 - 4;
    rsrc_t rw = make_rsrc(wqkv_img, D * 3 * D * 2);
    rsrc_t ro = make_rsrc(wcol < 8 ? wo_img : wo_txt, D * D * 2);
    int bo[4], bw[4], av[3];
#pragma unroll
    for (int i = 0; i < 4; ++i) {
      bo[i] = (wcol & 7) * 192 + t96_src(l * 4 + i, lane, D * 2, wcol & 1);
      bw[i] = col * 192 + t96_src(l * 4 + i, lane, WP, col & 1);
    }
#pragma unroll
    for (int i = 0; i < 3; ++i) {
      const int u = min(l * 3 + i, 7), r = u * 8 + (lane >> 3);
      av[i] = (wmb * 64 + r) * WP + 2 * D * 2 + (((lane & 7) ^ ((r >> 1) & 7)) * 16);
    }
    auto issueW = [&](int s, int slot) {
#pragma unroll
      for (int i = 0; i < 3; ++i) dma<0>(rw, smem + slot * SLOT + min(l * 3 + i, 7) * 1024, av[i], s * 128);
#pragma unroll
      for (int i = 0; i < 4; ++i) dma<0>(ro, smem + slot * SLOT + 18432 + (l * 4 + i) * 1024, bo[i], s * 64 * (D * 2));
    };
    auto issueA = [&](int s, int slot) {
#pragma unroll
      for (int i = 0; i < 3; ++i) {
        LDS_AS char* sa = smem + slot * SLOT + (l * 3 + i) * 2048;
        dma_sc1<0>(rs, sa, lane * 16, hb + ((l * 3 + i) * KD + 2 * s) * 1024);
        dma_sc1<1024>(rs, sa, lane * 16, hb + ((l * 3 + i) * KD + 2 * s) * 1024);
      }
    };
#pragma unroll
    for (int s = 0; s < 4; ++s) issueW(s, s);
    WAIT_VM(21); BARRIER();
    int slot = 4;
#pragma unroll 1
    for (int s = 4; s < 12; ++s) {
      WAIT_VM(14); BARRIER(); issueW(s, slot); slot = wrapn(slot + 1, 5);
    }
    stream12<7, 10>(slot, [&](int s, int sl) {
#pragma unroll
      for (int i = 0; i < 4; ++i) dma<0>(rw, smem + sl * SLOT + 18432 + (l * 4 + i) * 1024, bw[i], s * 64 * WP);
      issueA(s, sl);
    });
    stream12<10, 10>(slot, [&](int s, int sl) {
#pragma unroll
      for (int i = 0; i < 4; ++i) {
        const int pc = l * 4 + i;
        dma_sc1(rs, smem + sl * SLOT + 18432 + pc * 1024, lane * 16, (int)OFF_WVO + ((col * 6 + pc % 6) * KD + 2 * s + pc / 6) * 1024);
      }
      issueA(s, sl);
    });
    stream12<10, 8>(slot, [&](int s, int sl) { p2_issue(smem, rs, t2, l, lane, s, sl); });
    WAIT_VM(16); BARRIER();
    WAIT_VM(8); BARRIER();
    WAIT_VM(0); BARRIER();
    BARRIER();
    BARRIER();
    return;
  }
  const int c = wave8, wm = c >> 1, wn = c & 1;
  Pending ph{-1, -1, arr};
  if (col < 12) {
    const int W = col * 4 + c, ks = W >> 1, t0 = (W & 1) * 5;
    u32x4 xr[5], mp[4][KSPL];
#pragma unroll
    for (int i = 0; i < 4; ++i)
#pragma unroll
      for (int k = 0; k < KSPL; ++k)
        mp[i][k] = aload(srd_ws, (lane >> 4) * 32, (int)OFF_MOD + ((k * 4 + b) * 2304 + (i >> 1) * 768 + ks * 32 + (i & 1) * 4) * 4);
#pragma unroll
    for (int i = 0; i < 5; ++i) xr[i] = aload(srd_ws, lane * 16, (int)OFF_XN + ((gA + min(t0 + i, 8)) * KD + ks) * 1024);
    WAIT_VM(0);
    f32x4 gs[4];
#pragma unroll
    for (int i = 0; i < 4; ++i) {
      TIE(mp[i][0]); TIE(mp[i][1]);
      gs[i] = __builtin_bit_cast(f32x4, mp[i][0]) + __builtin_bit_cast(f32x4, mp[i][1]);
      if (i >= 2) gs[i] += 1.0f;
    }
#pragma unroll
    for (int i = 0; i < 5; ++i) {
      TIE(xr[i]);
      __builtin_amdgcn_raw_buffer_store_b128(modulate(xr[i], gs), rs, lane * 16, hb + (min(t0 + i, 8) * KD + ks) * 1024, 16);
    }
    ph.i0 = C_H + grp;
  }
  Pending pw{-1, -1, arr + 1};
  {
    const int sw = (lane & 15) >> 1, gq = lane >> 4;
    const unsigned ab = lds_addr(smem) + wm * 4096 + (lane & 15) * 128;
    unsigned brd[3];
#pragma unroll
    for (int n = 0; n < 3; ++n) brd[n] = lds_addr(smem) + 18432 + t96_rd(lane, wn * 3 + n, wcol & 1);
    f32x4 acc[2][3] = {};
    BARRIER();
    STAMP(ws, 5, 1);
    gemm12<2, 3, true, false, 0, 6144, SLOT, 0, 2>(acc, ab + ((gq ^ sw) * 16), ab + (((4 + gq) ^ sw) * 16), brd, 0, ws, ph, lane);
    STAMP(ws, 5, 2);
    if (idx < 24) {
      store_tiles<6, 16>(rs, (int)OFF_WVO, lane, [&](int i) { return tiled_blk(wcol * 6 + wn * 3 + i % 3, KD, wmb * 4 + wm * 2 + i / 3); },
                         [&](int i) { return acc[i / 3][i % 3]; });
      pw.i0 = C_W + wcol;
    }
    STAMP(ws, 5, 3);
  }
  Pending pv;
  if (wm == 0) big_p1<5>(smem, ws, gA, g, col, 0, wn, lane, arr, pw, pv);
  else big_p1<4>(smem, ws, gA, g, col, 5, wn, lane, arr, pw, pv);
  p2_mfma(smem, ws, t2, 1 * SLOT, c, lane, arr + 4, pv);
}

template <int MT, bool VT, bool FIRST>
__device__ __forceinline__ void small_p1(LDS_AS char* smem, char* __restrict__ ws, const u16* __restrict__ wsrc, int wcol, int obase,
                                         int gA, int col, int smod, int wave8, int lane, const Pending& pend) {
  constexpr int MH = MT / 2;
  const u32x4 srd_ws = make_srd(ws, WS_END);
  rsrc_t rs = make_rsrc(ws, WS_END);
  auto gtile = [&](int lt) { return lt < MH ? gA + lt : gA + RT + (lt - MH); };
  if (wave8 == 7) {
    if (FIRST) {
      u32x4 mp[12][KSPL];
#pragma unroll
      for (int h = 0; h < 12; ++h)
#pragma unroll
        for (int k = 0; k < KSPL; ++k) mp[h][k] = aload(srd_ws, lane * 16, (int)OFF_MOD + (((k * 2 + smod) * 2 + h / 6) * 2304 + (h % 6) * 256) * 4);
      WAIT_VM(0);
#pragma unroll
      for (int h = 0; h < 12; ++h) {
        TIE(mp[h][0]); TIE(mp[h][1]);
        f32x4 v = __builtin_bit_cast(f32x4, mp[h][0]) + __builtin_bit_cast(f32x4, mp[h][1]);
        if (h % 6 >= 3) v += 1.0f;
        *(LDS_AS f32x4*)(smem + P1_SGS + h * 1024 + lane * 16) = v;
      }
      WAIT_LGKM(0);
    }
    barriers(FIRST ? 15 : 14);
    return;
  }
  if (wave8 >= 4) {
    const int l = wave8 - 4;
    rsrc_t rb = VT ? rs : make_rsrc(wsrc, D * 3 * D * 2);
    int bv[4];
#pragma unroll
    for (int i = 0; i < 4; ++i) {
      const int pc = l * 4 + i;
      bv[i] = VT ? ((col * 6 + pc % 6) * KD + pc / 6) * 1024 + lane * 16 : wcol + t96_src(pc, lane, WP, col & 1);
    }
    if (VT) poll(srd_ws, C_W + col, 12);
    loader_loop<12, 5, 4, 0, FIRST>([&](int s, int slot) {
#pragma unroll
      for (int i = 0; i < 4; ++i) {
        if (VT) dma_sc1(rb, smem + P1_SB + slot * 12288 + (l * 4 + i) * 1024, bv[i], (int)OFF_WVO + s * 2048);
        else dma<0>(rb, smem + P1_SB + slot * 12288 + (l * 4 + i) * 1024, bv[i], s * 64 * WP);
      }
    });
    BARRIER();
    return;
  }
  const int c = wave8, wm = c >> 1, wn = c & 1;
  auto aoff = [&](int st) { return st * (MT * 2048); };
  if (FIRST) {
    u32x4 xr[6][MT];
#pragma unroll
    for (int k = 0; k < 6; ++k)
#pragma unroll
      for (int i = 0; i < MT; ++i) xr[k][i] = aload(srd_ws, lane * 16, (int)OFF_XN + (gtile(i) * KD + c * 6 + k) * 1024);
    BARRIER();
    WAIT_VM(0);
#pragma unroll
    for (int k = 0; k < 6; ++k) {
      f32x4 gs[2][4];
#pragma unroll
      for (int z = 0; z < 2; ++z) {
        const unsigned gp = lds_addr(smem) + P1_SGS + z * 6144 + (c * 6 + k) * 128 + (lane >> 4) * 32;
        rd_f4<0>(gs[z][0], gp); rd_f4<16>(gs[z][1], gp); rd_f4<3072>(gs[z][2], gp); rd_f4<3088>(gs[z][3], gp);
      }
      WAIT_LGKM(0);
#pragma unroll
      for (int z = 0; z < 2; ++z)
#pragma unroll
        for (int i = 0; i < 4; ++i) TIE(gs[z][i]);
#pragma unroll
      for (int i = 0; i < MT; ++i) {
        TIE(xr[k][i]);
        *(LDS_AS u32x4*)(smem + aoff(c * 3 + (k >> 1)) + i * 2048 + (k & 1) * 1024 + lane * 16) = modulate(xr[k][i], gs[i < MH ? 0 : 1]);
      }
    }
    WAIT_LGKM(0);
  }
  const unsigned aa = lds_addr(smem) + wm * MH * 2048 + lane * 16;
  unsigned brd[3];
#pragma unroll
  for (int n = 0; n < 3; ++n) brd[n] = lds_addr(smem) + P1_SB + (VT ? wn * 3072 + lane * 16 : t96_rd(lane, wn * 3 + n, col & 1));
  f32x4 acc[MH][3] = {};
  BARRIER();
  gemm12<MH, 3, !VT, !VT, 1024, 6144, 12288, MT * 2048, 4>(acc, aa, aa + 1024, brd, 0, ws, pend, lane);
  BARRIER();
  STAMP(ws, FIRST ? 1 : 4, 2);
  const int nt0 = (col & 7) * 6 + wn * 3;
  if (VT) store_tiles<MH * 3, 16>(rs, obase, lane, [&](int i) { return tiled_blk(wm * 48 + nt0 + i % 3, KK, gA + i / 3); },
                                  [&](int i) { return acc[i / 3][i % 3]; });
  else store_tiles<MH * 3, 16>(rs, obase, lane, [&](int i) { return tiled_blk((FIRST ? gA + wm * RT : wm * MH) + i / 3, KD, nt0 + i % 3); },
                               [&](int i) { return acc[i / 3][i % 3]; });
  STAMP(ws, FIRST ? 1 : 4, 3);
}

__device__ __forceinline__ void small_wg(LDS_AS char* smem, char* __restrict__ ws, const u16* __restrict__ wqkv_img,
                                         const u16* __restrict__ wqkv_txt, const u16* __restrict__ wo_img,
                                         const u16* __restrict__ wo_txt, int bid, int wave8, int lane) {
  const int x = bid & 7, idx = bid >> 3;
  const u32x4 srd_ws = make_srd(ws, WS_END);
  rsrc_t rs = make_rsrc(ws, WS_END);
  LDS_AS unsigned* arr = (LDS_AS unsigned*)(smem + ARR);
  Pending pv{-1, -1, arr + 1};
  if (bid < 240) {
    const int col = (x >> 1) * 4 + (x & 1) * 2 + (idx - 28), sec = col >> 3;
    small_p1<2, false, true>(smem, ws, wqkv_img, col * 192, (int)(sec ? OFF_K : OFF_Q), 63, col, 0, wave8, lane, Pending{-1, -1, arr});
    small_p1<2, true, false>(smem, ws, wqkv_img, 0, (int)(sec ? OFF_VT2 : OFF_VT1), 63, col, 0, wave8, lane,
                             Pending{C_QK + sec * 8 + 7, C_QK + 16 + sec * 8 + 7, arr});
    pv.i0 = C_V + sec * 8 + (col & 7); pv.i1 = pv.i0 + 16;
  } else {
    const int col = bid - 240;
    small_p1<4, false, true>(smem, ws, wqkv_txt, col * 192, (int)(col >> 3 ? OFF_K : OFF_Q), 64, col, 1, wave8, lane, Pending{-1, -1, arr});
    const Pending pq{C_T + (col >> 3), -1, arr};
    if (col < 8) {
      small_p1<4, false, false>(smem, ws, wqkv_txt, 3072 + col * 192, (int)OFF_VX, 64, col, 1, wave8, lane, pq);
      pv.i0 = C_T + 2;
    } else if (wave8 < 4) {
      flush(ws, pq, lane);
    }
  }
  if (wave8 < 4) flush(ws, pv, lane);
  if (bid < 242) {
    const P2Tile t2 = p2_tile(bid);
    if (wave8 == 7) barriers(14);
    else if (wave8 >= 4) {
      poll(srd_ws, p2_counter(t2, lane), 8);
      loader_loop<12, 5, 8>([&](int s, int slot) { p2_issue(smem, rs, t2, wave8 - 4, lane, s, slot); });
      BARRIER();
    } else {
      BARRIER();
      p2_mfma(smem, ws, t2, 0, wave8, lane, arr + 4, Pending{-1, -1, arr});
    }
  } else if (bid < 254) {
    const int e = bid - 242, wave = wave8 & 3;
    if (wave8 >= 4) {
      rsrc_t rb = make_rsrc(e < 6 ? wo_img : wo_txt, D * D * 2);
      const int bv0 = (e % 6) * 256 + wave * 16 * (D * 2) + timg_src(lane, 0, D * 2);
      const int bv1 = (e % 6) * 256 + (wave * 16 + 8) * (D * 2) + timg_src(lane, 1, D * 2);
      poll(srd_ws, C_T + 2, 8);
      loader_loop<12, 5, 6>([&](int s, int slot) {
        LDS_AS char* sb = smem + slot * SLOT;
        dma_sc1<0>(rs, sb + wave * 2048, lane * 16, (int)OFF_VX + (wave * KD + 2 * s) * 1024);
        dma_sc1<1024>(rs, sb + wave * 2048, lane * 16, (int)OFF_VX + (wave * KD + 2 * s) * 1024);
#pragma unroll
        for (int i = 0; i < 4; ++i) dma<0>(rb, sb + 8192 + (wave * 4 + i) * 1024, ((i >> 1) ? bv1 : bv0) + (i & 1) * 128, s * 64 * (D * 2));
      });
    } else {
      const int wm = wave >> 1, wn = wave & 1;
      const unsigned aa = lds_addr(smem) + wm * 4096 + lane * 16;
      unsigned brd[4];
#pragma unroll
      for (int n = 0; n < 4; ++n) brd[n] = lds_addr(smem) + 8192 + timg_rd<2>(lane, wn * 4 + n);
      f32x4 acc[2][4] = {};
      BARRIER();
      gemm12<2, 4, true, false, 0, 8192, SLOT, 0, 4>(acc, aa, aa + 1024, brd, 0, ws, Pending{-1, -1, arr}, lane);
      STAMP(ws, 2, 2);
      store_tiles<8, 16>(rs, (int)(e < 6 ? OFF_VT1 : OFF_VT2), lane,
                         [&](int i) { return tiled_blk(wm * 48 + (e % 6) * 8 + wn * 4 + (i & 3), KK, 64 + (i >> 2)); },
                         [&](int i) { return acc[i >> 2][i & 3]; });
      if (last_wave(arr + 4, 4, lane)) signal(ws, C_X + e);
      STAMP(ws, 2, 3);
    }
  }
}

constexpr int K3_NS = 4, K3_STAGE = 33 * 1024, K3_F = K3_NS * K3_STAGE;
template <int QT>
__device__ __forceinline__ void k3_mfma(LDS_AS char* smem, char* __restrict__ ws, int wn, int qoff, int nb, int lane, u16* __restrict__ outp) {
  const unsigned vrd = lds_addr(smem) + wn * 9216 + lane * 16, prd = lds_addr(smem) + 18432 + qoff * 3072 + lane * 16;
  const unsigned fs = lds_addr(smem) + K3_F + (qoff * 16 + (lane & 15)) * 4;
  auto rdf = [&](float (&f)[QT], int t) {
    rd_32<0>(f[0], fs + t * 320);
    if constexpr (QT > 1) rd_32<64>(f[QT > 1 ? 1 : 0], fs + t * 320);
    if constexpr (QT > 2) rd_32<128>(f[QT > 2 ? 2 : 0], fs + t * 320);
  };
  f32x4 acc[3][QT] = {}, t0[3][QT], t1[3][QT] = {};
  Frags<3, QT> f0, f1, f2;
  unsigned bc[QT];
  bc[0] = prd;
  BARRIER();
  STAMP(ws, 3, 1);
  rd_set<3, QT, false, 3072, 3072, 0, 0>(f0, vrd, bc);
  rd_set<3, QT, false, 3072, 3072, 1024, 1024>(f1, vrd, bc);
  int so = 0;
  auto stage = [&](int t, f32x4 (&cur)[3][QT], f32x4 (&prv)[3][QT]) {
    BARRIER();
    float fp[QT];
    rdf(fp, t);
    kstep<3, QT, false, false, true, 3072, 3072, 2048, 2048, true>(cur, f0, f2, vrd + so, bc, NoExtra{});
    so = wrapn(so + K3_STAGE, K3_NS * K3_STAGE);
    bc[0] = prd + so;
    kstep<3, QT, false, false, false, 3072, 3072, 0, 0, true>(cur, f1, f0, vrd + so, bc, [&](auto ic) {
      constexpr int I = decltype(ic)::value;
      if constexpr (I < QT) tie1(fp[I]);
      acc[I / QT][I % QT] += prv[I / QT][I % QT] * fp[I % QT];
      tie4(acc[I / QT][I % QT]);
    });
    kstep<3, QT, false, false, false, 3072, 3072, 1024, 1024, true>(cur, f2, f1, vrd + so, bc, NoExtra{});
  };
#pragma unroll 1
  for (int t = 0; t < 10; t += 2) {
    stage(t, t0, t1);
    stage(t + 1, t1, t0);
  }
  stage(10, t0, t1);
  float fp[QT];
  rdf(fp, 11);
  land<3, QT, false>(f0);
  land<3, QT, false>(f1);
#pragma unroll
  for (int j = 0; j < QT; ++j) TIE(fp[j]);
  STAMP(ws, 3, 2);
  LDS_AS char* xs = smem + 3 * K3_STAGE;
  const int cw = wn * 48 + (lane >> 4) * 4;
  f32x4 o[QT * 3];
#pragma unroll
  for (int i = 0; i < 3; ++i) {
    const f32x4 gate = *(LDS_AS f32x4*)(xs + 15360 + (cw + i * 16) * 4) + *(LDS_AS f32x4*)(xs + 15360 + 384 + (cw + i * 16) * 4);
#pragma unroll
    for (int j = 0; j < QT; ++j) {
      const u32x2 xv = *(LDS_AS u32x2*)(xs + ((qoff + j) * 16 + (lane & 15)) * 192 + (cw + i * 16) * 2);
      const f32x4 a = acc[i][j] + t0[i][j] * fp[j];
      o[j * 3 + i] = f32x4{fmaf(gate[0], a[0], bflo(xv[0])), fmaf(gate[1], a[1], bfhi(xv[0])),
                           fmaf(gate[2], a[2], bflo(xv[1])), fmaf(gate[3], a[3], bfhi(xv[1]))};
    }
  }
  const bool odd = (lane >> 4) & 1;
  u16* ob = outp + (size_t)(qoff * 16 + (lane & 15)) * D + nb * 96 + wn * 48;
  auto toff = [&](int i) { return (i / 3) * 16 * D + (i % 3) * 16; };
#pragma unroll
  for (int i = 0; i + 1 < QT * 3; i += 2) *(u32x4*)(ob + (odd ? toff(i + 1) : toff(i)) + (lane >> 5) * 8) = pair16(o[i], o[i + 1]);
  if (QT & 1) *(u32x2*)(ob + toff(QT * 3 - 1) + (lane >> 4) * 4) = pack4(o[QT * 3 - 1]);
  STAMP(ws, 3, 3);
}

__device__ __forceinline__ void phase3(LDS_AS char* smem, char* __restrict__ ws, const u16* __restrict__ x_img,
                                       const u16* __restrict__ x_txt, u16* __restrict__ out, int bid, int wave8, int lane) {
  if (bid >= 224) return;
  const int wave = wave8 & 3, x = bid & 7, idx = bid >> 3, b = x & 1;
  int rt0, nrt, nb, txt;
  if (idx < 26) {
    const int u = (x >> 1) * 26 + idx, rb = (u % 52) >> 2;
    nb = (u / 52) * 4 + (u & 3); rt0 = rb * 5; nrt = rb == 12 ? 4 : 5; txt = 0;
  } else {
    nb = (x >> 1) * 2 + idx - 26; rt0 = 64; nrt = 2; txt = 1;
  }
  if (wave8 >= 4) {
    rsrc_t rs = make_rsrc(ws, WS_END);
    const u32x4 srd_ws = make_srd(ws, WS_END);
    auto tail = [&](int slot) {
      const u16* xres = txt ? x_txt + (size_t)b * NTX * D : x_img + (size_t)(b * NI + rt0 * 16) * D;
      rsrc_t rx = make_rsrc(xres, nrt * 16 * D * 2);
      LDS_AS char* sb = smem + slot * K3_STAGE + wave * 4096;
#pragma unroll
      for (int i = 0; i < 4; ++i) {
        const int c = (wave * 4 + i) * 64 + lane;
        if (wave * 4 + i < 15) dma<0>(rx, sb + i * 1024, (c / 12) * (D * 2) + nb * 192 + (c % 12) * 16, 0);
        else dma<0>(rs, sb + i * 1024, lane < 24 * KSPL ? (int)OFF_MOD + ((((lane / 24) * 2 + txt) * 2 + b) * 2304 + 1536 + nb * 96 + (lane % 24) * 4) * 4 : OOB, 0);
      }
    };
    if (wave < 2) {
      poll(srd_ws, C_V + (b * 2 + txt) * 8 + nb, 8);
      const int vb = (int)(txt ? OFF_VT2 : OFF_VT1) + (b * 48 + nb * 6 + wave * 3) * KK * 1024;
      loader_loop<11, K3_NS, 9, 4>([&](int t, int slot) {
        if (t == 10) poll(srd_ws, C_X + txt * 6 + (nb * 96 + (lane & 1) * 95) / 128, 1);
        LDS_AS char* sb = smem + slot * K3_STAGE + wave * 9216;
#pragma unroll
        for (int i = 0; i < 3; ++i) {
          const int so = vb + i * KK * 1024 + t * 3072;
          dma_sc1<0>(rs, sb + i * 3072, lane * 16, so);
          dma_sc1<1024>(rs, sb + i * 3072, lane * 16, so);
          dma_sc1<2048>(rs, sb + i * 3072, lane * 16, so);
        }
      }, NoHook{}, tail);
    } else {
      poll(srd_ws, C_P + b * 11 + (rt0 * 16 + (lane & 1) * (nrt * 16 - 1)) / 96, 11);
      const int ltid = (wave - 2) * 64 + lane;
      u32x4 st[11];
      {
        const int r = min(ltid, nrt * 16 - 1), vs = (int)OFF_ST + (b * RT + rt0 + (r >> 4)) * 2816 + (r & 15) * 16;
#pragma unroll
        for (int i = 0; i < 11; ++i) st[i] = aload_sc1(srd_ws, vs, i * 256);
      }
      const int pb = (int)OFF_P + (b * RT + rt0) * KK * 1024;
      loader_loop<11, K3_NS, 8, 4>([&](int t, int slot) {
#pragma unroll
        for (int i = 0; i < 8; ++i) {
          const int pc = (wave - 2) * 8 + i, u = pc / 3, ks = pc % 3;
          dma_sc1(rs, smem + slot * K3_STAGE + 18432 + min(pc, 14) * 1024, lane * 16,
                  u < nrt ? pb + (u * KK + t * 3 + ks) * 1024 : (pc == 15 ? pb + ((nrt - 1) * KK + t * 3 + 2) * 1024 + (nrt == 5 ? 0 : OOB) : OOB));
        }
      }, [&]() {
        float m[11];
#pragma unroll
        for (int t = 0; t < 11; ++t) { TIE(st[t]); m[t] = __uint_as_float(st[t][0]); }
        float M = m[0];
#pragma unroll
        for (int t = 1; t < 11; ++t) M = fmaxf(M, m[t]);
        float L = 0.f;
#pragma unroll
        for (int t = 0; t < 11; ++t) {
          m[t] = __builtin_amdgcn_exp2f(m[t] - M);
          L = fmaf(m[t], __uint_as_float(st[t][1]) + __uint_as_float(st[t][3]), L);
        }
        const float inv = 1.0f / L;
        LDS_AS float* fw = (LDS_AS float*)(smem + K3_F);
        if (ltid < 80) {
          fw[ltid] = 0.f;
#pragma unroll
          for (int t = 0; t < 11; ++t) fw[(1 + t) * 80 + ltid] = m[t] * inv;
        }
        WAIT_LGKM(0);
      }, tail);
    }
    return;
  }
  u16* outp = txt ? out + (size_t)2 * NI * D + (size_t)b * NTX * D : out + (size_t)(b * NI + rt0 * 16) * D;
  const int wq = wave >> 1, q0 = (nrt + 1) >> 1;
  const int qt = wq ? nrt - q0 : q0, qoff = wq ? q0 : 0;
  if (qt == 3) k3_mfma<3>(smem, ws, wave & 1, qoff, nb, lane, outp);
  else if (qt == 2) k3_mfma<2>(smem, ws, wave & 1, qoff, nb, lane, outp);
  else k3_mfma<1>(smem, ws, wave & 1, qoff, nb, lane, outp);
}

__global__ __launch_bounds__(512, 2) void kmain(const u16* __restrict__ x_img, const u16* __restrict__ x_txt,
                                                const u16* __restrict__ wqkv_img, const u16* __restrict__ wqkv_txt,
                                                const u16* __restrict__ wo_img, const u16* __restrict__ wo_txt,
                                                char* __restrict__ ws, u16* __restrict__ out) {
  __shared__ __attribute__((aligned(1024))) char smem_[MAIN_LDS];
  LDS_AS char* smem = (LDS_AS char*)smem_;
  const int bid = blockIdx.x, tid = threadIdx.x, lane = tid & 63;
  const int wave = __builtin_amdgcn_readfirstlane(tid >> 6);
  asm volatile(".Lmmdit_main_begin:");
  if (tid < 8) ((LDS_AS unsigned*)(smem + ARR))[tid] = 0u;
  WAIT_LGKM(0);
  STAMP(ws, 5, 0);
  if (bid < 224) {
    big_wg(smem, ws, wqkv_img, wo_img, wo_txt, bid, wave, lane);
    STAMP(ws, 3, 0);
    phase3(smem, ws, x_img, x_txt, out, bid, wave, lane);
  } else {
    small_wg(smem, ws, wqkv_img, wqkv_txt, wo_img, wo_txt, bid, wave, lane);
  }
  asm volatile(".Lmmdit_main_end:");
}

extern "C" void kernel_launch(void* const* d_in, const int* in_sizes, int n_in, void* d_out, int out_size, void* d_ws,
                              size_t ws_size, hipStream_t stream) {
  const u16* x_img = (const u16*)d_in[0];
  const u16* x_txt = (const u16*)d_in[1];
  const u16* cvec = (const u16*)d_in[2];
  const u16* wqkv_img = (const u16*)d_in[3];
  const u16* wqkv_txt = (const u16*)d_in[4];
  const u16* wo_img = (const u16*)d_in[5];
  const u16* wo_txt = (const u16*)d_in[6];
  const u16* wmod_img = (const u16*)d_in[7];
  const u16* wmod_txt = (const u16*)d_in[8];
  char* ws = (char*)d_ws;
  k0_prep<<<K0_T1 + K0_T2 + K0_Z + K0_PF, 256, 0, stream>>>(x_img, x_txt, cvec, wmod_img, wmod_txt, wqkv_img, wqkv_txt, wo_img, wo_txt, ws);
  kmain<<<256, 512, 0, stream>>>(x_img, x_txt, wqkv_img, wqkv_txt, wo_img, wo_txt, ws, (u16*)d_out);
}
```

```cpp
#include <hip/hip_runtime.h>
#include <hip/hip_bf16.h>
#include <stdint.h>
#include <type_traits>

typedef __attribute__((ext_vector_type(8))) __bf16 bf16x8;
typedef __attribute__((ext_vector_type(4))) __bf16 bf16x4;
typedef __attribute__((ext_vector_type(2))) __bf16 bf16x2;
typedef __attribute__((ext_vector_type(4))) float f32x4;
typedef __attribute__((ext_vector_type(2))) float f32x2;
typedef __attribute__((ext_vector_type(4))) unsigned u32x4;
typedef __attribute__((ext_vector_type(2))) unsigned u32x2;
#define LDS_AS __attribute__((address_space(3)))
typedef __amdgpu_buffer_rsrc_t rsrc_t;
typedef unsigned short u16;

constexpr int D = 768, NI = 1024, NTX = 32, NK = 1056;
constexpr int RT = 66;
constexpr int KD = 24;
constexpr int KK = 33;
constexpr int KSPL = 2;
constexpr int OOB = 0x7f000000;

constexpr unsigned SZ_TOK = 2u * RT * KD * 1024u;
constexpr unsigned SZ_VT = 2u * 48u * KK * 1024u;
constexpr unsigned OFF_XN = 0;
constexpr unsigned OFF_Q = OFF_XN + SZ_TOK;
constexpr unsigned OFF_K = OFF_Q + SZ_TOK;
constexpr unsigned OFF_VX = OFF_K + SZ_TOK;
constexpr unsigned OFF_VT1 = OFF_VX + 4u * KD * 1024u;
constexpr unsigned OFF_VT2 = OFF_VT1 + SZ_VT;
constexpr unsigned OFF_P = OFF_VT2 + SZ_VT;
constexpr unsigned OFF_WVO = OFF_P + 2u * RT * KK * 1024u;
constexpr unsigned OFF_MOD = OFF_WVO + 96u * KD * 1024u;
constexpr unsigned OFF_ST = OFF_MOD + KSPL * 4u * 2304u * 4u;
constexpr unsigned OFF_CNT = OFF_ST + 2u * RT * 11u * 256u;
constexpr unsigned OFF_H = OFF_CNT + 20480u;
constexpr unsigned WS_END = OFF_H + SZ_TOK;
constexpr int C_H = 0;
constexpr int C_QK = 14;
constexpr int C_T = 46;
constexpr int C_V = 49;
constexpr int C_X = 81;
constexpr int C_P = 93;
constexpr int C_W = 115;
constexpr unsigned SZ_CNT = 20480u;

#define WAIT_VM(n) asm volatile("s_waitcnt vmcnt(%0)" ::"n"(n) : "memory")
#define WAIT_LGKM(n) asm volatile("s_waitcnt lgkmcnt(%0)" ::"n"(n) : "memory")
#define BARRIER() __builtin_amdgcn_s_barrier()
#define TIE(x) asm volatile("" : "+v"(x))
#define SCHED_FENCE() __builtin_amdgcn_sched_barrier(0)

#define STAMP(ws, k, i) do { } while (0)
#define TRACE(ws, k, i) do { } while (0)

template <int I, int N, class F>
__device__ __forceinline__ void static_for(F&& f) {
  if constexpr (I < N) { f(std::integral_constant<int, I>{}); static_for<I + 1, N>(f); }
}
__device__ __forceinline__ rsrc_t make_rsrc(const void* p, unsigned bytes) {
  return __builtin_amdgcn_make_buffer_rsrc((void*)p, 0, (int)bytes, 0x00020000);
}
__device__ __forceinline__ u32x4 make_srd(const void* p, unsigned bytes) {
  unsigned long long a = (unsigned long long)p;
  u32x4 r;
  r[0] = (unsigned)a; r[1] = (unsigned)(a >> 32) & 0xffffu; r[2] = bytes; r[3] = 0x00020000u;
  return r;
}
template <int IMM>
__device__ __forceinline__ void dma(rsrc_t rs, LDS_AS char* lds, int voff, int soff) {
  __builtin_amdgcn_raw_ptr_buffer_load_lds(rs, (LDS_AS void*)lds, 16, voff, soff, IMM, 0);
}
template <int IMM = 0>
__device__ __forceinline__ void dma_sc1(rsrc_t rs, LDS_AS char* lds, int voff, int soff) {
  __builtin_amdgcn_raw_ptr_buffer_load_lds(rs, (LDS_AS void*)lds, 16, voff, soff, IMM, 16);
}
__device__ __forceinline__ u32x4 aload(const u32x4& srd, int voff, int soff) {
  u32x4 r;
  asm volatile("buffer_load_dwordx4 %0, %1, %2, %3 offen" : "=v"(r) : "v"(voff), "s"(srd), "s"(soff));
  return r;
}
__device__ __forceinline__ u32x4 aload_sc1(const u32x4& srd, int voff, int soff) {
  u32x4 r;
  asm volatile("buffer_load_dwordx4 %0, %1, %2, %3 offen sc1" : "=v"(r) : "v"(voff), "s"(srd), "s"(soff));
  return r;
}
__device__ __forceinline__ void signal(char* ws, int idx) {
  asm volatile("global_atomic_add %0, %1, %2" ::"v"((int)OFF_CNT + idx * 128), "v"(1u), "s"(ws) : "memory");
}
__device__ __forceinline__ bool last_wave(LDS_AS unsigned* word, unsigned n, int lane) {
  WAIT_VM(0);
  unsigned old = 0;
  if (lane == 0) asm volatile("ds_add_rtn_u32 %0, %1, %2\n\ts_waitcnt lgkmcnt(0)" : "=v"(old) : "v"((unsigned)(size_t)word), "v"(1u) : "memory");
  return lane == 0 && old == n - 1;
}
struct Pending { int i0, i1; LDS_AS unsigned* arr; };
__device__ __forceinline__ void flush(char* ws, const Pending& p, int lane) {
  if (p.i0 >= 0 && last_wave(p.arr, 4, lane)) { signal(ws, p.i0); if (p.i1 >= 0) signal(ws, p.i1); }
}
__device__ __forceinline__ void poll(const u32x4& srd, int idx, unsigned target) {
#pragma unroll 1
  for (int spin = 0; spin < (1 << 20); ++spin) {
    unsigned f;
    asm volatile("buffer_load_dword %0, %1, %2, 0 offen sc1\n\ts_waitcnt vmcnt(0)" : "=v"(f) : "v"((int)OFF_CNT + idx * 128), "s"(srd) : "memory");
    if (__builtin_amdgcn_ballot_w64(f < target) == 0) break;
    __builtin_amdgcn_s_sleep(1);
  }
}
__device__ __forceinline__ unsigned peek(const u32x4& srd, int idx) {
  unsigned f;
  asm volatile("buffer_load_dword %0, %1, %2, 0 offen sc1" : "=v"(f) : "v"((int)OFF_CNT + idx * 128), "s"(srd) : "memory");
  return f;
}
__device__ __forceinline__ void check(unsigned f, const u32x4& srd, int idx, unsigned target) {
  WAIT_VM(0);
  TIE(f);
  if (__builtin_amdgcn_ballot_w64(f < target) != 0) poll(srd, idx, target);
}
__device__ __forceinline__ void barriers(int n) {
#pragma unroll 1
  for (int t = 0; t < n; ++t) BARRIER();
}
template <int OFF>
__device__ __forceinline__ void rd_tr(bf16x4& lo, bf16x4& hi, unsigned addr) {
  asm volatile("ds_read_b64_tr_b16 %0, %2 offset:%c3\n\tds_read_b64_tr_b16 %1, %2 offset:%c4"
               : "=&v"(lo), "=&v"(hi) : "v"(addr), "i"(OFF), "i"(OFF + 512));
}
template <int OFF>
__device__ __forceinline__ void rd_128(bf16x8& r, unsigned addr) {
  asm volatile("ds_read_b128 %0, %1 offset:%c2" : "=v"(r) : "v"(addr), "i"(OFF));
}
template <int OFF>
__device__ __forceinline__ void rd_32(float& r, unsigned addr) {
  asm volatile("ds_read_b32 %0, %1 offset:%c2" : "=v"(r) : "v"(addr), "i"(OFF));
}
__device__ __forceinline__ void tie1(float& x) { TIE(x); }
__device__ __forceinline__ void tie4(f32x4& x) { TIE(x); }
__device__ __forceinline__ void tie8(bf16x8& x) { TIE(x); }
__device__ __forceinline__ void tie4h(bf16x4& x) { TIE(x); }
__device__ __forceinline__ bf16x8 cat(bf16x4 lo, bf16x4 hi) { return __builtin_shufflevector(lo, hi, 0, 1, 2, 3, 4, 5, 6, 7); }
__device__ __forceinline__ unsigned lds_addr(LDS_AS char* p) { return (unsigned)(size_t)p; }
__device__ __forceinline__ int wrapn(int v, int n) { return v >= n ? v - n : v; }
__device__ __forceinline__ f32x4 mfma(bf16x8 a, bf16x8 b, f32x4 c) {
  return __builtin_amdgcn_mfma_f32_16x16x32_bf16(a, b, c, 0, 0, 0);
}
__device__ __forceinline__ unsigned pack2(float a, float b) {
  f32x2 f = {a, b};
  bf16x2 h = __builtin_convertvector(f, bf16x2);
  return __builtin_bit_cast(unsigned, h);
}
__device__ __forceinline__ u32x2 pack4(f32x4 v) { return u32x2{pack2(v[0], v[1]), pack2(v[2], v[3])}; }
__device__ __forceinline__ float bflo(unsigned v) { return __uint_as_float(v << 16); }
__device__ __forceinline__ float bfhi(unsigned v) { return __uint_as_float(v & 0xffff0000u); }
__device__ __forceinline__ float max2(float a, float b) { float r; asm("v_max_f32 %0, %1, %2" : "=v"(r) : "v"(a), "v"(b)); return r; }
__device__ __forceinline__ float max3(float a, float b, float c) { float r; asm("v_max3_f32 %0, %1, %2, %3" : "=v"(r) : "v"(a), "v"(b), "v"(c)); return r; }
__device__ __forceinline__ float red4_max(float x) {
  auto a = __builtin_amdgcn_permlane16_swap(__float_as_uint(x), __float_as_uint(x), false, false);
  x = max2(__uint_as_float(a[0]), __uint_as_float(a[1]));
  auto b = __builtin_amdgcn_permlane32_swap(__float_as_uint(x), __float_as_uint(x), false, false);
  return max2(__uint_as_float(b[0]), __uint_as_float(b[1]));
}
__device__ __forceinline__ float red4_sum(float x) {
  auto a = __builtin_amdgcn_permlane16_swap(__float_as_uint(x), __float_as_uint(x), false, false);
  x = __uint_as_float(a[0]) + __uint_as_float(a[1]);
  auto b = __builtin_amdgcn_permlane32_swap(__float_as_uint(x), __float_as_uint(x), false, false);
  return __uint_as_float(b[0]) + __uint_as_float(b[1]);
}
__device__ __forceinline__ unsigned tiled_lane(int lane) {
  int g = lane >> 4;
  return (unsigned)(((lane & 15) + 16 * (g >> 1)) * 16 + (g & 1) * 8);
}
__device__ __forceinline__ unsigned tiled_blk(int rowtile, int KS, int T16) {
  return (unsigned)((rowtile * KS + (T16 >> 1)) * 1024 + (T16 & 1) * 512);
}
__device__ __forceinline__ u32x4 pair16(f32x4 x, f32x4 y) {
  const u32x2 px = pack4(x), py = pack4(y);
  auto a = __builtin_amdgcn_permlane16_swap(px[0], py[0], false, false);
  auto b = __builtin_amdgcn_permlane16_swap(px[1], py[1], false, false);
  return u32x4{a[0], b[0], a[1], b[1]};
}
__device__ __forceinline__ unsigned tiled_lane16(int lane) { return (unsigned)(((lane & 15) + 16 * (lane >> 5)) * 16); }
template <int NTILE, int AUX, class Blk, class Val>
__device__ __forceinline__ void store_tiles(rsrc_t rs, int base, int lane, Blk&& blk, Val&& val) {
  const bool odd = (lane >> 4) & 1;
  const int l16 = (int)tiled_lane16(lane);
#pragma unroll
  for (int i = 0; i + 1 < NTILE; i += 2) {
    const int so = __builtin_amdgcn_readfirstlane(base - 0x100000 + (int)blk(i));
    const int d = __builtin_amdgcn_readfirstlane((int)(blk(i + 1) - blk(i)));
    __builtin_amdgcn_raw_buffer_store_b128(pair16(val(i), val(i + 1)), rs, l16 + 0x100000 + (odd ? d : 0), so, AUX);
  }
  if (NTILE & 1)
    __builtin_amdgcn_raw_buffer_store_b64(pack4(val(NTILE - 1)), rs, (int)tiled_lane(lane),
                                          __builtin_amdgcn_readfirstlane(base + (int)blk(NTILE - 1)), AUX);
}
__device__ __forceinline__ int timg_src(int lane, int sigma, int pitch) {
  int r = lane >> 3, c = (lane & 7) ^ (2 * ((r >> 1) & 1) + 4 * sigma);
  return r * pitch + c * 16;
}
template <int NB>
__device__ __forceinline__ int timg_rd(int lane, int nt) {
  int g = lane >> 4, q = (lane & 15) >> 2, p = lane & 3;
  int f = 2 * ((q >> 1) & 1) + 4 * (g & 1);
  return (g * NB + (nt >> 2)) * 1024 + q * 128 + (((2 * (nt & 3)) ^ f) + (p >> 1)) * 16 + (p & 1) * 8;
}

template <int MT, int NT>
struct Frags { bf16x8 a[MT]; bf16x8 b[NT]; bf16x4 l[NT], h[NT]; };

template <int MT, int NT, bool BTR, int AM, int BM, int AO, int BO, int I>
__device__ __forceinline__ void rd_item(Frags<MT, NT>& n, unsigned ab, const unsigned (&bb)[NT]) {
  if constexpr (I < NT) {
    if constexpr (BTR) rd_tr<BO>(n.l[I], n.h[I], bb[I]);
    else rd_128<BO + I * BM>(n.b[I], bb[0]);
  } else if constexpr (I < NT + MT) {
    rd_128<AO + (I - NT) * AM>(n.a[I - NT], ab);
  }
}
template <int MT, int NT, bool BTR, int AM, int BM, int AO, int BO>
__device__ __forceinline__ void rd_set(Frags<MT, NT>& n, unsigned ab, const unsigned (&bb)[NT]) {
  static_for<0, MT + NT>([&](auto ic) { rd_item<MT, NT, BTR, AM, BM, AO, BO, decltype(ic)::value>(n, ab, bb); });
}
template <int MT, int NT, bool BTR, bool AH = false>
__device__ __forceinline__ void land(Frags<MT, NT>& c) {
  WAIT_LGKM(AH ? MT + (BTR ? 2 : 1) * NT : 0);
#pragma unroll
  for (int j = 0; j < NT; ++j) {
    if constexpr (BTR) { TIE(c.l[j]); TIE(c.h[j]); } else TIE(c.b[j]);
  }
#pragma unroll
  for (int m = 0; m < MT; ++m) TIE(c.a[m]);
  SCHED_FENCE();
}
template <int MT, int NT, bool BTR, bool SWAP, bool ZC, int AM, int BM, int AO, int BO, bool AH = false, class X>
__device__ __forceinline__ void kstep(f32x4 (&acc)[MT][NT], Frags<MT, NT>& c, Frags<MT, NT>& n, unsigned ab,
                                      const unsigned (&bb)[NT], X&& extra) {
  land<MT, NT, BTR, AH>(c);
  static_for<0, MT * NT>([&](auto ic) {
    constexpr int I = decltype(ic)::value, m = I / NT, j = I % NT;
    bf16x8 bf;
    if constexpr (BTR) bf = cat(c.l[j], c.h[j]); else bf = c.b[j];
    const f32x4 ci = ZC ? f32x4{0.f, 0.f, 0.f, 0.f} : acc[m][j];
    acc[m][j] = SWAP ? mfma(bf, c.a[m], ci) : mfma(c.a[m], bf, ci);
    tie4(acc[m][j]);
    rd_item<MT, NT, BTR, AM, BM, AO, BO, I>(n, ab, bb);
    extra(ic);
    SCHED_FENCE();
  });
  static_for<MT * NT, (MT + NT > MT * NT ? MT + NT : MT * NT)>(
      [&](auto ic) { rd_item<MT, NT, BTR, AM, BM, AO, BO, decltype(ic)::value>(n, ab, bb); });
}
struct NoExtra { template <class T> __device__ __forceinline__ void operator()(T) const {} };

struct NoHook { __device__ __forceinline__ void operator()() const {} };
struct NoTail { __device__ __forceinline__ void operator()(int) const {} };
template <int NST, int NS, int V, int TV = 0, bool XB = false, class Issue, class Hook = NoHook, class Tail = NoTail, class Pre = NoHook>
__device__ __forceinline__ void loader_loop(Issue&& issue, Hook&& hook = NoHook{}, Tail&& tail = NoTail{}, Pre&& pre = NoHook{}) {
#pragma unroll
  for (int s = 0; s < NS - 1; ++s) issue(s, s);
  if (XB) BARRIER();
  pre();
  WAIT_VM(V * (NS - 2));
  BARRIER();
  hook();
  int slot = NS - 1;
#pragma unroll 1
  for (int t = 0; t <= NST - NS; ++t) {
    WAIT_VM(V * (NS - 3));
    BARRIER();
    issue(t + NS - 1, slot);
    slot = wrapn(slot + 1, NS);
  }
  static_for<1, NS>([&](auto rc) {
    constexpr int R = decltype(rc)::value;
    WAIT_VM(R == NS - 1 ? 0 : V * (NS - 2 - R) + (R > 1 ? TV : 0));
    BARRIER();
    if constexpr (R == 1) tail(slot);
  });
}

template <int VP, int V, class Issue>
__device__ __forceinline__ void stream12(int& slot, Issue&& issue) {
  WAIT_VM(2 * VP); BARRIER(); issue(0, slot); slot = wrapn(slot + 1, 5);
  WAIT_VM(VP + V); BARRIER(); issue(1, slot); slot = wrapn(slot + 1, 5);
#pragma unroll 1
  for (int t = 2; t < 12; ++t) {
    WAIT_VM(2 * V); BARRIER(); issue(t, slot); slot = wrapn(slot + 1, 5);
  }
}

template <int MW, int NW, bool BTR, bool SWAP, int BM, int BKS, int SLOT, int ASTEP, int FT>
__device__ __forceinline__ void gemm12(f32x4 (&acc)[MW][NW], unsigned a0, unsigned a1, const unsigned (&brd)[NW], int so,
                                       char* __restrict__ ws, const Pending& pend, int lane) {
  Frags<MW, NW> x0, x1, y0, y1;
  unsigned bc[NW];
#pragma unroll
  for (int n = 0; n < NW; ++n) bc[n] = brd[n] + so;
  rd_set<MW, NW, BTR, 2048, BM, 0, 0>(x0, ASTEP ? a0 : a0 + so, bc);
  rd_set<MW, NW, BTR, 2048, BM, 0, BKS>(x1, ASTEP ? a1 : a1 + so, bc);
  auto stage = [&](Frags<MW, NW>& c0, Frags<MW, NW>& c1, Frags<MW, NW>& n0, Frags<MW, NW>& n1) {
    so = wrapn(so + SLOT, 5 * SLOT);
    a0 += ASTEP; a1 += ASTEP;
#pragma unroll
    for (int n = 0; n < NW; ++n) bc[n] = brd[n] + so;
    BARRIER();
    kstep<MW, NW, BTR, SWAP, false, 2048, BM, 0, 0, true>(acc, c0, n0, ASTEP ? a0 : a0 + so, bc, NoExtra{});
    kstep<MW, NW, BTR, SWAP, false, 2048, BM, 0, BKS, true>(acc, c1, n1, ASTEP ? a1 : a1 + so, bc, NoExtra{});
  };
#pragma unroll 1
  for (int t = 0; t < 12; t += 2) {
    if (t == FT) flush(ws, pend, lane);
    stage(x0, x1, y0, y1);
    stage(y0, y1, x0, x1);
  }
  land<MW, NW, BTR>(x0);
  land<MW, NW, BTR>(x1);
}
__device__ __forceinline__ int t96_src(int pc, int lane, int pitch, int odd) {
  const int ks = pc / 6, r = pc % 6, g1 = (lane >> 4) & 1, r16 = 8 * g1 + 4 * (lane >> 5) + ((lane >> 2) & 3);
  return r < 4 ? (ks * 32 + r * 8) * pitch + odd * 64 + timg_src(lane, r & 1, pitch)
               : (ks * 32 + (r - 4) * 16 + r16) * pitch + (odd ? 0 : 128) + (((lane & 3) ^ (2 * g1)) * 16);
}
__device__ __forceinline__ int t96_rd(int lane, int nt, int odd) {
  const int g = lane >> 4, q = (lane & 15) >> 2, p = lane & 3;
  const bool narrow = odd ? nt < 2 : nt >= 4;
  return narrow ? 4096 + (g >> 1) * 1024 + (g & 1) * 256 + q * 64 + (((2 * (nt & 1) + (p >> 1)) ^ (2 * (g & 1))) * 16) + (p & 1) * 8
                : timg_rd<1>(lane, odd ? nt - 2 : nt);
}

#define K0_PFN 144
constexpr int K0_T1 = 72 * KSPL, K0_T2 = 2 * RT, K0_Z = 8, K0_PF = K0_PFN;
__global__ __launch_bounds__(256) void k0_prep(const u16* __restrict__ x_img, const u16* __restrict__ x_txt,
                                               const u16* __restrict__ cvec, const u16* __restrict__ wmod_img,
                                               const u16* __restrict__ wmod_txt, const u16* __restrict__ wqkv_img,
                                               const u16* __restrict__ wqkv_txt, const u16* __restrict__ wo_img,
                                               const u16* __restrict__ wo_txt, char* __restrict__ ws) {
  __shared__ __attribute__((aligned(1024))) char smem_[24576];
  LDS_AS char* smem = (LDS_AS char*)smem_;
  const int bid = blockIdx.x, tid = threadIdx.x, lane = tid & 63;
  const int wave = __builtin_amdgcn_readfirstlane(tid >> 6);
  STAMP(ws, 0, 0);
  if (bid >= K0_T1 + K0_T2 + K0_Z) {
    const int i = bid - (K0_T1 + K0_T2 + K0_Z);
    const char* p = i < 18 ? (const char*)wo_img + i * 65536 : (i < 36 ? (const char*)wo_txt + (i - 18) * 65536
                  : (i < 90 ? (const char*)wqkv_img + (i - 36) * 65536 : (const char*)wqkv_txt + (i - 90) * 65536));
    u32x4 v[16];
#pragma unroll
    for (int k = 0; k < 16; ++k) v[k] = *(const u32x4*)(p + k * 4096 + tid * 16);
#pragma unroll
    for (int k = 0; k < 16; ++k) asm volatile("" ::"v"(v[k]));
    return;
  } else if (bid >= K0_T1 + K0_T2) {
    for (unsigned o = ((bid - K0_T1 - K0_T2) * 256 + tid) * 16; o < SZ_CNT; o += K0_Z * 4096) *(u32x4*)(ws + OFF_CNT + o) = u32x4{0u, 0u, 0u, 0u};
    unsigned long long pc;
    int delta, len;
    asm volatile("s_getpc_b64 %0\n.Lmmdit_k0_pc:\n\ts_mov_b32 %1, .Lmmdit_main_begin-.Lmmdit_k0_pc\n\ts_mov_b32 %2, .Lmmdit_main_end-.Lmmdit_main_begin"
                 : "=s"(pc), "=s"(delta), "=s"(len));
    pc += delta;
    unsigned acc = 0;
    for (int o = tid * 128; o < len; o += 256 * 128) acc ^= *(const unsigned*)((const char*)pc + o);
    asm volatile("" ::"v"(acc));
    return;
  } else if (bid < K0_T1) {
    constexpr int ROWS = D / KSPL, PER = ROWS / 32;
    const int i = bid, s = i / (36 * KSPL), rem = i % (36 * KSPL), cb = rem % 36, kh = rem / 36;
    const u16* w = (s ? wmod_txt : wmod_img) + (size_t)(kh * ROWS + (tid >> 3)) * (6 * D) + cb * 64 + (tid & 7) * 8;
    u16 cv[3];
#pragma unroll
    for (int e = 0; e < 3; ++e) { const int z = tid + e * 256; cv[e] = cvec[(z / ROWS) * D + kh * ROWS + z % ROWS]; }
    u32x4 wv[PER];
#pragma unroll
    for (int r = 0; r < PER; ++r) wv[r] = *(const u32x4*)(w + (size_t)r * 32 * (6 * D));
    LDS_AS float* sc = (LDS_AS float*)smem;
    LDS_AS float* red = (LDS_AS float*)(smem + 8192);
#pragma unroll
    for (int e = 0; e < 3; ++e) {
      const int z = tid + e * 256;
      const float v = __uint_as_float((unsigned)cv[e] << 16);
      sc[(z % ROWS) * 2 + z / ROWS] = v / (1.0f + __expf(-v));
    }
    __syncthreads();
    float a0[8] = {}, a1[8] = {};
#pragma unroll
    for (int r = 0; r < PER; ++r) {
      f32x2 sv = *(LDS_AS f32x2*)(sc + ((tid >> 3) + r * 32) * 2);
#pragma unroll
      for (int j = 0; j < 4; ++j) {
        float lo = bflo(wv[r][j]), hi = bfhi(wv[r][j]);
        a0[2 * j] = fmaf(sv[0], lo, a0[2 * j]); a0[2 * j + 1] = fmaf(sv[0], hi, a0[2 * j + 1]);
        a1[2 * j] = fmaf(sv[1], lo, a1[2 * j]); a1[2 * j + 1] = fmaf(sv[1], hi, a1[2 * j + 1]);
      }
    }
    LDS_AS float* rp = red + (tid >> 3) * 128 + (tid & 7) * 8;
    *(LDS_AS f32x4*)(rp) = f32x4{a0[0], a0[1], a0[2], a0[3]};
    *(LDS_AS f32x4*)(rp + 4) = f32x4{a0[4], a0[5], a0[6], a0[7]};
    *(LDS_AS f32x4*)(rp + 64) = f32x4{a1[0], a1[1], a1[2], a1[3]};
    *(LDS_AS f32x4*)(rp + 68) = f32x4{a1[4], a1[5], a1[6], a1[7]};
    __syncthreads();
    {
      const int o = tid >> 1, hf = tid & 1;
      float t0 = 0.f, t1 = 0.f;
#pragma unroll
      for (int g = 0; g < 16; g += 2) { t0 += red[(hf * 16 + g) * 128 + o]; t1 += red[(hf * 16 + g + 1) * 128 + o]; }
      float t = t0 + t1;
      t += __shfl_xor(t, 1);
      float* mod = (float*)(ws + OFF_MOD);
      if (hf == 0) mod[((kh * 2 + s) * 2 + (o >> 6)) * 2304 + cb * 64 + (o & 63)] = t;
    }
  } else {
    const int i = bid - K0_T1, b = i / RT, rt = i % RT;
    const u16* src = rt < 64 ? x_img + (size_t)(b * NI + rt * 16) * D : x_txt + (size_t)(b * NTX + (rt - 64) * 16) * D;
    src += (lane & 15) * D + wave * 192 + (lane >> 4) * 8;
    u32x4 v[6];
#pragma unroll
    for (int k = 0; k < 6; ++k) v[k] = *(const u32x4*)(src + k * 32);
    LDS_AS float* red = (LDS_AS float*)smem;
    float s0 = 0.f, s1 = 0.f;
#pragma unroll
    for (int k = 0; k < 6; ++k)
#pragma unroll
      for (int j = 0; j < 4; ++j) { s0 += bflo(v[k][j]); s1 += bfhi(v[k][j]); }
    const float s = red4_sum(s0 + s1);
    if (lane < 16) red[wave * 16 + lane] = s;
    __syncthreads();
    const int r = lane & 15;
    const float mean = ((red[r] + red[16 + r]) + (red[32 + r] + red[48 + r])) * (1.0f / D);
    float q0 = 0.f, q1 = 0.f;
#pragma unroll
    for (int k = 0; k < 6; ++k)
#pragma unroll
      for (int j = 0; j < 4; ++j) {
        float lo = bflo(v[k][j]) - mean, hi = bfhi(v[k][j]) - mean;
        q0 = fmaf(lo, lo, q0); q1 = fmaf(hi, hi, q1);
      }
    const float q = red4_sum(q0 + q1);
    if (lane < 16) red[64 + wave * 16 + lane] = q;
    __syncthreads();
    const float var = ((red[64 + r] + red[80 + r]) + (red[96 + r] + red[112 + r])) * (1.0f / D);
    const float rstd = 1.0f / sqrtf(var + 1e-5f);
    char* dst = ws + OFF_XN + (size_t)(i * KD + wave * 6) * 1024 + lane * 16;
#pragma unroll
    for (int k = 0; k < 6; ++k) {
      u32x4 o;
#pragma unroll
      for (int j = 0; j < 4; ++j) {
        typedef __attribute__((ext_vector_type(2))) _Float16 f16x2;
        const f16x2 h = {(_Float16)((bflo(v[k][j]) - mean) * rstd), (_Float16)((bfhi(v[k][j]) - mean) * rstd)};
        o[j] = __builtin_bit_cast(unsigned, h);
      }
      *(u32x4*)(dst + k * 1024) = o;
    }
  }
  STAMP(ws, 0, 3);
}

#define FTX 2
constexpr int SLOT = 30 * 1024;
constexpr int P1_SB = 98304, P1_SGS = P1_SB + 4 * 12288;
constexpr int RED = P1_SB + 5 * 12288, ARR = RED + 1024, MAIN_LDS = ARR + 32;
constexpr int WP = 3 * D * 2;

__device__ __forceinline__ void fmix_lo(float& d, unsigned v, float g, float s) {
  asm volatile("v_fma_mix_f32 %0, %1, %2, %3 op_sel_hi:[1,0,0]" : "=v"(d) : "v"(v), "v"(g), "v"(s));
}
__device__ __forceinline__ void fmix_hi(float& d, unsigned v, float g, float s) {
  asm volatile("v_fma_mix_f32 %0, %1, %2, %3 op_sel:[1,0,0] op_sel_hi:[1,0,0]" : "=v"(d) : "v"(v), "v"(g), "v"(s));
}
template <int OFF>
__device__ __forceinline__ void rd_f4(f32x4& r, unsigned addr) {
  asm volatile("ds_read_b128 %0, %1 offset:%c2" : "=v"(r) : "v"(addr), "i"(OFF));
}
__device__ __forceinline__ u32x4 modulate(u32x4 x, const f32x4 (&g)[4]) {
  float t[8];
#pragma unroll
  for (int d = 0; d < 4; ++d) {
    fmix_lo(t[2 * d], x[d], g[2 + (d >> 1)][(d & 1) * 2], g[d >> 1][(d & 1) * 2]);
    fmix_hi(t[2 * d + 1], x[d], g[2 + (d >> 1)][(d & 1) * 2 + 1], g[d >> 1][(d & 1) * 2 + 1]);
  }
  return u32x4{pack2(t[0], t[1]), pack2(t[2], t[3]), pack2(t[4], t[5]), pack2(t[6], t[7])};
}

struct P2Tile { int b, ti, tj; };
__device__ __forceinline__ P2Tile p2_tile(int bid) {
  const int x = bid & 7, idx = bid >> 3, qd = x >> 1;
  P2Tile t;
  t.b = x & 1;
  if (idx == 30) { t.ti = 5; t.tj = 5; }
  else if (qd == 0) { t.ti = idx / 6; t.tj = idx % 6; }
  else if (qd == 1) { t.ti = idx / 5; t.tj = 6 + idx % 5; }
  else if (qd == 2) { t.ti = 6 + idx / 6; t.tj = 5 + idx % 6; }
  else { t.ti = 5 + idx / 5; t.tj = idx % 5; }
  return t;
}
__device__ __forceinline__ int p2_counter(const P2Tile& t, int lane) {
  const int u = min(lane, 11), sec = u < 6 ? 1 : 0, rt = (u < 6 ? t.tj * 6 + u : t.ti * 6 + u - 6);
  return rt < 64 ? C_QK + (t.b * 2 + sec) * 8 + rt / 9 : C_T + sec;
}
__device__ __forceinline__ void p2_issue(LDS_AS char* smem, rsrc_t rs, const P2Tile& t, int l, int lane, int s, int slot) {
#pragma unroll
  for (int i = 0; i < 4; ++i) {
    const int u = l * 4 + i;
    const int so = (u < 6 ? (int)OFF_K + (t.b * RT + t.tj * 6 + u) * KD * 1024 : (int)OFF_Q + (t.b * RT + t.ti * 6 + u - 6) * KD * 1024) + s * 2048;
    dma_sc1<0>(rs, smem + slot * SLOT + u * 2048, lane * 16, so);
    dma_sc1<1024>(rs, smem + slot * SLOT + u * 2048, lane * 16, so);
  }
}
__device__ __forceinline__ void p2_mfma(LDS_AS char* smem, char* __restrict__ ws, const P2Tile& t, int so, int wave, int lane,
                                        LDS_AS unsigned* arr, const Pending& pend) {
  rsrc_t rs = make_rsrc(ws, WS_END);
  const int wk = wave >> 1, wq = wave & 1, b = t.b, ti = t.ti, tj = t.tj;
  const unsigned krd = lds_addr(smem) + wk * 6144 + lane * 16;
  const unsigned qrd[3] = {lds_addr(smem) + 12288 + wq * 6144 + lane * 16, 0, 0};
  const int g = lane >> 4;
  int pso[3];
#pragma unroll
  for (int j = 0; j < 3; ++j) pso[j] = (int)OFF_P + (b * RT + ti * 6 + wq * 3 + j) * KK * 1024 + (tj * 6 + wk * 3) * 512;
  int po16 = (int)tiled_lane16(lane) + (g & 1) * 512, po8 = (int)tiled_lane(lane) + 1024;
  int sso = (int)OFF_ST + ((b * RT + ti * 6 + wq * 3) * 11 + tj) * 256 + wk * 8, svo = g < 3 ? g * 2816 + (lane & 15) * 16 : OOB;
  unsigned rw = lds_addr(smem) + RED + (wk * 64 + lane) * 4 + wq * 512, rr = lds_addr(smem) + RED + ((wk ^ 1) * 64 + (lane & 15)) * 4 + wq * 512;
  asm volatile("" : "+s"(pso[0]), "+s"(pso[1]), "+s"(pso[2]), "+s"(sso), "+v"(po16), "+v"(po8), "+v"(svo), "+v"(rw), "+v"(rr));
  f32x4 acc[3][3] = {};
  gemm12<3, 3, false, false, 2048, 1024, SLOT, 0, FTX>(acc, krd, krd + 1024, qrd, so, ws, pend, lane);
  STAMP(ws, 2, 2);
  constexpr float SC = 0.036084391824351615f * 1.4426950408889634f;
  float mx[3], ls[3];
#pragma unroll
  for (int j = 0; j < 3; ++j) {
    const float m0 = max3(acc[0][j][0], acc[0][j][1], acc[0][j][2]), m1 = max3(acc[0][j][3], acc[1][j][0], acc[1][j][1]);
    const float m2 = max3(acc[1][j][2], acc[1][j][3], acc[2][j][0]), m3 = max3(acc[2][j][1], acc[2][j][2], acc[2][j][3]);
    mx[j] = red4_max(max2(max3(m0, m1, m2), m3));
  }
  {
    const float mine = g == 0 ? mx[0] : (g == 1 ? mx[1] : mx[2]);
    asm volatile("ds_write_b32 %0, %1\n\ts_waitcnt lgkmcnt(0)" ::"v"(rw), "v"(mine) : "memory");
    BARRIER();
    float o0, o1, o2;
    asm volatile("ds_read_b32 %0, %3\n\tds_read_b32 %1, %3 offset:64\n\tds_read_b32 %2, %3 offset:128\n\ts_waitcnt lgkmcnt(0)"
                 : "=&v"(o0), "=&v"(o1), "=&v"(o2) : "v"(rr) : "memory");
    mx[0] = max2(mx[0], o0) * SC; mx[1] = max2(mx[1], o1) * SC; mx[2] = max2(mx[2], o2) * SC;
  }
#pragma unroll
  for (int j = 0; j < 3; ++j) {
#pragma unroll
    for (int i = 0; i < 3; ++i)
#pragma unroll
      for (int r = 0; r < 4; ++r) acc[i][j][r] = __builtin_amdgcn_exp2f(fmaf(acc[i][j][r], SC, -mx[j]));
    __builtin_amdgcn_raw_buffer_store_b128(pair16(acc[0][j], acc[1][j]), rs, po16, pso[j], 16);
    __builtin_amdgcn_raw_buffer_store_b64(pack4(acc[2][j]), rs, po8, pso[j], 16);
    const f32x4 t4 = (acc[0][j] + acc[1][j]) + acc[2][j];
    ls[j] = red4_sum((t4[0] + t4[1]) + (t4[2] + t4[3]));
  }
  {
    const u32x2 v = {__float_as_uint(g == 0 ? mx[0] : (g == 1 ? mx[1] : mx[2])), __float_as_uint(g == 0 ? ls[0] : (g == 1 ? ls[1] : ls[2]))};
    __builtin_amdgcn_raw_buffer_store_b64(v, rs, svo, sso, 16);
  }
  BARRIER();
  if (last_wave(arr, 4, lane)) signal(ws, C_P + b * 11 + ti);
  STAMP(ws, 2, 3);
}

template <int MW>
__device__ __forceinline__ void big_p1(LDS_AS char* smem, char* __restrict__ ws, int gA, int g, int col, int m0, int wn, int lane,
                                       LDS_AS unsigned* arr, const Pending& pw, Pending& pv) {
  rsrc_t rs = make_rsrc(ws, WS_END);
  const unsigned l0 = lds_addr(smem), aa = l0 + m0 * 2048 + lane * 16;
  const int b = gA >= RT ? 1 : 0, sec = col >> 3, nt0 = (col & 7) * 6 + wn * 3;
  {
    unsigned brd[3];
#pragma unroll
    for (int n = 0; n < 3; ++n) brd[n] = l0 + 18432 + t96_rd(lane, wn * 3 + n, col & 1);
    f32x4 acc[MW][3] = {};
    gemm12<MW, 3, true, true, 0, 6144, SLOT, 0, FTX>(acc, aa, aa + 1024, brd, 2 * SLOT, ws, pw, lane);
    STAMP(ws, 1, 2);
    store_tiles<MW * 3, 16>(rs, (int)(sec ? OFF_K : OFF_Q), lane, [&](int i) { return tiled_blk(gA + m0 + i / 3, KD, nt0 + i % 3); },
                            [&](int i) { return acc[i / 3][i % 3]; });
    STAMP(ws, 1, 3);
  }
  {
    const unsigned brd[3] = {l0 + 18432 + wn * 3072 + lane * 16, 0, 0};
    f32x4 acc[MW][3] = {};
    gemm12<MW, 3, false, false, 1024, 6144, SLOT, 0, FTX>(acc, aa, aa + 1024, brd, 4 * SLOT, ws,
                                                         Pending{C_QK + (b * 2 + sec) * 8 + g, -1, arr + 2}, lane);
    STAMP(ws, 4, 2);
    store_tiles<MW * 3, 16>(rs, (int)(sec ? OFF_VT2 : OFF_VT1), lane,
                            [&](int i) { return tiled_blk(b * 48 + nt0 + i % 3, KK, gA - b * RT + m0 + i / 3); },
                            [&](int i) { return acc[i / 3][i % 3]; });
    pv = Pending{C_V + (b * 2 + sec) * 8 + (col & 7), -1, arr + 3};
    STAMP(ws, 4, 3);
  }
}

__device__ __forceinline__ void big_wg(LDS_AS char* smem, char* __restrict__ ws, const u16* __restrict__ wqkv_img,
                                       const u16* __restrict__ wo_img, const u16* __restrict__ wo_txt, int bid, int wave8, int lane) {
  const int x = bid & 7, idx = bid >> 3, b = x & 1, g = idx % 7, col = (x >> 1) * 4 + idx / 7;
  const int gA = b * RT + g * 9, grp = b * 7 + g, hb = (int)OFF_H + gA * KD * 1024;
  const int wi = idx < 24 ? idx : idx - 4, wmb = b * 6 + wi % 6, wcol = (x >> 1) * 4 + wi / 6;
  const P2Tile t2 = p2_tile(bid);
  const u32x4 srd_ws = make_srd(ws, WS_END);
  rsrc_t rs = make_rsrc(ws, WS_END);
  LDS_AS unsigned* arr = (LDS_AS unsigned*)(smem + ARR);
  if (wave8 == 7) {
    const int c2 = p2_counter(t2, lane);
    barriers(7);
    unsigned f = peek(srd_ws, C_H + grp);
    barriers(2);
    check(f, srd_ws, C_H + grp, 12);
    barriers(10);
    f = peek(srd_ws, C_W + col);
    barriers(2);
    check(f, srd_ws, C_W + col, 12);
    barriers(10);
    f = peek(srd_ws, c2);
    barriers(2);
    check(f, srd_ws, c2, 8);
    barriers(18);
    return;
  }
  if (wave8 >= 4) {
    const int l = wave8 - 4;
    rsrc_t rw = make_rsrc(wqkv_img, D * 3 * D * 2);
    rsrc_t ro = make_rsrc(wcol < 8 ? wo_img : wo_txt, D * D * 2);
    int bo[4], bw[4], av[3];
#pragma unroll
    for (int i = 0; i < 4; ++i) {
      bo[i] = (wcol & 7) * 192 + t96_src(l * 4 + i, lane, D * 2, wcol & 1);
      bw[i] = col * 192 + t96_src(l * 4 + i, lane, WP, col & 1);
    }
#pragma unroll
    for (int i = 0; i < 3; ++i) {
      const int u = min(l * 3 + i, 7), r = u * 8 + (lane >> 3);
      av[i] = (wmb * 64 + r) * WP + 2 * D * 2 + (((lane & 7) ^ ((r >> 1) & 7)) * 16);
    }
    const int wz = idx < 24 ? 0 : OOB;
    auto issueW = [&](int s, int slot) {
#pragma unroll
      for (int i = 0; i < 3; ++i) dma<0>(rw, smem + slot * SLOT + min(l * 3 + i, 7) * 1024, av[i], wz + s * 128);
#pragma unroll
      for (int i = 0; i < 4; ++i) dma<0>(ro, smem + slot * SLOT + 18432 + (l * 4 + i) * 1024, bo[i], wz + s * 64 * (D * 2));
    };
    auto issueA = [&](int s, int slot) {
#pragma unroll
      for (int i = 0; i < 3; ++i) {
        LDS_AS char* sa = smem + slot * SLOT + (l * 3 + i) * 2048;
        dma_sc1<0>(rs, sa, lane * 16, hb + ((l * 3 + i) * KD + 2 * s) * 1024);
        dma_sc1<1024>(rs, sa, lane * 16, hb + ((l * 3 + i) * KD + 2 * s) * 1024);
      }
    };
    issueW(0, 0);
#define LD_SLEEP 4
    __builtin_amdgcn_s_sleep(LD_SLEEP);
#pragma unroll
    for (int s = 1; s < 4; ++s) issueW(s, s);
    WAIT_VM(21); BARRIER();
    int slot = 4;
#pragma unroll 1
    for (int s = 4; s < 12; ++s) {
      WAIT_VM(14); BARRIER(); issueW(s, slot); slot = wrapn(slot + 1, 5);
    }
    stream12<7, 10>(slot, [&](int s, int sl) {
#pragma unroll
      for (int i = 0; i < 4; ++i) dma<0>(rw, smem + sl * SLOT + 18432 + (l * 4 + i) * 1024, bw[i], s * 64 * WP);
      issueA(s, sl);
    });
    stream12<10, 10>(slot, [&](int s, int sl) {
#pragma unroll
      for (int i = 0; i < 4; ++i) {
        const int pc = l * 4 + i;
        dma_sc1(rs, smem + sl * SLOT + 18432 + pc * 1024, lane * 16, (int)OFF_WVO + ((col * 6 + pc % 6) * KD + 2 * s + pc / 6) * 1024);
      }
      issueA(s, sl);
    });
    stream12<10, 8>(slot, [&](int s, int sl) { p2_issue(smem, rs, t2, l, lane, s, sl); });
    WAIT_VM(16); BARRIER();
    WAIT_VM(8); BARRIER();
    WAIT_VM(0); BARRIER();
    barriers(3);
    return;
  }
  const int c = wave8, wm = c >> 1, wn = c & 1;
  Pending ph{-1, -1, arr};
  if (col < 12) {
    const int W = col * 4 + c, ks = W >> 1, t0 = (W & 1) * 5;
    u32x4 xr[5], mp[4][KSPL];
#pragma unroll
    for (int i = 0; i < 4; ++i)
#pragma unroll
      for (int k = 0; k < KSPL; ++k)
        mp[i][k] = aload(srd_ws, (lane >> 4) * 32, (int)OFF_MOD + ((k * 4 + b) * 2304 + (i >> 1) * 768 + ks * 32 + (i & 1) * 4) * 4);
#pragma unroll
    for (int i = 0; i < 5; ++i) xr[i] = aload(srd_ws, lane * 16, (int)OFF_XN + ((gA + min(t0 + i, 8)) * KD + ks) * 1024);
    WAIT_VM(0);
    f32x4 gs[4];
#pragma unroll
    for (int i = 0; i < 4; ++i) {
      TIE(mp[i][0]); TIE(mp[i][1]);
      gs[i] = __builtin_bit_cast(f32x4, mp[i][0]) + __builtin_bit_cast(f32x4, mp[i][1]);
      if (i >= 2) gs[i] += 1.0f;
    }
#pragma unroll
    for (int i = 0; i < 5; ++i) {
      TIE(xr[i]);
      __builtin_amdgcn_raw_buffer_store_b128(modulate(xr[i], gs), rs, lane * 16, hb + (min(t0 + i, 8) * KD + ks) * 1024, 16);
    }
    ph.i0 = C_H + grp;
  }
  Pending pw{-1, -1, arr + 1};
  {
    const int sw = (lane & 15) >> 1, gq = lane >> 4;
    const unsigned ab = lds_addr(smem) + wm * 4096 + (lane & 15) * 128;
    unsigned brd[3];
#pragma unroll
    for (int n = 0; n < 3; ++n) brd[n] = lds_addr(smem) + 18432 + t96_rd(lane, wn * 3 + n, wcol & 1);
    f32x4 acc[2][3] = {};
    BARRIER();
    STAMP(ws, 5, 1);
    gemm12<2, 3, true, false, 0, 6144, SLOT, 0, 2>(acc, ab + ((gq ^ sw) * 16), ab + (((4 + gq) ^ sw) * 16), brd, 0, ws, ph, lane);
    STAMP(ws, 5, 2);
    if (idx < 24) {
      store_tiles<6, 16>(rs, (int)OFF_WVO, lane, [&](int i) { return tiled_blk(wcol * 6 + wn * 3 + i % 3, KD, wmb * 4 + wm * 2 + i / 3); },
                         [&](int i) { return acc[i / 3][i % 3]; });
      pw.i0 = C_W + wcol;
    }
    STAMP(ws, 5, 3);
  }
  Pending pv;
  if (wm == 0) big_p1<5>(smem, ws, gA, g, col, 0, wn, lane, arr, pw, pv);
  else big_p1<4>(smem, ws, gA, g, col, 5, wn, lane, arr, pw, pv);
  p2_mfma(smem, ws, t2, 1 * SLOT, c, lane, arr + 4, pv);
}

template <int MT, bool VT, bool FIRST>
__device__ __forceinline__ void small_p1(LDS_AS char* smem, char* __restrict__ ws, const u16* __restrict__ wsrc, int wcol, int obase,
                                         int gA, int col, int smod, int wave8, int lane, const Pending& pend) {
  constexpr int MH = MT / 2;
  const u32x4 srd_ws = make_srd(ws, WS_END);
  rsrc_t rs = make_rsrc(ws, WS_END);
  auto gtile = [&](int lt) { return lt < MH ? gA + lt : gA + RT + (lt - MH); };
  if (wave8 == 7) {
    if (FIRST) {
      u32x4 mp[12][KSPL];
#pragma unroll
      for (int h = 0; h < 12; ++h)
#pragma unroll
        for (int k = 0; k < KSPL; ++k) mp[h][k] = aload(srd_ws, lane * 16, (int)OFF_MOD + (((k * 2 + smod) * 2 + h / 6) * 2304 + (h % 6) * 256) * 4);
      WAIT_VM(0);
#pragma unroll
      for (int h = 0; h < 12; ++h) {
        TIE(mp[h][0]); TIE(mp[h][1]);
        f32x4 v = __builtin_bit_cast(f32x4, mp[h][0]) + __builtin_bit_cast(f32x4, mp[h][1]);
        if (h % 6 >= 3) v += 1.0f;
        *(LDS_AS f32x4*)(smem + P1_SGS + h * 1024 + lane * 16) = v;
      }
      WAIT_LGKM(0);
    }
    barriers(FIRST ? 15 : 14);
    return;
  }
  if (wave8 >= 4) {
    const int l = wave8 - 4;
    rsrc_t rb = VT ? rs : make_rsrc(wsrc, D * 3 * D * 2);
    int bv[4];
#pragma unroll
    for (int i = 0; i < 4; ++i) {
      const int pc = l * 4 + i;
      bv[i] = VT ? ((col * 6 + pc % 6) * KD + pc / 6) * 1024 + lane * 16 : wcol + t96_src(pc, lane, WP, col & 1);
    }
    if (VT) poll(srd_ws, C_W + col, 12);
    loader_loop<12, 5, 4, 0, FIRST>([&](int s, int slot) {
#pragma unroll
      for (int i = 0; i < 4; ++i) {
        if (VT) dma_sc1(rb, smem + P1_SB + slot * 12288 + (l * 4 + i) * 1024, bv[i], (int)OFF_WVO + s * 2048);
        else dma<0>(rb, smem + P1_SB + slot * 12288 + (l * 4 + i) * 1024, bv[i], s * 64 * WP);
      }
    });
    BARRIER();
    return;
  }
  const int c = wave8, wm = c >> 1, wn = c & 1;
  auto aoff = [&](int st) { return st * (MT * 2048); };
  if (FIRST) {
    u32x4 xr[6][MT];
#pragma unroll
    for (int k = 0; k < 6; ++k)
#pragma unroll
      for (int i = 0; i < MT; ++i) xr[k][i] = aload(srd_ws, lane * 16, (int)OFF_XN + (gtile(i) * KD + c * 6 + k) * 1024);
    BARRIER();
    WAIT_VM(0);
#pragma unroll
    for (int k = 0; k < 6; ++k) {
      f32x4 gs[2][4];
#pragma unroll
      for (int z = 0; z < 2; ++z) {
        const unsigned gp = lds_addr(smem) + P1_SGS + z * 6144 + (c * 6 + k) * 128 + (lane >> 4) * 32;
        rd_f4<0>(gs[z][0], gp); rd_f4<16>(gs[z][1], gp); rd_f4<3072>(gs[z][2], gp); rd_f4<3088>(gs[z][3], gp);
      }
      WAIT_LGKM(0);
#pragma unroll
      for (int z = 0; z < 2; ++z)
#pragma unroll
        for (int i = 0; i < 4; ++i) TIE(gs[z][i]);
#pragma unroll
      for (int i = 0; i < MT; ++i) {
        TIE(xr[k][i]);
        *(LDS_AS u32x4*)(smem + aoff(c * 3 + (k >> 1)) + i * 2048 + (k & 1) * 1024 + lane * 16) = modulate(xr[k][i], gs[i < MH ? 0 : 1]);
      }
    }
    WAIT_LGKM(0);
  }
  const unsigned aa = lds_addr(smem) + wm * MH * 2048 + lane * 16;
  unsigned brd[3];
#pragma unroll
  for (int n = 0; n < 3; ++n) brd[n] = lds_addr(smem) + P1_SB + (VT ? wn * 3072 + lane * 16 : t96_rd(lane, wn * 3 + n, col & 1));
  f32x4 acc[MH][3] = {};
  BARRIER();
  gemm12<MH, 3, !VT, !VT, 1024, 6144, 12288, MT * 2048, 4>(acc, aa, aa + 1024, brd, 0, ws, pend, lane);
  BARRIER();
  STAMP(ws, FIRST ? 1 : 4, 2);
  const int nt0 = (col & 7) * 6 + wn * 3;
  if (VT) store_tiles<MH * 3, 16>(rs, obase, lane, [&](int i) { return tiled_blk(wm * 48 + nt0 + i % 3, KK, gA + i / 3); },
                                  [&](int i) { return acc[i / 3][i % 3]; });
  else store_tiles<MH * 3, 16>(rs, obase, lane, [&](int i) { return tiled_blk((FIRST ? gA + wm * RT : wm * MH) + i / 3, KD, nt0 + i % 3); },
                               [&](int i) { return acc[i / 3][i % 3]; });
  STAMP(ws, FIRST ? 1 : 4, 3);
}

__device__ __forceinline__ void small_wg(LDS_AS char* smem, char* __restrict__ ws, const u16* __restrict__ wqkv_img,
                                         const u16* __restrict__ wqkv_txt, const u16* __restrict__ wo_img,
                                         const u16* __restrict__ wo_txt, int bid, int wave8, int lane) {
  const int x = bid & 7, idx = bid >> 3;
  const u32x4 srd_ws = make_srd(ws, WS_END);
  rsrc_t rs = make_rsrc(ws, WS_END);
  LDS_AS unsigned* arr = (LDS_AS unsigned*)(smem + ARR);
  Pending pv{-1, -1, arr + 1};
  if (bid < 240) {
    const int col = (x >> 1) * 4 + (x & 1) * 2 + (idx - 28), sec = col >> 3;
    small_p1<2, false, true>(smem, ws, wqkv_img, col * 192, (int)(sec ? OFF_K : OFF_Q), 63, col, 0, wave8, lane, Pending{-1, -1, arr});
    small_p1<2, true, false>(smem, ws, wqkv_img, 0, (int)(sec ? OFF_VT2 : OFF_VT1), 63, col, 0, wave8, lane,
                             Pending{C_QK + sec * 8 + 7, C_QK + 16 + sec * 8 + 7, arr});
    pv.i0 = C_V + sec * 8 + (col & 7); pv.i1 = pv.i0 + 16;
  } else {
    const int col = bid - 240;
    small_p1<4, false, true>(smem, ws, wqkv_txt, col * 192, (int)(col >> 3 ? OFF_K : OFF_Q), 64, col, 1, wave8, lane, Pending{-1, -1, arr});
    const Pending pq{C_T + (col >> 3), -1, arr};
    if (col < 8) {
      small_p1<4, false, false>(smem, ws, wqkv_txt, 3072 + col * 192, (int)OFF_VX, 64, col, 1, wave8, lane, pq);
      pv.i0 = C_T + 2;
    } else if (wave8 < 4) {
      flush(ws, pq, lane);
    }
  }
  if (wave8 < 4) flush(ws, pv, lane);
  if (bid < 242) {
    const P2Tile t2 = p2_tile(bid);
    if (wave8 == 7) barriers(15);
    else if (wave8 >= 4) {
      poll(srd_ws, p2_counter(t2, lane), 8);
      loader_loop<12, 5, 8>([&](int s, int slot) { p2_issue(smem, rs, t2, wave8 - 4, lane, s, slot); });
      barriers(2);
    } else {
      BARRIER();
      p2_mfma(smem, ws, t2, 0, wave8, lane, arr + 4, Pending{-1, -1, arr});
    }
  } else if (bid < 254) {
    const int e = bid - 242, wave = wave8 & 3;
    if (wave8 >= 4) {
      rsrc_t rb = make_rsrc(e < 6 ? wo_img : wo_txt, D * D * 2);
      const int bv0 = (e % 6) * 256 + wave * 16 * (D * 2) + timg_src(lane, 0, D * 2);
      const int bv1 = (e % 6) * 256 + (wave * 16 + 8) * (D * 2) + timg_src(lane, 1, D * 2);
      poll(srd_ws, C_T + 2, 8);
      loader_loop<12, 5, 6>([&](int s, int slot) {
        LDS_AS char* sb = smem + slot * SLOT;
        dma_sc1<0>(rs, sb + wave * 2048, lane * 16, (int)OFF_VX + (wave * KD + 2 * s) * 1024);
        dma_sc1<1024>(rs, sb + wave * 2048, lane * 16, (int)OFF_VX + (wave * KD + 2 * s) * 1024);
#pragma unroll
        for (int i = 0; i < 4; ++i) dma<0>(rb, sb + 8192 + (wave * 4 + i) * 1024, ((i >> 1) ? bv1 : bv0) + (i & 1) * 128, s * 64 * (D * 2));
      });
    } else {
      const int wm = wave >> 1, wn = wave & 1;
      const unsigned aa = lds_addr(smem) + wm * 4096 + lane * 16;
      unsigned brd[4];
#pragma unroll
      for (int n = 0; n < 4; ++n) brd[n] = lds_addr(smem) + 8192 + timg_rd<2>(lane, wn * 4 + n);
      f32x4 acc[2][4] = {};
      BARRIER();
      gemm12<2, 4, true, false, 0, 8192, SLOT, 0, FTX>(acc, aa, aa + 1024, brd, 0, ws, Pending{-1, -1, arr}, lane);
      STAMP(ws, 2, 2);
      store_tiles<8, 16>(rs, (int)(e < 6 ? OFF_VT1 : OFF_VT2), lane,
                         [&](int i) { return tiled_blk(wm * 48 + (e % 6) * 8 + wn * 4 + (i & 3), KK, 64 + (i >> 2)); },
                         [&](int i) { return acc[i >> 2][i & 3]; });
      if (last_wave(arr + 4, 4, lane)) signal(ws, C_X + e);
      STAMP(ws, 2, 3);
    }
  }
}

constexpr int K3_NS = 4, K3_STAGE = 33 * 1024, K3_F = K3_NS * K3_STAGE;
template <int QT>
__device__ __forceinline__ void k3_mfma(LDS_AS char* smem, char* __restrict__ ws, int wn, int qoff, int nb, int lane, u16* __restrict__ outp) {
  const unsigned vrd = lds_addr(smem) + wn * 9216 + lane * 16, prd = lds_addr(smem) + 18432 + qoff * 3072 + lane * 16;
  const unsigned fs = lds_addr(smem) + K3_F + (qoff * 16 + (lane & 15)) * 4;
  auto rdf = [&](float (&f)[QT], int t) {
    rd_32<0>(f[0], fs + t * 320);
    if constexpr (QT > 1) rd_32<64>(f[QT > 1 ? 1 : 0], fs + t * 320);
    if constexpr (QT > 2) rd_32<128>(f[QT > 2 ? 2 : 0], fs + t * 320);
  };
  f32x4 acc[3][QT] = {}, t0[3][QT], t1[3][QT] = {};
  Frags<3, QT> f0, f1, f2;
  unsigned bc[QT];
  bc[0] = prd;
  BARRIER();
  STAMP(ws, 3, 1);
  rd_set<3, QT, false, 3072, 3072, 0, 0>(f0, vrd, bc);
  rd_set<3, QT, false, 3072, 3072, 1024, 1024>(f1, vrd, bc);
  int so = 0;
  auto stage = [&](int t, f32x4 (&cur)[3][QT], f32x4 (&prv)[3][QT]) {
    BARRIER();
    float fp[QT];
    rdf(fp, t);
    kstep<3, QT, false, false, true, 3072, 3072, 2048, 2048, true>(cur, f0, f2, vrd + so, bc, NoExtra{});
    so = wrapn(so + K3_STAGE, K3_NS * K3_STAGE);
    bc[0] = prd + so;
    kstep<3, QT, false, false, false, 3072, 3072, 0, 0, true>(cur, f1, f0, vrd + so, bc, [&](auto ic) {
      constexpr int I = decltype(ic)::value;
      if constexpr (I < QT) tie1(fp[I]);
      acc[I / QT][I % QT] += prv[I / QT][I % QT] * fp[I % QT];
      tie4(acc[I / QT][I % QT]);
    });
    kstep<3, QT, false, false, false, 3072, 3072, 1024, 1024, true>(cur, f2, f1, vrd + so, bc, NoExtra{});
  };
#pragma unroll 1
  for (int t = 0; t < 10; t += 2) {
    stage(t, t0, t1);
    stage(t + 1, t1, t0);
  }
  stage(10, t0, t1);
  float fp[QT];
  rdf(fp, 11);
  land<3, QT, false>(f0);
  land<3, QT, false>(f1);
#pragma unroll
  for (int j = 0; j < QT; ++j) TIE(fp[j]);
  STAMP(ws, 3, 2);
  LDS_AS char* xs = smem + 3 * K3_STAGE;
  const int cw = wn * 48 + (lane >> 4) * 4;
  f32x4 o[QT * 3];
#pragma unroll
  for (int i = 0; i < 3; ++i) {
    const f32x4 gate = *(LDS_AS f32x4*)(xs + 15360 + (cw + i * 16) * 4) + *(LDS_AS f32x4*)(xs + 15360 + 384 + (cw + i * 16) * 4);
#pragma unroll
    for (int j = 0; j < QT; ++j) {
      const u32x2 xv = *(LDS_AS u32x2*)(xs + ((qoff + j) * 16 + (lane & 15)) * 192 + (cw + i * 16) * 2);
      const f32x4 a = acc[i][j] + t0[i][j] * fp[j];
      o[j * 3 + i] = f32x4{fmaf(gate[0], a[0], bflo(xv[0])), fmaf(gate[1], a[1], bfhi(xv[0])),
                           fmaf(gate[2], a[2], bflo(xv[1])), fmaf(gate[3], a[3], bfhi(xv[1]))};
    }
  }
  const bool odd = (lane >> 4) & 1;
  u16* ob = outp + (size_t)(qoff * 16 + (lane & 15)) * D + nb * 96 + wn * 48;
  auto toff = [&](int i) { return (i / 3) * 16 * D + (i % 3) * 16; };
#pragma unroll
  for (int i = 0; i + 1 < QT * 3; i += 2) *(u32x4*)(ob + (odd ? toff(i + 1) : toff(i)) + (lane >> 5) * 8) = pair16(o[i], o[i + 1]);
  if (QT & 1) *(u32x2*)(ob + toff(QT * 3 - 1) + (lane >> 4) * 4) = pack4(o[QT * 3 - 1]);
  STAMP(ws, 3, 3);
}

__device__ __forceinline__ void phase3(LDS_AS char* smem, char* __restrict__ ws, const u16* __restrict__ x_img,
                                       const u16* __restrict__ x_txt, u16* __restrict__ out, int bid, int wave8, int lane) {
  if (bid >= 224) return;
  const int wave = wave8 & 3, x = bid & 7, idx = bid >> 3, b = x & 1;
  int rt0, nrt, nb, txt;
  if (idx < 26) {
    const int u = (x >> 1) * 26 + idx, rb = (u % 52) >> 2;
    nb = (u / 52) * 4 + (u & 3); rt0 = rb * 5; nrt = rb == 12 ? 4 : 5; txt = 0;
  } else {
    nb = (x >> 1) * 2 + idx - 26; rt0 = 64; nrt = 2; txt = 1;
  }
  if (wave8 >= 4) {
    rsrc_t rs = make_rsrc(ws, WS_END);
    const u32x4 srd_ws = make_srd(ws, WS_END);
    auto tail = [&](int slot) {
      const u16* xres = txt ? x_txt + (size_t)b * NTX * D : x_img + (size_t)(b * NI + rt0 * 16) * D;
      rsrc_t rx = make_rsrc(xres, nrt * 16 * D * 2);
      LDS_AS char* sb = smem + slot * K3_STAGE + wave * 4096;
#pragma unroll
      for (int i = 0; i < 4; ++i) {
        const int c = (wave * 4 + i) * 64 + lane;
        if (wave * 4 + i < 15) dma<0>(rx, sb + i * 1024, (c / 12) * (D * 2) + nb * 192 + (c % 12) * 16, 0);
        else dma<0>(rs, sb + i * 1024, lane < 24 * KSPL ? (int)OFF_MOD + ((((lane / 24) * 2 + txt) * 2 + b) * 2304 + 1536 + nb * 96 + (lane % 24) * 4) * 4 : OOB, 0);
      }
    };
    const int cp = C_P + b * 11 + (rt0 * 16 + (lane & 1) * (nrt * 16 - 1)) / 96;
    if (wave < 2) {
      poll(srd_ws, C_V + (b * 2 + txt) * 8 + nb, 8);
      const int vb = (int)(txt ? OFF_VT2 : OFF_VT1) + (b * 48 + nb * 6 + wave * 3) * KK * 1024;
      const int ltid = wave * 64 + lane;
      u32x4 st[11];
      loader_loop<11, K3_NS, 9, 4>([&](int t, int slot) {
        if (t == 10) poll(srd_ws, C_X + txt * 6 + (nb * 96 + (lane & 1) * 95) / 128, 1);
        LDS_AS char* sb = smem + slot * K3_STAGE + wave * 9216;
#pragma unroll
        for (int i = 0; i < 3; ++i) {
          const int so = vb + i * KK * 1024 + t * 3072;
          dma_sc1<0>(rs, sb + i * 3072, lane * 16, so);
          dma_sc1<1024>(rs, sb + i * 3072, lane * 16, so);
          dma_sc1<2048>(rs, sb + i * 3072, lane * 16, so);
        }
      }, [&]() {
        WAIT_VM(0);
        float m[11];
#pragma unroll
        for (int t = 0; t < 11; ++t) { TIE(st[t]); m[t] = __uint_as_float(st[t][0]); }
        float M = m[0];
#pragma unroll
        for (int t = 1; t < 11; ++t) M = fmaxf(M, m[t]);
        float L = 0.f;
#pragma unroll
        for (int t = 0; t < 11; ++t) {
          m[t] = __builtin_amdgcn_exp2f(m[t] - M);
          L = fmaf(m[t], __uint_as_float(st[t][1]) + __uint_as_float(st[t][3]), L);
        }
        const float inv = 1.0f / L;
        LDS_AS float* fw = (LDS_AS float*)(smem + K3_F);
        if (ltid < 80) {
          fw[ltid] = 0.f;
#pragma unroll
          for (int t = 0; t < 11; ++t) fw[(1 + t) * 80 + ltid] = m[t] * inv;
        }
        WAIT_LGKM(0);
      }, tail, [&]() {
        poll(srd_ws, cp, 11);
        const int r = min(ltid, nrt * 16 - 1), vs = (int)OFF_ST + (b * RT + rt0 + (r >> 4)) * 2816 + (r & 15) * 16;
#pragma unroll
        for (int i = 0; i < 11; ++i) st[i] = aload_sc1(srd_ws, vs, i * 256);
      });
    } else {
      poll(srd_ws, cp, 11);
      const int pb = (int)OFF_P + (b * RT + rt0) * KK * 1024;
      loader_loop<11, K3_NS, 8, 4>([&](int t, int slot) {
#pragma unroll
        for (int i = 0; i < 8; ++i) {
          const int pc = (wave - 2) * 8 + i, u = pc / 3, ks = pc % 3;
          dma_sc1(rs, smem + slot * K3_STAGE + 18432 + min(pc, 14) * 1024, lane * 16,
                  u < nrt ? pb + (u * KK + t * 3 + ks) * 1024 : (pc == 15 ? pb + ((nrt - 1) * KK + t * 3 + 2) * 1024 + (nrt == 5 ? 0 : OOB) : OOB));
        }
      }, NoHook{}, tail);
    }
    return;
  }
  u16* outp = txt ? out + (size_t)2 * NI * D + (size_t)b * NTX * D : out + (size_t)(b * NI + rt0 * 16) * D;
  const int wq = wave >> 1, q0 = (nrt + 1) >> 1;
  const int qt = wq ? nrt - q0 : q0, qoff = wq ? q0 : 0;
  if (qt == 3) k3_mfma<3>(smem, ws, wave & 1, qoff, nb, lane, outp);
  else if (qt == 2) k3_mfma<2>(smem, ws, wave & 1, qoff, nb, lane, outp);
  else k3_mfma<1>(smem, ws, wave & 1, qoff, nb, lane, outp);
}

__global__ __launch_bounds__(512, 2) void kmain(const u16* __restrict__ x_img, const u16* __restrict__ x_txt,
                                                const u16* __restrict__ wqkv_img, const u16* __restrict__ wqkv_txt,
                                                const u16* __restrict__ wo_img, const u16* __restrict__ wo_txt,
                                                char* __restrict__ ws, u16* __restrict__ out) {
  __shared__ __attribute__((aligned(1024))) char smem_[MAIN_LDS];
  LDS_AS char* smem = (LDS_AS char*)smem_;
  const int bid = blockIdx.x, tid = threadIdx.x, lane = tid & 63;
  const int wave = __builtin_amdgcn_readfirstlane(tid >> 6);
  asm volatile(".Lmmdit_main_begin:");
  if (tid < 8) ((LDS_AS unsigned*)(smem + ARR))[tid] = 0u;
  WAIT_LGKM(0);
  STAMP(ws, 5, 0);
  if (bid < 224) {
    big_wg(smem, ws, wqkv_img, wo_img, wo_txt, bid, wave, lane);
    STAMP(ws, 3, 0);
    phase3(smem, ws, x_img, x_txt, out, bid, wave, lane);
  } else {
    small_wg(smem, ws, wqkv_img, wqkv_txt, wo_img, wo_txt, bid, wave, lane);
  }
  asm volatile(".Lmmdit_main_end:");
}

extern "C" void kernel_launch(void* const* d_in, const int* in_sizes, int n_in, void* d_out, int out_size, void* d_ws,
                              size_t ws_size, hipStream_t stream) {
  const u16* x_img = (const u16*)d_in[0];
  const u16* x_txt = (const u16*)d_in[1];
  const u16* cvec = (const u16*)d_in[2];
  const u16* wqkv_img = (const u16*)d_in[3];
  const u16* wqkv_txt = (const u16*)d_in[4];
  const u16* wo_img = (const u16*)d_in[5];
  const u16* wo_txt = (const u16*)d_in[6];
  const u16* wmod_img = (const u16*)d_in[7];
  const u16* wmod_txt = (const u16*)d_in[8];
  char* ws = (char*)d_ws;
  k0_prep<<<K0_T1 + K0_T2 + K0_Z + K0_PF, 256, 0, stream>>>(x_img, x_txt, cvec, wmod_img, wmod_txt, wqkv_img, wqkv_txt, wo_img, wo_txt, ws);
  kmain<<<256, 512, 0, stream>>>(x_img, x_txt, wqkv_img, wqkv_txt, wo_img, wo_txt, ws, (u16*)d_out);
}
```

```cpp
#include <hip/hip_runtime.h>
#include <hip/hip_bf16.h>
#include <stdint.h>
#include <type_traits>

typedef __attribute__((ext_vector_type(8))) __bf16 bf16x8;
typedef __attribute__((ext_vector_type(4))) __bf16 bf16x4;
typedef __attribute__((ext_vector_type(2))) __bf16 bf16x2;
typedef __attribute__((ext_vector_type(4))) float f32x4;
typedef __attribute__((ext_vector_type(2))) float f32x2;
typedef __attribute__((ext_vector_type(4))) unsigned u32x4;
typedef __attribute__((ext_vector_type(2))) unsigned u32x2;
#define LDS_AS __attribute__((address_space(3)))
typedef __amdgpu_buffer_rsrc_t rsrc_t;
typedef unsigned short u16;

constexpr int D = 768, NI = 1024, NTX = 32;
constexpr int RT = 66;
constexpr int KD = 24;
constexpr int KK = 33;
constexpr int KSPL = 2;
constexpr int OOB = 0x7f000000;

constexpr unsigned SZ_TOK = 2u * RT * KD * 1024u;
constexpr unsigned SZ_VT = 2u * 48u * KK * 1024u;
constexpr unsigned OFF_XN = 0;
constexpr unsigned OFF_Q = OFF_XN + SZ_TOK;
constexpr unsigned OFF_K = OFF_Q + SZ_TOK;
constexpr unsigned OFF_VX = OFF_K + SZ_TOK;
constexpr unsigned OFF_VT1 = OFF_VX + 4u * KD * 1024u;
constexpr unsigned OFF_VT2 = OFF_VT1 + SZ_VT;
constexpr unsigned OFF_P = OFF_VT2 + SZ_VT;
constexpr unsigned OFF_WVO = OFF_P + 2u * RT * KK * 1024u;
constexpr unsigned OFF_MOD = OFF_WVO + 96u * KD * 1024u;
constexpr unsigned OFF_ST = OFF_MOD + KSPL * 4u * 2304u * 4u;
constexpr unsigned OFF_CNT = OFF_ST + 2u * RT * 11u * 256u;
constexpr unsigned OFF_H = OFF_CNT + 20480u;
constexpr unsigned WS_END = OFF_H + SZ_TOK;
constexpr int C_H = 0;
constexpr int C_QK = 14;
constexpr int C_T = 46;
constexpr int C_V = 49;
constexpr int C_X = 81;
constexpr int C_P = 93;
constexpr int C_W = 115;
constexpr unsigned SZ_CNT = 20480u;

#define WAIT_VM(n) asm volatile("s_waitcnt vmcnt(%0)" ::"n"(n) : "memory")
#define WAIT_LGKM(n) asm volatile("s_waitcnt lgkmcnt(%0)" ::"n"(n) : "memory")
#define BARRIER() __builtin_amdgcn_s_barrier()
#define TIE(x) asm volatile("" : "+v"(x))
#define SCHED_FENCE() __builtin_amdgcn_sched_barrier(0)

template <int I, int N, class F>
__device__ __forceinline__ void static_for(F&& f) {
  if constexpr (I < N) { f(std::integral_constant<int, I>{}); static_for<I + 1, N>(f); }
}
__device__ __forceinline__ rsrc_t make_rsrc(const void* p, unsigned bytes) {
  return __builtin_amdgcn_make_buffer_rsrc((void*)p, 0, (int)bytes, 0x00020000);
}
__device__ __forceinline__ u32x4 make_srd(const void* p, unsigned bytes) {
  unsigned long long a = (unsigned long long)p;
  u32x4 r;
  r[0] = (unsigned)a; r[1] = (unsigned)(a >> 32) & 0xffffu; r[2] = bytes; r[3] = 0x00020000u;
  return r;
}
template <int IMM>
__device__ __forceinline__ void dma(rsrc_t rs, LDS_AS char* lds, int voff, int soff) {
  __builtin_amdgcn_raw_ptr_buffer_load_lds(rs, (LDS_AS void*)lds, 16, voff, soff, IMM, 0);
}
template <int IMM = 0>
__device__ __forceinline__ void dma_sc1(rsrc_t rs, LDS_AS char* lds, int voff, int soff) {
  __builtin_amdgcn_raw_ptr_buffer_load_lds(rs, (LDS_AS void*)lds, 16, voff, soff, IMM, 16);
}
__device__ __forceinline__ u32x4 aload(const u32x4& srd, int voff, int soff) {
  u32x4 r;
  asm volatile("buffer_load_dwordx4 %0, %1, %2, %3 offen" : "=v"(r) : "v"(voff), "s"(srd), "s"(soff));
  return r;
}
__device__ __forceinline__ u32x4 aload_sc1(const u32x4& srd, int voff, int soff) {
  u32x4 r;
  asm volatile("buffer_load_dwordx4 %0, %1, %2, %3 offen sc1" : "=v"(r) : "v"(voff), "s"(srd), "s"(soff));
  return r;
}
__device__ __forceinline__ void signal(char* ws, int idx) {
  asm volatile("global_atomic_add %0, %1, %2" ::"v"((int)OFF_CNT + idx * 128), "v"(1u), "s"(ws) : "memory");
}
__device__ __forceinline__ bool last_wave(LDS_AS unsigned* word, unsigned n, int lane) {
  WAIT_VM(0);
  unsigned old = 0;
  if (lane == 0) asm volatile("ds_add_rtn_u32 %0, %1, %2\n\ts_waitcnt lgkmcnt(0)" : "=v"(old) : "v"((unsigned)(size_t)word), "v"(1u) : "memory");
  return lane == 0 && old == n - 1;
}
struct Pending { int i0, i1; LDS_AS unsigned* arr; };
__device__ __forceinline__ void flush(char* ws, const Pending& p, int lane) {
  if (p.i0 >= 0 && last_wave(p.arr, 4, lane)) { signal(ws, p.i0); if (p.i1 >= 0) signal(ws, p.i1); }
}
__device__ __forceinline__ void poll(const u32x4& srd, int idx, unsigned target) {
#pragma unroll 1
  for (int spin = 0; spin < (1 << 20); ++spin) {
    unsigned f;
    asm volatile("buffer_load_dword %0, %1, %2, 0 offen sc1\n\ts_waitcnt vmcnt(0)" : "=v"(f) : "v"((int)OFF_CNT + idx * 128), "s"(srd) : "memory");
    if (__builtin_amdgcn_ballot_w64(f < target) == 0) break;
    __builtin_amdgcn_s_sleep(1);
  }
}
__device__ __forceinline__ unsigned peek(const u32x4& srd, int idx) {
  unsigned f;
  asm volatile("buffer_load_dword %0, %1, %2, 0 offen sc1" : "=v"(f) : "v"((int)OFF_CNT + idx * 128), "s"(srd) : "memory");
  return f;
}
__device__ __forceinline__ void check(unsigned f, const u32x4& srd, int idx, unsigned target) {
  WAIT_VM(0);
  TIE(f);
  if (__builtin_amdgcn_ballot_w64(f < target) != 0) poll(srd, idx, target);
}
__device__ __forceinline__ void barriers(int n) {
#pragma unroll 1
  for (int t = 0; t < n; ++t) BARRIER();
}
template <int OFF>
__device__ __forceinline__ void rd_tr(bf16x4& lo, bf16x4& hi, unsigned addr) {
  asm volatile("ds_read_b64_tr_b16 %0, %2 offset:%c3\n\tds_read_b64_tr_b16 %1, %2 offset:%c4"
               : "=&v"(lo), "=&v"(hi) : "v"(addr), "i"(OFF), "i"(OFF + 512));
}
template <int OFF>
__device__ __forceinline__ void rd_128(bf16x8& r, unsigned addr) {
  asm volatile("ds_read_b128 %0, %1 offset:%c2" : "=v"(r) : "v"(addr), "i"(OFF));
}
template <int OFF>
__device__ __forceinline__ void rd_32(float& r, unsigned addr) {
  asm volatile("ds_read_b32 %0, %1 offset:%c2" : "=v"(r) : "v"(addr), "i"(OFF));
}
__device__ __forceinline__ void tie1(float& x) { TIE(x); }
__device__ __forceinline__ void tie4(f32x4& x) { TIE(x); }
__device__ __forceinline__ void tie8(bf16x8& x) { TIE(x); }
__device__ __forceinline__ void tie4h(bf16x4& x) { TIE(x); }
__device__ __forceinline__ bf16x8 cat(bf16x4 lo, bf16x4 hi) { return __builtin_shufflevector(lo, hi, 0, 1, 2, 3, 4, 5, 6, 7); }
__device__ __forceinline__ unsigned lds_addr(LDS_AS char* p) { return (unsigned)(size_t)p; }
__device__ __forceinline__ int wrapn(int v, int n) { return v >= n ? v - n : v; }
__device__ __forceinline__ f32x4 mfma(bf16x8 a, bf16x8 b, f32x4 c) {
  return __builtin_amdgcn_mfma_f32_16x16x32_bf16(a, b, c, 0, 0, 0);
}
__device__ __forceinline__ unsigned pack2(float a, float b) {
  f32x2 f = {a, b};
  bf16x2 h = __builtin_convertvector(f, bf16x2);
  return __builtin_bit_cast(unsigned, h);
}
__device__ __forceinline__ u32x2 pack4(f32x4 v) { return u32x2{pack2(v[0], v[1]), pack2(v[2], v[3])}; }
__device__ __forceinline__ float bflo(unsigned v) { return __uint_as_float(v << 16); }
__device__ __forceinline__ float bfhi(unsigned v) { return __uint_as_float(v & 0xffff0000u); }
__device__ __forceinline__ float max2(float a, float b) { float r; asm("v_max_f32 %0, %1, %2" : "=v"(r) : "v"(a), "v"(b)); return r; }
__device__ __forceinline__ float max3(float a, float b, float c) { float r; asm("v_max3_f32 %0, %1, %2, %3" : "=v"(r) : "v"(a), "v"(b), "v"(c)); return r; }
__device__ __forceinline__ float red4_max(float x) {
  auto a = __builtin_amdgcn_permlane16_swap(__float_as_uint(x), __float_as_uint(x), false, false);
  x = max2(__uint_as_float(a[0]), __uint_as_float(a[1]));
  auto b = __builtin_amdgcn_permlane32_swap(__float_as_uint(x), __float_as_uint(x), false, false);
  return max2(__uint_as_float(b[0]), __uint_as_float(b[1]));
}
__device__ __forceinline__ float red4_sum(float x) {
  auto a = __builtin_amdgcn_permlane16_swap(__float_as_uint(x), __float_as_uint(x), false, false);
  x = __uint_as_float(a[0]) + __uint_as_float(a[1]);
  auto b = __builtin_amdgcn_permlane32_swap(__float_as_uint(x), __float_as_uint(x), false, false);
  return __uint_as_float(b[0]) + __uint_as_float(b[1]);
}
__device__ __forceinline__ unsigned tiled_lane(int lane) {
  int g = lane >> 4;
  return (unsigned)(((lane & 15) + 16 * (g >> 1)) * 16 + (g & 1) * 8);
}
__device__ __forceinline__ unsigned tiled_blk(int rowtile, int KS, int T16) {
  return (unsigned)((rowtile * KS + (T16 >> 1)) * 1024 + (T16 & 1) * 512);
}
__device__ __forceinline__ u32x4 pair16(f32x4 x, f32x4 y) {
  const u32x2 px = pack4(x), py = pack4(y);
  auto a = __builtin_amdgcn_permlane16_swap(px[0], py[0], false, false);
  auto b = __builtin_amdgcn_permlane16_swap(px[1], py[1], false, false);
  return u32x4{a[0], b[0], a[1], b[1]};
}
__device__ __forceinline__ unsigned tiled_lane16(int lane) { return (unsigned)(((lane & 15) + 16 * (lane >> 5)) * 16); }
template <int NTILE, int AUX, class Blk, class Val>
__device__ __forceinline__ void store_tiles(rsrc_t rs, int base, int lane, Blk&& blk, Val&& val) {
  const bool odd = (lane >> 4) & 1;
  const int l16 = (int)tiled_lane16(lane);
#pragma unroll
  for (int i = 0; i + 1 < NTILE; i += 2) {
    const int so = __builtin_amdgcn_readfirstlane(base - 0x100000 + (int)blk(i));
    const int d = __builtin_amdgcn_readfirstlane((int)(blk(i + 1) - blk(i)));
    __builtin_amdgcn_raw_buffer_store_b128(pair16(val(i), val(i + 1)), rs, l16 + 0x100000 + (odd ? d : 0), so, AUX);
  }
  if (NTILE & 1)
    __builtin_amdgcn_raw_buffer_store_b64(pack4(val(NTILE - 1)), rs, (int)tiled_lane(lane),
                                          __builtin_amdgcn_readfirstlane(base + (int)blk(NTILE - 1)), AUX);
}
__device__ __forceinline__ int timg_src(int lane, int sigma, int pitch) {
  int r = lane >> 3, c = (lane & 7) ^ (2 * ((r >> 1) & 1) + 4 * sigma);
  return r * pitch + c * 16;
}
template <int NB>
__device__ __forceinline__ int timg_rd(int lane, int nt) {
  int g = lane >> 4, q = (lane & 15) >> 2, p = lane & 3;
  int f = 2 * ((q >> 1) & 1) + 4 * (g & 1);
  return (g * NB + (nt >> 2)) * 1024 + q * 128 + (((2 * (nt & 3)) ^ f) + (p >> 1)) * 16 + (p & 1) * 8;
}

template <int MT, int NT>
struct Frags { bf16x8 a[MT]; bf16x8 b[NT]; bf16x4 l[NT], h[NT]; };

template <int MT, int NT, bool BTR, int AM, int BM, int AO, int BO, int I>
__device__ __forceinline__ void rd_item(Frags<MT, NT>& n, unsigned ab, const unsigned (&bb)[NT]) {
  if constexpr (I < NT) {
    if constexpr (BTR) rd_tr<BO>(n.l[I], n.h[I], bb[I]);
    else rd_128<BO + I * BM>(n.b[I], bb[0]);
  } else if constexpr (I < NT + MT) {
    rd_128<AO + (I - NT) * AM>(n.a[I - NT], ab);
  }
}
template <int MT, int NT, bool BTR, int AM, int BM, int AO, int BO>
__device__ __forceinline__ void rd_set(Frags<MT, NT>& n, unsigned ab, const unsigned (&bb)[NT]) {
  static_for<0, MT + NT>([&](auto ic) { rd_item<MT, NT, BTR, AM, BM, AO, BO, decltype(ic)::value>(n, ab, bb); });
}
template <int MT, int NT, bool BTR, bool AH = false>
__device__ __forceinline__ void land(Frags<MT, NT>& c) {
  WAIT_LGKM(AH ? MT + (BTR ? 2 : 1) * NT : 0);
#pragma unroll
  for (int j = 0; j < NT; ++j) {
    if constexpr (BTR) { TIE(c.l[j]); TIE(c.h[j]); } else TIE(c.b[j]);
  }
#pragma unroll
  for (int m = 0; m < MT; ++m) TIE(c.a[m]);
  SCHED_FENCE();
}
template <int MT, int NT, bool BTR, bool SWAP, bool ZC, int AM, int BM, int AO, int BO, bool AH = false, class X>
__device__ __forceinline__ void kstep(f32x4 (&acc)[MT][NT], Frags<MT, NT>& c, Frags<MT, NT>& n, unsigned ab,
                                      const unsigned (&bb)[NT], X&& extra) {
  land<MT, NT, BTR, AH>(c);
  static_for<0, MT * NT>([&](auto ic) {
    constexpr int I = decltype(ic)::value, m = I / NT, j = I % NT;
    bf16x8 bf;
    if constexpr (BTR) bf = cat(c.l[j], c.h[j]); else bf = c.b[j];
    const f32x4 ci = ZC ? f32x4{0.f, 0.f, 0.f, 0.f} : acc[m][j];
    acc[m][j] = SWAP ? mfma(bf, c.a[m], ci) : mfma(c.a[m], bf, ci);
    tie4(acc[m][j]);
    rd_item<MT, NT, BTR, AM, BM, AO, BO, I>(n, ab, bb);
    extra(ic);
    SCHED_FENCE();
  });
  static_for<MT * NT, (MT + NT > MT * NT ? MT + NT : MT * NT)>(
      [&](auto ic) { rd_item<MT, NT, BTR, AM, BM, AO, BO, decltype(ic)::value>(n, ab, bb); });
}
struct NoExtra { template <class T> __device__ __forceinline__ void operator()(T) const {} };

struct NoHook { __device__ __forceinline__ void operator()() const {} };
struct NoTail { __device__ __forceinline__ void operator()(int) const {} };
template <int NST, int NS, int V, int TV = 0, bool XB = false, class Issue, class Hook = NoHook, class Tail = NoTail, class Pre = NoHook>
__device__ __forceinline__ void loader_loop(Issue&& issue, Hook&& hook = NoHook{}, Tail&& tail = NoTail{}, Pre&& pre = NoHook{}) {
#pragma unroll
  for (int s = 0; s < NS - 1; ++s) issue(s, s);
  if (XB) BARRIER();
  pre();
  WAIT_VM(V * (NS - 2));
  BARRIER();
  hook();
  int slot = NS - 1;
#pragma unroll 1
  for (int t = 0; t <= NST - NS; ++t) {
    WAIT_VM(V * (NS - 3));
    BARRIER();
    issue(t + NS - 1, slot);
    slot = wrapn(slot + 1, NS);
  }
  static_for<1, NS>([&](auto rc) {
    constexpr int R = decltype(rc)::value;
    WAIT_VM(R == NS - 1 ? 0 : V * (NS - 2 - R) + (R > 1 ? TV : 0));
    BARRIER();
    if constexpr (R == 1) tail(slot);
  });
}

template <int VP, int V, class Issue>
__device__ __forceinline__ void stream12(int& slot, Issue&& issue) {
  WAIT_VM(2 * VP); BARRIER(); issue(0, slot); slot = wrapn(slot + 1, 5);
  WAIT_VM(VP + V); BARRIER(); issue(1, slot); slot = wrapn(slot + 1, 5);
#pragma unroll 1
  for (int t = 2; t < 12; ++t) {
    WAIT_VM(2 * V); BARRIER(); issue(t, slot); slot = wrapn(slot + 1, 5);
  }
}

template <int MW, int NW, bool BTR, bool SWAP, int BM, int BKS, int SLOT, int ASTEP, int FT>
__device__ __forceinline__ void gemm12(f32x4 (&acc)[MW][NW], unsigned a0, unsigned a1, const unsigned (&brd)[NW], int so,
                                       char* __restrict__ ws, const Pending& pend, int lane) {
  Frags<MW, NW> x0, x1, y0, y1;
  unsigned bc[NW];
#pragma unroll
  for (int n = 0; n < NW; ++n) bc[n] = brd[n] + so;
  rd_set<MW, NW, BTR, 2048, BM, 0, 0>(x0, ASTEP ? a0 : a0 + so, bc);
  rd_set<MW, NW, BTR, 2048, BM, 0, BKS>(x1, ASTEP ? a1 : a1 + so, bc);
  auto stage = [&](Frags<MW, NW>& c0, Frags<MW, NW>& c1, Frags<MW, NW>& n0, Frags<MW, NW>& n1) {
    so = wrapn(so + SLOT, 5 * SLOT);
    a0 += ASTEP; a1 += ASTEP;
#pragma unroll
    for (int n = 0; n < NW; ++n) bc[n] = brd[n] + so;
    BARRIER();
    kstep<MW, NW, BTR, SWAP, false, 2048, BM, 0, 0, true>(acc, c0, n0, ASTEP ? a0 : a0 + so, bc, NoExtra{});
    kstep<MW, NW, BTR, SWAP, false, 2048, BM, 0, BKS, true>(acc, c1, n1, ASTEP ? a1 : a1 + so, bc, NoExtra{});
  };
#pragma unroll 1
  for (int t = 0; t < 12; t += 2) {
    if (t == FT) flush(ws, pend, lane);
    stage(x0, x1, y0, y1);
    stage(y0, y1, x0, x1);
  }
  land<MW, NW, BTR>(x0);
  land<MW, NW, BTR>(x1);
}
__device__ __forceinline__ int t96_src(int pc, int lane, int pitch, int odd) {
  const int ks = pc / 6, r = pc % 6, g1 = (lane >> 4) & 1, r16 = 8 * g1 + 4 * (lane >> 5) + ((lane >> 2) & 3);
  return r < 4 ? (ks * 32 + r * 8) * pitch + odd * 64 + timg_src(lane, r & 1, pitch)
               : (ks * 32 + (r - 4) * 16 + r16) * pitch + (odd ? 0 : 128) + (((lane & 3) ^ (2 * g1)) * 16);
}
__device__ __forceinline__ int t96_rd(int lane, int nt, int odd) {
  const int g = lane >> 4, q = (lane & 15) >> 2, p = lane & 3;
  const bool narrow = odd ? nt < 2 : nt >= 4;
  return narrow ? 4096 + (g >> 1) * 1024 + (g & 1) * 256 + q * 64 + (((2 * (nt & 1) + (p >> 1)) ^ (2 * (g & 1))) * 16) + (p & 1) * 8
                : timg_rd<1>(lane, odd ? nt - 2 : nt);
}

constexpr int K0_T1 = 72 * KSPL, K0_T2 = 2 * RT, K0_Z = 8, K0_PF = 144;
__global__ __launch_bounds__(256) void k0_prep(const u16* __restrict__ x_img, const u16* __restrict__ x_txt,
                                               const u16* __restrict__ cvec, const u16* __restrict__ wmod_img,
                                               const u16* __restrict__ wmod_txt, const u16* __restrict__ wqkv_img,
                                               const u16* __restrict__ wqkv_txt, const u16* __restrict__ wo_img,
                                               const u16* __restrict__ wo_txt, char* __restrict__ ws) {
  __shared__ __attribute__((aligned(1024))) char smem_[24576];
  LDS_AS char* smem = (LDS_AS char*)smem_;
  const int bid = blockIdx.x, tid = threadIdx.x, lane = tid & 63;
  const int wave = __builtin_amdgcn_readfirstlane(tid >> 6);
  if (bid >= K0_T1 + K0_T2 + K0_Z) {
    const int i = bid - (K0_T1 + K0_T2 + K0_Z);
    const char* p = i < 18 ? (const char*)wo_img + i * 65536 : (i < 36 ? (const char*)wo_txt + (i - 18) * 65536
                  : (i < 90 ? (const char*)wqkv_img + (i - 36) * 65536 : (const char*)wqkv_txt + (i - 90) * 65536));
    u32x4 v[16];
#pragma unroll
    for (int k = 0; k < 16; ++k) v[k] = *(const u32x4*)(p + k * 4096 + tid * 16);
#pragma unroll
    for (int k = 0; k < 16; ++k) asm volatile("" ::"v"(v[k]));
    return;
  } else if (bid >= K0_T1 + K0_T2) {
    for (unsigned o = ((bid - K0_T1 - K0_T2) * 256 + tid) * 16; o < SZ_CNT; o += K0_Z * 4096) *(u32x4*)(ws + OFF_CNT + o) = u32x4{0u, 0u, 0u, 0u};
    unsigned long long pc;
    int delta, len;
    asm volatile("s_getpc_b64 %0\n.Lmmdit_k0_pc:\n\ts_mov_b32 %1, .Lmmdit_main_begin-.Lmmdit_k0_pc\n\ts_mov_b32 %2, .Lmmdit_main_end-.Lmmdit_main_begin"
                 : "=s"(pc), "=s"(delta), "=s"(len));
    pc += delta;
    unsigned acc = 0;
    for (int o = tid * 128; o < len; o += 256 * 128) acc ^= *(const unsigned*)((const char*)pc + o);
    asm volatile("" ::"v"(acc));
    return;
  } else if (bid < K0_T1) {
    constexpr int ROWS = D / KSPL, PER = ROWS / 32;
    const int i = bid, s = i / (36 * KSPL), rem = i % (36 * KSPL), cb = rem % 36, kh = rem / 36;
    const u16* w = (s ? wmod_txt : wmod_img) + (size_t)(kh * ROWS + (tid >> 3)) * (6 * D) + cb * 64 + (tid & 7) * 8;
    u16 cv[3];
#pragma unroll
    for (int e = 0; e < 3; ++e) { const int z = tid + e * 256; cv[e] = cvec[(z / ROWS) * D + kh * ROWS + z % ROWS]; }
    u32x4 wv[PER];
#pragma unroll
    for (int r = 0; r < PER; ++r) wv[r] = *(const u32x4*)(w + (size_t)r * 32 * (6 * D));
    LDS_AS float* sc = (LDS_AS float*)smem;
    LDS_AS float* red = (LDS_AS float*)(smem + 8192);
#pragma unroll
    for (int e = 0; e < 3; ++e) {
      const int z = tid + e * 256;
      const float v = __uint_as_float((unsigned)cv[e] << 16);
      sc[(z % ROWS) * 2 + z / ROWS] = v / (1.0f + __expf(-v));
    }
    __syncthreads();
    float a0[8] = {}, a1[8] = {};
#pragma unroll
    for (int r = 0; r < PER; ++r) {
      f32x2 sv = *(LDS_AS f32x2*)(sc + ((tid >> 3) + r * 32) * 2);
#pragma unroll
      for (int j = 0; j < 4; ++j) {
        float lo = bflo(wv[r][j]), hi = bfhi(wv[r][j]);
        a0[2 * j] = fmaf(sv[0], lo, a0[2 * j]); a0[2 * j + 1] = fmaf(sv[0], hi, a0[2 * j + 1]);
        a1[2 * j] = fmaf(sv[1], lo, a1[2 * j]); a1[2 * j + 1] = fmaf(sv[1], hi, a1[2 * j + 1]);
      }
    }
    LDS_AS float* rp = red + (tid >> 3) * 128 + (tid & 7) * 8;
    *(LDS_AS f32x4*)(rp) = f32x4{a0[0], a0[1], a0[2], a0[3]};
    *(LDS_AS f32x4*)(rp + 4) = f32x4{a0[4], a0[5], a0[6], a0[7]};
    *(LDS_AS f32x4*)(rp + 64) = f32x4{a1[0], a1[1], a1[2], a1[3]};
    *(LDS_AS f32x4*)(rp + 68) = f32x4{a1[4], a1[5], a1[6], a1[7]};
    __syncthreads();
    {
      const int o = tid >> 1, hf = tid & 1;
      float t0 = 0.f, t1 = 0.f;
#pragma unroll
      for (int g = 0; g < 16; g += 2) { t0 += red[(hf * 16 + g) * 128 + o]; t1 += red[(hf * 16 + g + 1) * 128 + o]; }
      float t = t0 + t1;
      t += __shfl_xor(t, 1);
      float* mod = (float*)(ws + OFF_MOD);
      if (hf == 0) mod[((kh * 2 + s) * 2 + (o >> 6)) * 2304 + cb * 64 + (o & 63)] = t;
    }
  } else {
    const int i = bid - K0_T1, b = i / RT, rt = i % RT;
    const u16* src = rt < 64 ? x_img + (size_t)(b * NI + rt * 16) * D : x_txt + (size_t)(b * NTX + (rt - 64) * 16) * D;
    src += (lane & 15) * D + wave * 192 + (lane >> 4) * 8;
    u32x4 v[6];
#pragma unroll
    for (int k = 0; k < 6; ++k) v[k] = *(const u32x4*)(src + k * 32);
    LDS_AS float* red = (LDS_AS float*)smem;
    float s0 = 0.f, s1 = 0.f;
#pragma unroll
    for (int k = 0; k < 6; ++k)
#pragma unroll
      for (int j = 0; j < 4; ++j) { s0 += bflo(v[k][j]); s1 += bfhi(v[k][j]); }
    const float s = red4_sum(s0 + s1);
    if (lane < 16) red[wave * 16 + lane] = s;
    __syncthreads();
    const int r = lane & 15;
    const float mean = ((red[r] + red[16 + r]) + (red[32 + r] + red[48 + r])) * (1.0f / D);
    float q0 = 0.f, q1 = 0.f;
#pragma unroll
    for (int k = 0; k < 6; ++k)
#pragma unroll
      for (int j = 0; j < 4; ++j) {
        float lo = bflo(v[k][j]) - mean, hi = bfhi(v[k][j]) - mean;
        q0 = fmaf(lo, lo, q0); q1 = fmaf(hi, hi, q1);
      }
    const float q = red4_sum(q0 + q1);
    if (lane < 16) red[64 + wave * 16 + lane] = q;
    __syncthreads();
    const float var = ((red[64 + r] + red[80 + r]) + (red[96 + r] + red[112 + r])) * (1.0f / D);
    const float rstd = 1.0f / sqrtf(var + 1e-5f);
    char* dst = ws + OFF_XN + (size_t)(i * KD + wave * 6) * 1024 + lane * 16;
#pragma unroll
    for (int k = 0; k < 6; ++k) {
      u32x4 o;
#pragma unroll
      for (int j = 0; j < 4; ++j) {
        typedef __attribute__((ext_vector_type(2))) _Float16 f16x2;
        const f16x2 h = {(_Float16)((bflo(v[k][j]) - mean) * rstd), (_Float16)((bfhi(v[k][j]) - mean) * rstd)};
        o[j] = __builtin_bit_cast(unsigned, h);
      }
      *(u32x4*)(dst + k * 1024) = o;
    }
  }
}

constexpr int FTX = 2;
constexpr int SLOT = 30 * 1024;
constexpr int P1_SB = 98304, P1_SGS = P1_SB + 4 * 12288;
constexpr int RED = P1_SB + 5 * 12288, ARR = RED + 1024, MAIN_LDS = ARR + 32;
constexpr int WP = 3 * D * 2;

__device__ __forceinline__ void fmix_lo(float& d, unsigned v, float g, float s) {
  asm volatile("v_fma_mix_f32 %0, %1, %2, %3 op_sel_hi:[1,0,0]" : "=v"(d) : "v"(v), "v"(g), "v"(s));
}
__device__ __forceinline__ void fmix_hi(float& d, unsigned v, float g, float s) {
  asm volatile("v_fma_mix_f32 %0, %1, %2, %3 op_sel:[1,0,0] op_sel_hi:[1,0,0]" : "=v"(d) : "v"(v), "v"(g), "v"(s));
}
template <int OFF>
__device__ __forceinline__ void rd_f4(f32x4& r, unsigned addr) {
  asm volatile("ds_read_b128 %0, %1 offset:%c2" : "=v"(r) : "v"(addr), "i"(OFF));
}
__device__ __forceinline__ u32x4 modulate(u32x4 x, const f32x4 (&g)[4]) {
  float t[8];
#pragma unroll
  for (int d = 0; d < 4; ++d) {
    fmix_lo(t[2 * d], x[d], g[2 + (d >> 1)][(d & 1) * 2], g[d >> 1][(d & 1) * 2]);
    fmix_hi(t[2 * d + 1], x[d], g[2 + (d >> 1)][(d & 1) * 2 + 1], g[d >> 1][(d & 1) * 2 + 1]);
  }
  return u32x4{pack2(t[0], t[1]), pack2(t[2], t[3]), pack2(t[4], t[5]), pack2(t[6], t[7])};
}

struct P2Tile { int b, ti, tj; };
__device__ __forceinline__ P2Tile p2_tile(int bid) {
  const int x = bid & 7, idx = bid >> 3, qd = x >> 1;
  P2Tile t;
  t.b = x & 1;
  if (idx == 30) { t.ti = 5; t.tj = 5; }
  else if (qd == 0) { t.ti = idx / 6; t.tj = idx % 6; }
  else if (qd == 1) { t.ti = idx / 5; t.tj = 6 + idx % 5; }
  else if (qd == 2) { t.ti = 6 + idx / 6; t.tj = 5 + idx % 6; }
  else { t.ti = 5 + idx / 5; t.tj = idx % 5; }
  return t;
}
__device__ __forceinline__ int p2_counter(const P2Tile& t, int lane) {
  const int u = min(lane, 11), sec = u < 6 ? 1 : 0, rt = (u < 6 ? t.tj * 6 + u : t.ti * 6 + u - 6);
  return rt < 64 ? C_QK + (t.b * 2 + sec) * 8 + rt / 9 : C_T + sec;
}
__device__ __forceinline__ void p2_issue(LDS_AS char* smem, rsrc_t rs, const P2Tile& t, int l, int lane, int s, int slot) {
#pragma unroll
  for (int i = 0; i < 4; ++i) {
    const int u = l * 4 + i;
    const int so = (u < 6 ? (int)OFF_K + (t.b * RT + t.tj * 6 + u) * KD * 1024 : (int)OFF_Q + (t.b * RT + t.ti * 6 + u - 6) * KD * 1024) + s * 2048;
    dma_sc1<0>(rs, smem + slot * SLOT + u * 2048, lane * 16, so);
    dma_sc1<1024>(rs, smem + slot * SLOT + u * 2048, lane * 16, so);
  }
}
__device__ __forceinline__ void p2_mfma(LDS_AS char* smem, char* __restrict__ ws, const P2Tile& t, int so, int wave, int lane,
                                        LDS_AS unsigned* arr, const Pending& pend) {
  rsrc_t rs = make_rsrc(ws, WS_END);
  const int wk = wave >> 1, wq = wave & 1, b = t.b, ti = t.ti, tj = t.tj;
  const unsigned krd = lds_addr(smem) + wk * 6144 + lane * 16;
  const unsigned qrd[3] = {lds_addr(smem) + 12288 + wq * 6144 + lane * 16, 0, 0};
  const int g = lane >> 4;
  int pso[3];
#pragma unroll
  for (int j = 0; j < 3; ++j) pso[j] = (int)OFF_P + (b * RT + ti * 6 + wq * 3 + j) * KK * 1024 + (tj * 6 + wk * 3) * 512;
  int po16 = (int)tiled_lane16(lane) + (g & 1) * 512, po8 = (int)tiled_lane(lane) + 1024;
  int sso = (int)OFF_ST + ((b * RT + ti * 6 + wq * 3) * 11 + tj) * 256 + wk * 8, svo = g < 3 ? g * 2816 + (lane & 15) * 16 : OOB;
  unsigned rw = lds_addr(smem) + RED + (wk * 64 + lane) * 4 + wq * 512, rr = lds_addr(smem) + RED + ((wk ^ 1) * 64 + (lane & 15)) * 4 + wq * 512;
  asm volatile("" : "+s"(pso[0]), "+s"(pso[1]), "+s"(pso[2]), "+s"(sso), "+v"(po16), "+v"(po8), "+v"(svo), "+v"(rw), "+v"(rr));
  f32x4 acc[3][3] = {};
  gemm12<3, 3, false, false, 2048, 1024, SLOT, 0, FTX>(acc, krd, krd + 1024, qrd, so, ws, pend, lane);
  asm volatile("s_nop 7");
  SCHED_FENCE();
  constexpr float SC = 0.036084391824351615f * 1.4426950408889634f;
  float mx[3], ls[3];
#pragma unroll
  for (int j = 0; j < 3; ++j) {
    const float m0 = max3(acc[0][j][0], acc[0][j][1], acc[0][j][2]), m1 = max3(acc[0][j][3], acc[1][j][0], acc[1][j][1]);
    const float m2 = max3(acc[1][j][2], acc[1][j][3], acc[2][j][0]), m3 = max3(acc[2][j][1], acc[2][j][2], acc[2][j][3]);
    mx[j] = red4_max(max2(max3(m0, m1, m2), m3));
  }
  {
    const float mine = g == 0 ? mx[0] : (g == 1 ? mx[1] : mx[2]);
    asm volatile("ds_write_b32 %0, %1\n\ts_waitcnt lgkmcnt(0)" ::"v"(rw), "v"(mine) : "memory");
    BARRIER();
    float o0, o1, o2;
    asm volatile("ds_read_b32 %0, %3\n\tds_read_b32 %1, %3 offset:64\n\tds_read_b32 %2, %3 offset:128\n\ts_waitcnt lgkmcnt(0)"
                 : "=&v"(o0), "=&v"(o1), "=&v"(o2) : "v"(rr) : "memory");
    mx[0] = max2(mx[0], o0) * SC; mx[1] = max2(mx[1], o1) * SC; mx[2] = max2(mx[2], o2) * SC;
  }
#pragma unroll
  for (int j = 0; j < 3; ++j) {
#pragma unroll
    for (int i = 0; i < 3; ++i)
#pragma unroll
      for (int r = 0; r < 4; ++r) acc[i][j][r] = __builtin_amdgcn_exp2f(fmaf(acc[i][j][r], SC, -mx[j]));
    __builtin_amdgcn_raw_buffer_store_b128(pair16(acc[0][j], acc[1][j]), rs, po16, pso[j], 16);
    __builtin_amdgcn_raw_buffer_store_b64(pack4(acc[2][j]), rs, po8, pso[j], 16);
    const f32x4 t4 = (acc[0][j] + acc[1][j]) + acc[2][j];
    ls[j] = red4_sum((t4[0] + t4[1]) + (t4[2] + t4[3]));
  }
  {
    const u32x2 v = {__float_as_uint(g == 0 ? mx[0] : (g == 1 ? mx[1] : mx[2])), __float_as_uint(g == 0 ? ls[0] : (g == 1 ? ls[1] : ls[2]))};
    __builtin_amdgcn_raw_buffer_store_b64(v, rs, svo, sso, 16);
  }
  BARRIER();
  if (last_wave(arr, 4, lane)) signal(ws, C_P + b * 11 + ti);
}

template <int MW>
__device__ __forceinline__ void big_p1(LDS_AS char* smem, char* __restrict__ ws, int gA, int g, int col, int m0, int wn, int lane,
                                       LDS_AS unsigned* arr, const Pending& pw, Pending& pv) {
  rsrc_t rs = make_rsrc(ws, WS_END);
  const unsigned l0 = lds_addr(smem), aa = l0 + m0 * 2048 + lane * 16;
  const int b = gA >= RT ? 1 : 0, sec = col >> 3, nt0 = (col & 7) * 6 + wn * 3;
  {
    unsigned brd[3];
#pragma unroll
    for (int n = 0; n < 3; ++n) brd[n] = l0 + 18432 + t96_rd(lane, wn * 3 + n, col & 1);
    f32x4 acc[MW][3] = {};
    gemm12<MW, 3, true, true, 0, 6144, SLOT, 0, FTX>(acc, aa, aa + 1024, brd, 2 * SLOT, ws, pw, lane);
    store_tiles<MW * 3, 16>(rs, (int)(sec ? OFF_K : OFF_Q), lane, [&](int i) { return tiled_blk(gA + m0 + i / 3, KD, nt0 + i % 3); },
                            [&](int i) { return acc[i / 3][i % 3]; });
  }
  {
    const unsigned brd[3] = {l0 + 18432 + wn * 3072 + lane * 16, 0, 0};
    f32x4 acc[MW][3] = {};
    gemm12<MW, 3, false, false, 1024, 6144, SLOT, 0, FTX>(acc, aa, aa + 1024, brd, 4 * SLOT, ws,
                                                         Pending{C_QK + (b * 2 + sec) * 8 + g, -1, arr + 2}, lane);
    store_tiles<MW * 3, 16>(rs, (int)(sec ? OFF_VT2 : OFF_VT1), lane,
                            [&](int i) { return tiled_blk(b * 48 + nt0 + i % 3, KK, gA - b * RT + m0 + i / 3); },
                            [&](int i) { return acc[i / 3][i % 3]; });
    pv = Pending{C_V + (b * 2 + sec) * 8 + (col & 7), -1, arr + 3};
  }
}

__device__ __forceinline__ void big_wg(LDS_AS char* smem, char* __restrict__ ws, const u16* __restrict__ wqkv_img,
                                       const u16* __restrict__ wo_img, const u16* __restrict__ wo_txt, int bid, int wave8, int lane) {
  const int x = bid & 7, idx = bid >> 3, b = x & 1, g = idx % 7, col = (x >> 1) * 4 + idx / 7;
  const int gA = b * RT + g * 9, grp = b * 7 + g, hb = (int)OFF_H + gA * KD * 1024;
  const int wi = idx < 24 ? idx : idx - 4, wmb = b * 6 + wi % 6, wcol = (x >> 1) * 4 + wi / 6;
  const P2Tile t2 = p2_tile(bid);
  const u32x4 srd_ws = make_srd(ws, WS_END);
  rsrc_t rs = make_rsrc(ws, WS_END);
  LDS_AS unsigned* arr = (LDS_AS unsigned*)(smem + ARR);
  if (wave8 == 7) {
    const int c2 = p2_counter(t2, lane);
    barriers(7);
    unsigned f = peek(srd_ws, C_H + grp);
    barriers(2);
    check(f, srd_ws, C_H + grp, 12);
    barriers(10);
    f = peek(srd_ws, C_W + col);
    barriers(2);
    check(f, srd_ws, C_W + col, 12);
    barriers(10);
    f = peek(srd_ws, c2);
    barriers(2);
    check(f, srd_ws, c2, 8);
    barriers(18);
    return;
  }
  if (wave8 >= 4) {
    const int l = wave8 - 4;
    rsrc_t rw = make_rsrc(wqkv_img, D * 3 * D * 2);
    rsrc_t ro = make_rsrc(wcol < 8 ? wo_img : wo_txt, D * D * 2);
    int bo[4], bw[4], av[3];
#pragma unroll
    for (int i = 0; i < 4; ++i) {
      bo[i] = (wcol & 7) * 192 + t96_src(l * 4 + i, lane, D * 2, wcol & 1);
      bw[i] = col * 192 + t96_src(l * 4 + i, lane, WP, col & 1);
    }
#pragma unroll
    for (int i = 0; i < 3; ++i) {
      const int u = min(l * 3 + i, 7), r = u * 8 + (lane >> 3);
      av[i] = (wmb * 64 + r) * WP + 2 * D * 2 + (((lane & 7) ^ ((r >> 1) & 7)) * 16);
    }
    const int wz = idx < 24 ? 0 : OOB;
    auto issueW = [&](int s, int slot) {
#pragma unroll
      for (int i = 0; i < 3; ++i) dma<0>(rw, smem + slot * SLOT + min(l * 3 + i, 7) * 1024, av[i], wz + s * 128);
#pragma unroll
      for (int i = 0; i < 4; ++i) dma<0>(ro, smem + slot * SLOT + 18432 + (l * 4 + i) * 1024, bo[i], wz + s * 64 * (D * 2));
    };
    auto issueA = [&](int s, int slot) {
#pragma unroll
      for (int i = 0; i < 3; ++i) {
        LDS_AS char* sa = smem + slot * SLOT + (l * 3 + i) * 2048;
        dma_sc1<0>(rs, sa, lane * 16, hb + ((l * 3 + i) * KD + 2 * s) * 1024);
        dma_sc1<1024>(rs, sa, lane * 16, hb + ((l * 3 + i) * KD + 2 * s) * 1024);
      }
    };
    issueW(0, 0);
    __builtin_amdgcn_s_sleep(4);
#pragma unroll
    for (int s = 1; s < 4; ++s) issueW(s, s);
    WAIT_VM(21); BARRIER();
    int slot = 4;
#pragma unroll 1
    for (int s = 4; s < 12; ++s) {
      WAIT_VM(14); BARRIER(); issueW(s, slot); slot = wrapn(slot + 1, 5);
    }
    stream12<7, 10>(slot, [&](int s, int sl) {
#pragma unroll
      for (int i = 0; i < 4; ++i) dma<0>(rw, smem + sl * SLOT + 18432 + (l * 4 + i) * 1024, bw[i], s * 64 * WP);
      issueA(s, sl);
    });
    stream12<10, 10>(slot, [&](int s, int sl) {
#pragma unroll
      for (int i = 0; i < 4; ++i) {
        const int pc = l * 4 + i;
        dma_sc1(rs, smem + sl * SLOT + 18432 + pc * 1024, lane * 16, (int)OFF_WVO + ((col * 6 + pc % 6) * KD + 2 * s + pc / 6) * 1024);
      }
      issueA(s, sl);
    });
    stream12<10, 8>(slot, [&](int s, int sl) { p2_issue(smem, rs, t2, l, lane, s, sl); });
    WAIT_VM(16); BARRIER();
    WAIT_VM(8); BARRIER();
    WAIT_VM(0); BARRIER();
    barriers(3);
    return;
  }
  const int c = wave8, wm = c >> 1, wn = c & 1;
  Pending ph{-1, -1, arr};
  if (col < 12) {
    const int W = col * 4 + c, ks = W >> 1, t0 = (W & 1) * 5;
    u32x4 xr[5], mp[4][KSPL];
#pragma unroll
    for (int i = 0; i < 4; ++i)
#pragma unroll
      for (int k = 0; k < KSPL; ++k)
        mp[i][k] = aload(srd_ws, (lane >> 4) * 32, (int)OFF_MOD + ((k * 4 + b) * 2304 + (i >> 1) * 768 + ks * 32 + (i & 1) * 4) * 4);
#pragma unroll
    for (int i = 0; i < 5; ++i) xr[i] = aload(srd_ws, lane * 16, (int)OFF_XN + ((gA + min(t0 + i, 8)) * KD + ks) * 1024);
    WAIT_VM(0);
    f32x4 gs[4];
#pragma unroll
    for (int i = 0; i < 4; ++i) {
      TIE(mp[i][0]); TIE(mp[i][1]);
      gs[i] = __builtin_bit_cast(f32x4, mp[i][0]) + __builtin_bit_cast(f32x4, mp[i][1]);
      if (i >= 2) gs[i] += 1.0f;
    }
#pragma unroll
    for (int i = 0; i < 5; ++i) {
      TIE(xr[i]);
      __builtin_amdgcn_raw_buffer_store_b128(modulate(xr[i], gs), rs, lane * 16, hb + (min(t0 + i, 8) * KD + ks) * 1024, 16);
    }
    ph.i0 = C_H + grp;
  }
  Pending pw{-1, -1, arr + 1};
  {
    const int sw = (lane & 15) >> 1, gq = lane >> 4;
    const unsigned ab = lds_addr(smem) + wm * 4096 + (lane & 15) * 128;
    unsigned brd[3];
#pragma unroll
    for (int n = 0; n < 3; ++n) brd[n] = lds_addr(smem) + 18432 + t96_rd(lane, wn * 3 + n, wcol & 1);
    f32x4 acc[2][3] = {};
    BARRIER();
    gemm12<2, 3, true, false, 0, 6144, SLOT, 0, 2>(acc, ab + ((gq ^ sw) * 16), ab + (((4 + gq) ^ sw) * 16), brd, 0, ws, ph, lane);
    if (idx < 24) {
      store_tiles<6, 16>(rs, (int)OFF_WVO, lane, [&](int i) { return tiled_blk(wcol * 6 + wn * 3 + i % 3, KD, wmb * 4 + wm * 2 + i / 3); },
                         [&](int i) { return acc[i / 3][i % 3]; });
      pw.i0 = C_W + wcol;
    }
  }
  Pending pv;
  if (wm == 0) big_p1<5>(smem, ws, gA, g, col, 0, wn, lane, arr, pw, pv);
  else big_p1<4>(smem, ws, gA, g, col, 5, wn, lane, arr, pw, pv);
  p2_mfma(smem, ws, t2, 1 * SLOT, c, lane, arr + 4, pv);
}

template <int MT, bool VT, bool FIRST>
__device__ __forceinline__ void small_p1(LDS_AS char* smem, char* __restrict__ ws, const u16* __restrict__ wsrc, int wcol, int obase,
                                         int gA, int col, int smod, int wave8, int lane, const Pending& pend) {
  constexpr int MH = MT / 2;
  const u32x4 srd_ws = make_srd(ws, WS_END);
  rsrc_t rs = make_rsrc(ws, WS_END);
  auto gtile = [&](int lt) { return lt < MH ? gA + lt : gA + RT + (lt - MH); };
  if (wave8 == 7) {
    if (FIRST) {
      u32x4 mp[12][KSPL];
#pragma unroll
      for (int h = 0; h < 12; ++h)
#pragma unroll
        for (int k = 0; k < KSPL; ++k) mp[h][k] = aload(srd_ws, lane * 16, (int)OFF_MOD + (((k * 2 + smod) * 2 + h / 6) * 2304 + (h % 6) * 256) * 4);
      WAIT_VM(0);
#pragma unroll
      for (int h = 0; h < 12; ++h) {
        TIE(mp[h][0]); TIE(mp[h][1]);
        f32x4 v = __builtin_bit_cast(f32x4, mp[h][0]) + __builtin_bit_cast(f32x4, mp[h][1]);
        if (h % 6 >= 3) v += 1.0f;
        *(LDS_AS f32x4*)(smem + P1_SGS + h * 1024 + lane * 16) = v;
      }
      WAIT_LGKM(0);
    }
    barriers(FIRST ? 15 : 14);
    return;
  }
  if (wave8 >= 4) {
    const int l = wave8 - 4;
    rsrc_t rb = VT ? rs : make_rsrc(wsrc, D * 3 * D * 2);
    int bv[4];
#pragma unroll
    for (int i = 0; i < 4; ++i) {
      const int pc = l * 4 + i;
      bv[i] = VT ? ((col * 6 + pc % 6) * KD + pc / 6) * 1024 + lane * 16 : wcol + t96_src(pc, lane, WP, col & 1);
    }
    if (VT) poll(srd_ws, C_W + col, 12);
    loader_loop<12, 5, 4, 0, FIRST>([&](int s, int slot) {
#pragma unroll
      for (int i = 0; i < 4; ++i) {
        if (VT) dma_sc1(rb, smem + P1_SB + slot * 12288 + (l * 4 + i) * 1024, bv[i], (int)OFF_WVO + s * 2048);
        else dma<0>(rb, smem + P1_SB + slot * 12288 + (l * 4 + i) * 1024, bv[i], s * 64 * WP);
      }
    });
    BARRIER();
    return;
  }
  const int c = wave8, wm = c >> 1, wn = c & 1;
  auto aoff = [&](int st) { return st * (MT * 2048); };
  if (FIRST) {
    u32x4 xr[6][MT];
#pragma unroll
    for (int k = 0; k < 6; ++k)
#pragma unroll
      for (int i = 0; i < MT; ++i) xr[k][i] = aload(srd_ws, lane * 16, (int)OFF_XN + (gtile(i) * KD + c * 6 + k) * 1024);
    BARRIER();
    WAIT_VM(0);
#pragma unroll
    for (int k = 0; k < 6; ++k) {
      f32x4 gs[2][4];
#pragma unroll
      for (int z = 0; z < 2; ++z) {
        const unsigned gp = lds_addr(smem) + P1_SGS + z * 6144 + (c * 6 + k) * 128 + (lane >> 4) * 32;
        rd_f4<0>(gs[z][0], gp); rd_f4<16>(gs[z][1], gp); rd_f4<3072>(gs[z][2], gp); rd_f4<3088>(gs[z][3], gp);
      }
      WAIT_LGKM(0);
#pragma unroll
      for (int z = 0; z < 2; ++z)
#pragma unroll
        for (int i = 0; i < 4; ++i) TIE(gs[z][i]);
#pragma unroll
      for (int i = 0; i < MT; ++i) {
        TIE(xr[k][i]);
        *(LDS_AS u32x4*)(smem + aoff(c * 3 + (k >> 1)) + i * 2048 + (k & 1) * 1024 + lane * 16) = modulate(xr[k][i], gs[i < MH ? 0 : 1]);
      }
    }
    WAIT_LGKM(0);
  }
  const unsigned aa = lds_addr(smem) + wm * MH * 2048 + lane * 16;
  unsigned brd[3];
#pragma unroll
  for (int n = 0; n < 3; ++n) brd[n] = lds_addr(smem) + P1_SB + (VT ? wn * 3072 + lane * 16 : t96_rd(lane, wn * 3 + n, col & 1));
  f32x4 acc[MH][3] = {};
  BARRIER();
  gemm12<MH, 3, !VT, !VT, 1024, 6144, 12288, MT * 2048, 4>(acc, aa, aa + 1024, brd, 0, ws, pend, lane);
  BARRIER();
  const int nt0 = (col & 7) * 6 + wn * 3;
  if (VT) store_tiles<MH * 3, 16>(rs, obase, lane, [&](int i) { return tiled_blk(wm * 48 + nt0 + i % 3, KK, gA + i / 3); },
                                  [&](int i) { return acc[i / 3][i % 3]; });
  else store_tiles<MH * 3, 16>(rs, obase, lane, [&](int i) { return tiled_blk((FIRST ? gA + wm * RT : wm * MH) + i / 3, KD, nt0 + i % 3); },
                               [&](int i) { return acc[i / 3][i % 3]; });
}

__device__ __forceinline__ void small_wg(LDS_AS char* smem, char* __restrict__ ws, const u16* __restrict__ wqkv_img,
                                         const u16* __restrict__ wqkv_txt, const u16* __restrict__ wo_img,
                                         const u16* __restrict__ wo_txt, int bid, int wave8, int lane) {
  const int x = bid & 7, idx = bid >> 3;
  const u32x4 srd_ws = make_srd(ws, WS_END);
  rsrc_t rs = make_rsrc(ws, WS_END);
  LDS_AS unsigned* arr = (LDS_AS unsigned*)(smem + ARR);
  Pending pv{-1, -1, arr + 1};
  if (bid < 240) {
    const int col = (x >> 1) * 4 + (x & 1) * 2 + (idx - 28), sec = col >> 3;
    small_p1<2, false, true>(smem, ws, wqkv_img, col * 192, (int)(sec ? OFF_K : OFF_Q), 63, col, 0, wave8, lane, Pending{-1, -1, arr});
    small_p1<2, true, false>(smem, ws, wqkv_img, 0, (int)(sec ? OFF_VT2 : OFF_VT1), 63, col, 0, wave8, lane,
                             Pending{C_QK + sec * 8 + 7, C_QK + 16 + sec * 8 + 7, arr});
    pv.i0 = C_V + sec * 8 + (col & 7); pv.i1 = pv.i0 + 16;
  } else {
    const int col = bid - 240;
    small_p1<4, false, true>(smem, ws, wqkv_txt, col * 192, (int)(col >> 3 ? OFF_K : OFF_Q), 64, col, 1, wave8, lane, Pending{-1, -1, arr});
    const Pending pq{C_T + (col >> 3), -1, arr};
    if (col < 8) {
      small_p1<4, false, false>(smem, ws, wqkv_txt, 3072 + col * 192, (int)OFF_VX, 64, col, 1, wave8, lane, pq);
      pv.i0 = C_T + 2;
    } else if (wave8 < 4) {
      flush(ws, pq, lane);
    }
  }
  if (wave8 < 4) flush(ws, pv, lane);
  if (bid < 242) {
    const P2Tile t2 = p2_tile(bid);
    if (wave8 == 7) barriers(15);
    else if (wave8 >= 4) {
      poll(srd_ws, p2_counter(t2, lane), 8);
      loader_loop<12, 5, 8>([&](int s, int slot) { p2_issue(smem, rs, t2, wave8 - 4, lane, s, slot); });
      barriers(2);
    } else {
      BARRIER();
      p2_mfma(smem, ws, t2, 0, wave8, lane, arr + 4, Pending{-1, -1, arr});
    }
  } else if (bid < 254) {
    const int e = bid - 242, wave = wave8 & 3;
    if (wave8 >= 4) {
      rsrc_t rb = make_rsrc(e < 6 ? wo_img : wo_txt, D * D * 2);
      const int bv0 = (e % 6) * 256 + wave * 16 * (D * 2) + timg_src(lane, 0, D * 2);
      const int bv1 = (e % 6) * 256 + (wave * 16 + 8) * (D * 2) + timg_src(lane, 1, D * 2);
      poll(srd_ws, C_T + 2, 8);
      loader_loop<12, 5, 6>([&](int s, int slot) {
        LDS_AS char* sb = smem + slot * SLOT;
        dma_sc1<0>(rs, sb + wave * 2048, lane * 16, (int)OFF_VX + (wave * KD + 2 * s) * 1024);
        dma_sc1<1024>(rs, sb + wave * 2048, lane * 16, (int)OFF_VX + (wave * KD + 2 * s) * 1024);
#pragma unroll
        for (int i = 0; i < 4; ++i) dma<0>(rb, sb + 8192 + (wave * 4 + i) * 1024, ((i >> 1) ? bv1 : bv0) + (i & 1) * 128, s * 64 * (D * 2));
      });
    } else {
      const int wm = wave >> 1, wn = wave & 1;
      const unsigned aa = lds_addr(smem) + wm * 4096 + lane * 16;
      unsigned brd[4];
#pragma unroll
      for (int n = 0; n < 4; ++n) brd[n] = lds_addr(smem) + 8192 + timg_rd<2>(lane, wn * 4 + n);
      f32x4 acc[2][4] = {};
      BARRIER();
      gemm12<2, 4, true, false, 0, 8192, SLOT, 0, FTX>(acc, aa, aa + 1024, brd, 0, ws, Pending{-1, -1, arr}, lane);
      store_tiles<8, 16>(rs, (int)(e < 6 ? OFF_VT1 : OFF_VT2), lane,
                         [&](int i) { return tiled_blk(wm * 48 + (e % 6) * 8 + wn * 4 + (i & 3), KK, 64 + (i >> 2)); },
                         [&](int i) { return acc[i >> 2][i & 3]; });
      if (last_wave(arr + 4, 4, lane)) signal(ws, C_X + e);
    }
  }
}

constexpr int K3_NS = 4, K3_STAGE = 33 * 1024, K3_F = K3_NS * K3_STAGE;
__device__ __forceinline__ void fmac(f32x4& a, const f32x4& x, float f, int e) {
  float a0 = a[e], a1 = a[e + 1];
  asm volatile("v_fmac_f32 %0, %2, %4\n\tv_fmac_f32 %1, %3, %4" : "+v"(a0), "+v"(a1) : "v"(x[e]), "v"(x[e + 1]), "v"(f));
  a[e] = a0; a[e + 1] = a1;
}
template <int QT>
__device__ __forceinline__ void k3_mfma(LDS_AS char* smem, char* __restrict__ ws, int wn, int qoff, int nb, int lane, u16* __restrict__ outp) {
  const unsigned vrd = lds_addr(smem) + wn * 9216 + lane * 16, prd = lds_addr(smem) + 18432 + qoff * 3072 + lane * 16;
  const unsigned fs = lds_addr(smem) + K3_F + (qoff * 16 + (lane & 15)) * 4;
  auto rdf = [&](float (&f)[QT], int t) {
    rd_32<0>(f[0], fs + t * 320);
    if constexpr (QT > 1) rd_32<64>(f[QT > 1 ? 1 : 0], fs + t * 320);
    if constexpr (QT > 2) rd_32<128>(f[QT > 2 ? 2 : 0], fs + t * 320);
  };
  f32x4 acc[3][QT] = {}, t0[3][QT], t1[3][QT] = {};
  Frags<3, QT> f0, f1, f2;
  unsigned bc[QT];
  bc[0] = prd;
  BARRIER();
  rd_set<3, QT, false, 3072, 3072, 0, 0>(f0, vrd, bc);
  rd_set<3, QT, false, 3072, 3072, 1024, 1024>(f1, vrd, bc);
  int so = 0;
  auto stage = [&](int t, f32x4 (&cur)[3][QT], f32x4 (&prv)[3][QT]) {
    BARRIER();
    float fp[QT];
    rdf(fp, t);
    kstep<3, QT, false, false, true, 3072, 3072, 2048, 2048, true>(cur, f0, f2, vrd + so, bc, NoExtra{});
    so = wrapn(so + K3_STAGE, K3_NS * K3_STAGE);
    bc[0] = prd + so;
    auto fold = [&](auto sc) {
      constexpr int S = decltype(sc)::value, T = S / 2, E = (S % 2) * 2;
      fmac(acc[T / QT][T % QT], prv[T / QT][T % QT], fp[T % QT], E);
    };
    kstep<3, QT, false, false, false, 3072, 3072, 0, 0, true>(cur, f1, f0, vrd + so, bc, [&](auto ic) {
      if constexpr (decltype(ic)::value == 0) {
#pragma unroll
        for (int j = 0; j < QT; ++j) tie1(fp[j]);
      }
      fold(ic);
    });
    kstep<3, QT, false, false, false, 3072, 3072, 1024, 1024, true>(cur, f2, f1, vrd + so, bc, [&](auto ic) {
      fold(std::integral_constant<int, 3 * QT + decltype(ic)::value>{});
    });
  };
#pragma unroll 1
  for (int t = 0; t < 10; t += 2) {
    stage(t, t0, t1);
    stage(t + 1, t1, t0);
  }
  stage(10, t0, t1);
  float fp[QT];
  rdf(fp, 11);
  land<3, QT, false>(f0);
  land<3, QT, false>(f1);
#pragma unroll
  for (int j = 0; j < QT; ++j) TIE(fp[j]);
  LDS_AS char* xs = smem + 3 * K3_STAGE;
  const int cw = wn * 48 + (lane >> 4) * 4;
  f32x4 o[QT * 3];
#pragma unroll
  for (int i = 0; i < 3; ++i) {
    const f32x4 gate = *(LDS_AS f32x4*)(xs + 15360 + (cw + i * 16) * 4) + *(LDS_AS f32x4*)(xs + 15360 + 384 + (cw + i * 16) * 4);
#pragma unroll
    for (int j = 0; j < QT; ++j) {
      const u32x2 xv = *(LDS_AS u32x2*)(xs + ((qoff + j) * 16 + (lane & 15)) * 192 + (cw + i * 16) * 2);
      const f32x4 a = acc[i][j] + t0[i][j] * fp[j];
      o[j * 3 + i] = f32x4{fmaf(gate[0], a[0], bflo(xv[0])), fmaf(gate[1], a[1], bfhi(xv[0])),
                           fmaf(gate[2], a[2], bflo(xv[1])), fmaf(gate[3], a[3], bfhi(xv[1]))};
    }
  }
  const bool odd = (lane >> 4) & 1;
  u16* ob = outp + (size_t)(qoff * 16 + (lane & 15)) * D + nb * 96 + wn * 48;
  auto toff = [&](int i) { return (i / 3) * 16 * D + (i % 3) * 16; };
#pragma unroll
  for (int i = 0; i + 1 < QT * 3; i += 2) *(u32x4*)(ob + (odd ? toff(i + 1) : toff(i)) + (lane >> 5) * 8) = pair16(o[i], o[i + 1]);
  if (QT & 1) *(u32x2*)(ob + toff(QT * 3 - 1) + (lane >> 4) * 4) = pack4(o[QT * 3 - 1]);
}

__device__ __forceinline__ void phase3(LDS_AS char* smem, char* __restrict__ ws, const u16* __restrict__ x_img,
                                       const u16* __restrict__ x_txt, u16* __restrict__ out, int bid, int wave8, int lane) {
  if (bid >= 224) return;
  const int wave = wave8 & 3, x = bid & 7, idx = bid >> 3, b = x & 1;
  int rt0, nrt, nb, txt;
  if (idx < 26) {
    const int u = (x >> 1) * 26 + idx, rb = (u % 52) >> 2;
    nb = (u / 52) * 4 + (u & 3); rt0 = rb * 5; nrt = rb == 12 ? 4 : 5; txt = 0;
  } else {
    nb = (x >> 1) * 2 + idx - 26; rt0 = 64; nrt = 2; txt = 1;
  }
  if (wave8 >= 4) {
    rsrc_t rs = make_rsrc(ws, WS_END);
    const u32x4 srd_ws = make_srd(ws, WS_END);
    auto tail = [&](int slot) {
      const u16* xres = txt ? x_txt + (size_t)b * NTX * D : x_img + (size_t)(b * NI + rt0 * 16) * D;
      rsrc_t rx = make_rsrc(xres, nrt * 16 * D * 2);
      LDS_AS char* sb = smem + slot * K3_STAGE + wave * 4096;
#pragma unroll
      for (int i = 0; i < 4; ++i) {
        const int c = (wave * 4 + i) * 64 + lane;
        if (wave * 4 + i < 15) dma<0>(rx, sb + i * 1024, (c / 12) * (D * 2) + nb * 192 + (c % 12) * 16, 0);
        else dma<0>(rs, sb + i * 1024, lane < 24 * KSPL ? (int)OFF_MOD + ((((lane / 24) * 2 + txt) * 2 + b) * 2304 + 1536 + nb * 96 + (lane % 24) * 4) * 4 : OOB, 0);
      }
    };
    const int cp = C_P + b * 11 + (rt0 * 16 + (lane & 1) * (nrt * 16 - 1)) / 96;
    if (wave < 2) {
      poll(srd_ws, C_V + (b * 2 + txt) * 8 + nb, 8);
      const int vb = (int)(txt ? OFF_VT2 : OFF_VT1) + (b * 48 + nb * 6 + wave * 3) * KK * 1024;
      const int ltid = wave * 64 + lane;
      u32x4 st[11];
      loader_loop<11, K3_NS, 9, 4>([&](int t, int slot) {
        if (t == 10) poll(srd_ws, C_X + txt * 6 + (nb * 96 + (lane & 1) * 95) / 128, 1);
        LDS_AS char* sb = smem + slot * K3_STAGE + wave * 9216;
#pragma unroll
        for (int i = 0; i < 3; ++i) {
          const int so = vb + i * KK * 1024 + t * 3072;
          dma_sc1<0>(rs, sb + i * 3072, lane * 16, so);
          dma_sc1<1024>(rs, sb + i * 3072, lane * 16, so);
          dma_sc1<2048>(rs, sb + i * 3072, lane * 16, so);
        }
      }, [&]() {
        WAIT_VM(0);
        float m[11];
#pragma unroll
        for (int t = 0; t < 11; ++t) { TIE(st[t]); m[t] = __uint_as_float(st[t][0]); }
        float M = m[0];
#pragma unroll
        for (int t = 1; t < 11; ++t) M = fmaxf(M, m[t]);
        float L = 0.f;
#pragma unroll
        for (int t = 0; t < 11; ++t) {
          m[t] = __builtin_amdgcn_exp2f(m[t] - M);
          L = fmaf(m[t], __uint_as_float(st[t][1]) + __uint_as_float(st[t][3]), L);
        }
        const float inv = 1.0f / L;
        LDS_AS float* fw = (LDS_AS float*)(smem + K3_F);
        if (ltid < 80) {
          fw[ltid] = 0.f;
#pragma unroll
          for (int t = 0; t < 11; ++t) fw[(1 + t) * 80 + ltid] = m[t] * inv;
        }
        WAIT_LGKM(0);
      }, tail, [&]() {
        poll(srd_ws, cp, 11);
        const int r = min(ltid, nrt * 16 - 1), vs = (int)OFF_ST + (b * RT + rt0 + (r >> 4)) * 2816 + (r & 15) * 16;
#pragma unroll
        for (int i = 0; i < 11; ++i) st[i] = aload_sc1(srd_ws, vs, i * 256);
      });
    } else {
      poll(srd_ws, cp, 11);
      const int pb = (int)OFF_P + (b * RT + rt0) * KK * 1024;
      loader_loop<11, K3_NS, 8, 4>([&](int t, int slot) {
#pragma unroll
        for (int i = 0; i < 8; ++i) {
          const int pc = (wave - 2) * 8 + i, u = pc / 3, ks = pc % 3;
          dma_sc1(rs, smem + slot * K3_STAGE + 18432 + min(pc, 14) * 1024, lane * 16,
                  u < nrt ? pb + (u * KK + t * 3 + ks) * 1024 : (pc == 15 ? pb + ((nrt - 1) * KK + t * 3 + 2) * 1024 + (nrt == 5 ? 0 : OOB) : OOB));
        }
      }, NoHook{}, tail);
    }
    return;
  }
  u16* outp = txt ? out + (size_t)2 * NI * D + (size_t)b * NTX * D : out + (size_t)(b * NI + rt0 * 16) * D;
  const int wq = wave >> 1, q0 = (nrt + 1) >> 1;
  const int qt = wq ? nrt - q0 : q0, qoff = wq ? q0 : 0;
  if (qt == 3) k3_mfma<3>(smem, ws, wave & 1, qoff, nb, lane, outp);
  else if (qt == 2) k3_mfma<2>(smem, ws, wave & 1, qoff, nb, lane, outp);
  else k3_mfma<1>(smem, ws, wave & 1, qoff, nb, lane, outp);
}

__global__ __launch_bounds__(512, 2) void kmain(const u16* __restrict__ x_img, const u16* __restrict__ x_txt,
                                                const u16* __restrict__ wqkv_img, const u16* __restrict__ wqkv_txt,
                                                const u16* __restrict__ wo_img, const u16* __restrict__ wo_txt,
                                                char* __restrict__ ws, u16* __restrict__ out) {
  __shared__ __attribute__((aligned(1024))) char smem_[MAIN_LDS];
  LDS_AS char* smem = (LDS_AS char*)smem_;
  const int bid = blockIdx.x, tid = threadIdx.x, lane = tid & 63;
  const int wave = __builtin_amdgcn_readfirstlane(tid >> 6);
  asm volatile(".Lmmdit_main_begin:");
  if (tid < 8) ((LDS_AS unsigned*)(smem + ARR))[tid] = 0u;
  WAIT_LGKM(0);
  if (bid < 224) {
    big_wg(smem, ws, wqkv_img, wo_img, wo_txt, bid, wave, lane);
    phase3(smem, ws, x_img, x_txt, out, bid, wave, lane);
  } else {
    small_wg(smem, ws, wqkv_img, wqkv_txt, wo_img, wo_txt, bid, wave, lane);
  }
  asm volatile(".Lmmdit_main_end:");
}

extern "C" void kernel_launch(void* const* d_in, const int* in_sizes, int n_in, void* d_out, int out_size, void* d_ws,
                              size_t ws_size, hipStream_t stream) {
  const u16* x_img = (const u16*)d_in[0];
  const u16* x_txt = (const u16*)d_in[1];
  const u16* cvec = (const u16*)d_in[2];
  const u16* wqkv_img = (const u16*)d_in[3];
  const u16* wqkv_txt = (const u16*)d_in[4];
  const u16* wo_img = (const u16*)d_in[5];
  const u16* wo_txt = (const u16*)d_in[6];
  const u16* wmod_img = (const u16*)d_in[7];
  const u16* wmod_txt = (const u16*)d_in[8];
  char* ws = (char*)d_ws;
  k0_prep<<<K0_T1 + K0_T2 + K0_Z + K0_PF, 256, 0, stream>>>(x_img, x_txt, cvec, wmod_img, wmod_txt, wqkv_img, wqkv_txt, wo_img, wo_txt, ws);
  kmain<<<256, 512, 0, stream>>>(x_img, x_txt, wqkv_img, wqkv_txt, wo_img, wo_txt, ws, (u16*)d_out);
}
```

```cpp
#include <hip/hip_runtime.h>
#include <hip/hip_bf16.h>
#include <stdint.h>
#include <type_traits>

typedef __attribute__((ext_vector_type(8))) __bf16 bf16x8;
typedef __attribute__((ext_vector_type(4))) __bf16 bf16x4;
typedef __attribute__((ext_vector_type(2))) __bf16 bf16x2;
typedef __attribute__((ext_vector_type(4))) float f32x4;
typedef __attribute__((ext_vector_type(2))) float f32x2;
typedef __attribute__((ext_vector_type(4))) unsigned u32x4;
typedef __attribute__((ext_vector_type(2))) unsigned u32x2;
#define LDS_AS __attribute__((address_space(3)))
typedef __amdgpu_buffer_rsrc_t rsrc_t;
typedef unsigned short u16;

constexpr int D = 768, NI = 1024, NTX = 32;
constexpr int RT = 66;
constexpr int KD = 24;
constexpr int KK = 33;
constexpr int KSPL = 2;
constexpr int OOB = 0x7f000000;

constexpr unsigned SZ_TOK = 2u * RT * KD * 1024u;
constexpr unsigned SZ_VT = 2u * 48u * KK * 1024u;
constexpr unsigned OFF_XN = 0;
constexpr unsigned OFF_Q = OFF_XN + SZ_TOK;
constexpr unsigned OFF_K = OFF_Q + SZ_TOK;
constexpr unsigned OFF_VX = OFF_K + SZ_TOK;
constexpr unsigned OFF_VT1 = OFF_VX + 4u * KD * 1024u;
constexpr unsigned OFF_VT2 = OFF_VT1 + SZ_VT;
constexpr unsigned OFF_P = OFF_VT2 + SZ_VT;
constexpr unsigned OFF_WVO = OFF_P + 2u * RT * KK * 1024u;
constexpr unsigned OFF_MOD = OFF_WVO + 96u * KD * 1024u;
constexpr unsigned OFF_ST = OFF_MOD + KSPL * 4u * 2304u * 4u;
constexpr unsigned OFF_CNT = OFF_ST + 2u * RT * 11u * 256u;
constexpr unsigned OFF_H = OFF_CNT + 24576u;
constexpr unsigned WS_END = OFF_H + SZ_TOK;
constexpr int C_H = 0;
constexpr int C_QK = 14;
constexpr int C_T = 46;
constexpr int C_V = 49;
constexpr int C_X = 81;
constexpr int C_P = 93;
constexpr int C_W = 159;
constexpr unsigned SZ_CNT = 24576u;

#define WAIT_VM(n) asm volatile("s_waitcnt vmcnt(%0)" ::"n"(n) : "memory")
#define WAIT_LGKM(n) asm volatile("s_waitcnt lgkmcnt(%0)" ::"n"(n) : "memory")
#define BARRIER() __builtin_amdgcn_s_barrier()
#define TIE(x) asm volatile("" : "+v"(x))
#define SCHED_FENCE() __builtin_amdgcn_sched_barrier(0)

template <int I, int N, class F>
__device__ __forceinline__ void static_for(F&& f) {
  if constexpr (I < N) { f(std::integral_constant<int, I>{}); static_for<I + 1, N>(f); }
}
__device__ __forceinline__ rsrc_t make_rsrc(const void* p, unsigned bytes) {
  return __builtin_amdgcn_make_buffer_rsrc((void*)p, 0, (int)bytes, 0x00020000);
}
__device__ __forceinline__ u32x4 make_srd(const void* p, unsigned bytes) {
  unsigned long long a = (unsigned long long)p;
  u32x4 r;
  r[0] = (unsigned)a; r[1] = (unsigned)(a >> 32) & 0xffffu; r[2] = bytes; r[3] = 0x00020000u;
  return r;
}
template <int IMM>
__device__ __forceinline__ void dma(rsrc_t rs, LDS_AS char* lds, int voff, int soff) {
  __builtin_amdgcn_raw_ptr_buffer_load_lds(rs, (LDS_AS void*)lds, 16, voff, soff, IMM, 0);
}
template <int IMM = 0>
__device__ __forceinline__ void dma_sc1(rsrc_t rs, LDS_AS char* lds, int voff, int soff) {
  __builtin_amdgcn_raw_ptr_buffer_load_lds(rs, (LDS_AS void*)lds, 16, voff, soff, IMM, 16);
}
__device__ __forceinline__ u32x4 aload(const u32x4& srd, int voff, int soff) {
  u32x4 r;
  asm volatile("buffer_load_dwordx4 %0, %1, %2, %3 offen" : "=v"(r) : "v"(voff), "s"(srd), "s"(soff));
  return r;
}
__device__ __forceinline__ u32x4 aload_sc1(const u32x4& srd, int voff, int soff) {
  u32x4 r;
  asm volatile("buffer_load_dwordx4 %0, %1, %2, %3 offen sc1" : "=v"(r) : "v"(voff), "s"(srd), "s"(soff));
  return r;
}
__device__ __forceinline__ void signal(char* ws, int idx) {
  asm volatile("global_atomic_add %0, %1, %2" ::"v"((int)OFF_CNT + idx * 128), "v"(1u), "s"(ws) : "memory");
}
__device__ __forceinline__ bool last_wave(LDS_AS unsigned* word, unsigned n, int lane) {
  WAIT_VM(0);
  unsigned old = 0;
  if (lane == 0) asm volatile("ds_add_rtn_u32 %0, %1, %2\n\ts_waitcnt lgkmcnt(0)" : "=v"(old) : "v"((unsigned)(size_t)word), "v"(1u) : "memory");
  return lane == 0 && old == n - 1;
}
struct Pending { int i0, i1; LDS_AS unsigned* arr; };
__device__ __forceinline__ void flush(char* ws, const Pending& p, int lane) {
  if (p.i0 >= 0 && last_wave(p.arr, 4, lane)) { signal(ws, p.i0); if (p.i1 >= 0) signal(ws, p.i1); }
}
__device__ __forceinline__ void poll(const u32x4& srd, int idx, unsigned target) {
#pragma unroll 1
  for (int spin = 0; spin < (1 << 20); ++spin) {
    unsigned f;
    asm volatile("buffer_load_dword %0, %1, %2, 0 offen sc1\n\ts_waitcnt vmcnt(0)" : "=v"(f) : "v"((int)OFF_CNT + idx * 128), "s"(srd) : "memory");
    if (__builtin_amdgcn_ballot_w64(f < target) == 0) break;
    __builtin_amdgcn_s_sleep(1);
  }
}
__device__ __forceinline__ unsigned peek(const u32x4& srd, int idx) {
  unsigned f;
  asm volatile("buffer_load_dword %0, %1, %2, 0 offen sc1" : "=v"(f) : "v"((int)OFF_CNT + idx * 128), "s"(srd) : "memory");
  return f;
}
__device__ __forceinline__ void check(unsigned f, const u32x4& srd, int idx, unsigned target) {
  WAIT_VM(0);
  TIE(f);
  if (__builtin_amdgcn_ballot_w64(f < target) != 0) poll(srd, idx, target);
}
__device__ __forceinline__ void barriers(int n) {
#pragma unroll 1
  for (int t = 0; t < n; ++t) BARRIER();
}
template <int OFF>
__device__ __forceinline__ void rd_tr(bf16x4& lo, bf16x4& hi, unsigned addr) {
  asm volatile("ds_read_b64_tr_b16 %0, %2 offset:%c3\n\tds_read_b64_tr_b16 %1, %2 offset:%c4"
               : "=&v"(lo), "=&v"(hi) : "v"(addr), "i"(OFF), "i"(OFF + 512));
}
template <int OFF>
__device__ __forceinline__ void rd_128(bf16x8& r, unsigned addr) {
  asm volatile("ds_read_b128 %0, %1 offset:%c2" : "=v"(r) : "v"(addr), "i"(OFF));
}
template <int OFF>
__device__ __forceinline__ void rd_32(float& r, unsigned addr) {
  asm volatile("ds_read_b32 %0, %1 offset:%c2" : "=v"(r) : "v"(addr), "i"(OFF));
}
__device__ __forceinline__ void tie1(float& x) { TIE(x); }
__device__ __forceinline__ void tie4(f32x4& x) { TIE(x); }
__device__ __forceinline__ void tie8(bf16x8& x) { TIE(x); }
__device__ __forceinline__ void tie4h(bf16x4& x) { TIE(x); }
__device__ __forceinline__ bf16x8 cat(bf16x4 lo, bf16x4 hi) { return __builtin_shufflevector(lo, hi, 0, 1, 2, 3, 4, 5, 6, 7); }
__device__ __forceinline__ unsigned lds_addr(LDS_AS char* p) { return (unsigned)(size_t)p; }
__device__ __forceinline__ int wrapn(int v, int n) { return v >= n ? v - n : v; }
__device__ __forceinline__ f32x4 mfma(bf16x8 a, bf16x8 b, f32x4 c) {
  return __builtin_amdgcn_mfma_f32_16x16x32_bf16(a, b, c, 0, 0, 0);
}
__device__ __forceinline__ unsigned pack2(float a, float b) {
  f32x2 f = {a, b};
  bf16x2 h = __builtin_convertvector(f, bf16x2);
  return __builtin_bit_cast(unsigned, h);
}
__device__ __forceinline__ u32x2 pack4(f32x4 v) { return u32x2{pack2(v[0], v[1]), pack2(v[2], v[3])}; }
__device__ __forceinline__ float bflo(unsigned v) { return __uint_as_float(v << 16); }
__device__ __forceinline__ float bfhi(unsigned v) { return __uint_as_float(v & 0xffff0000u); }
__device__ __forceinline__ float max2(float a, float b) { float r; asm("v_max_f32 %0, %1, %2" : "=v"(r) : "v"(a), "v"(b)); return r; }
__device__ __forceinline__ float max3(float a, float b, float c) { float r; asm("v_max3_f32 %0, %1, %2, %3" : "=v"(r) : "v"(a), "v"(b), "v"(c)); return r; }
__device__ __forceinline__ float red4_max(float x) {
  auto a = __builtin_amdgcn_permlane16_swap(__float_as_uint(x), __float_as_uint(x), false, false);
  x = max2(__uint_as_float(a[0]), __uint_as_float(a[1]));
  auto b = __builtin_amdgcn_permlane32_swap(__float_as_uint(x), __float_as_uint(x), false, false);
  return max2(__uint_as_float(b[0]), __uint_as_float(b[1]));
}
__device__ __forceinline__ float red4_sum(float x) {
  auto a = __builtin_amdgcn_permlane16_swap(__float_as_uint(x), __float_as_uint(x), false, false);
  x = __uint_as_float(a[0]) + __uint_as_float(a[1]);
  auto b = __builtin_amdgcn_permlane32_swap(__float_as_uint(x), __float_as_uint(x), false, false);
  return __uint_as_float(b[0]) + __uint_as_float(b[1]);
}
__device__ __forceinline__ unsigned tiled_lane(int lane) {
  int g = lane >> 4;
  return (unsigned)(((lane & 15) + 16 * (g >> 1)) * 16 + (g & 1) * 8);
}
__device__ __forceinline__ unsigned tiled_blk(int rowtile, int KS, int T16) {
  return (unsigned)((rowtile * KS + (T16 >> 1)) * 1024 + (T16 & 1) * 512);
}
__device__ __forceinline__ u32x4 pair16(f32x4 x, f32x4 y) {
  const u32x2 px = pack4(x), py = pack4(y);
  auto a = __builtin_amdgcn_permlane16_swap(px[0], py[0], false, false);
  auto b = __builtin_amdgcn_permlane16_swap(px[1], py[1], false, false);
  return u32x4{a[0], b[0], a[1], b[1]};
}
__device__ __forceinline__ unsigned tiled_lane16(int lane) { return (unsigned)(((lane & 15) + 16 * (lane >> 5)) * 16); }
template <int NTILE, int AUX, class Blk, class Val>
__device__ __forceinline__ void store_tiles(rsrc_t rs, int base, int lane, Blk&& blk, Val&& val) {
  const bool odd = (lane >> 4) & 1;
  const int l16 = (int)tiled_lane16(lane);
#pragma unroll
  for (int i = 0; i + 1 < NTILE; i += 2) {
    const int so = __builtin_amdgcn_readfirstlane(base - 0x100000 + (int)blk(i));
    const int d = __builtin_amdgcn_readfirstlane((int)(blk(i + 1) - blk(i)));
    __builtin_amdgcn_raw_buffer_store_b128(pair16(val(i), val(i + 1)), rs, l16 + 0x100000 + (odd ? d : 0), so, AUX);
  }
  if (NTILE & 1)
    __builtin_amdgcn_raw_buffer_store_b64(pack4(val(NTILE - 1)), rs, (int)tiled_lane(lane),
                                          __builtin_amdgcn_readfirstlane(base + (int)blk(NTILE - 1)), AUX);
}
__device__ __forceinline__ int timg_src(int lane, int sigma, int pitch) {
  int r = lane >> 3, c = (lane & 7) ^ (2 * ((r >> 1) & 1) + 4 * sigma);
  return r * pitch + c * 16;
}
template <int NB>
__device__ __forceinline__ int timg_rd(int lane, int nt) {
  int g = lane >> 4, q = (lane & 15) >> 2, p = lane & 3;
  int f = 2 * ((q >> 1) & 1) + 4 * (g & 1);
  return (g * NB + (nt >> 2)) * 1024 + q * 128 + (((2 * (nt & 3)) ^ f) + (p >> 1)) * 16 + (p & 1) * 8;
}

template <int MT, int NT>
struct Frags { bf16x8 a[MT]; bf16x8 b[NT]; bf16x4 l[NT], h[NT]; };

template <int MT, int NT, bool BTR, int AM, int BM, int AO, int BO, int I>
__device__ __forceinline__ void rd_item(Frags<MT, NT>& n, unsigned ab, const unsigned (&bb)[NT]) {
  if constexpr (I < NT) {
    if constexpr (BTR) rd_tr<BO>(n.l[I], n.h[I], bb[I]);
    else rd_128<BO + I * BM>(n.b[I], bb[0]);
  } else if constexpr (I < NT + MT) {
    rd_128<AO + (I - NT) * AM>(n.a[I - NT], ab);
  }
}
template <int MT, int NT, bool BTR, int AM, int BM, int AO, int BO>
__device__ __forceinline__ void rd_set(Frags<MT, NT>& n, unsigned ab, const unsigned (&bb)[NT]) {
  static_for<0, MT + NT>([&](auto ic) { rd_item<MT, NT, BTR, AM, BM, AO, BO, decltype(ic)::value>(n, ab, bb); });
}
template <int MT, int NT, bool BTR, bool AH = false>
__device__ __forceinline__ void land(Frags<MT, NT>& c) {
  WAIT_LGKM(AH ? MT + (BTR ? 2 : 1) * NT : 0);
#pragma unroll
  for (int j = 0; j < NT; ++j) {
    if constexpr (BTR) { TIE(c.l[j]); TIE(c.h[j]); } else TIE(c.b[j]);
  }
#pragma unroll
  for (int m = 0; m < MT; ++m) TIE(c.a[m]);
  SCHED_FENCE();
}
template <int MT, int NT, bool BTR, bool SWAP, bool ZC, int AM, int BM, int AO, int BO, bool AH = false, class X>
__device__ __forceinline__ void kstep(f32x4 (&acc)[MT][NT], Frags<MT, NT>& c, Frags<MT, NT>& n, unsigned ab,
                                      const unsigned (&bb)[NT], X&& extra) {
  land<MT, NT, BTR, AH>(c);
  static_for<0, MT * NT>([&](auto ic) {
    constexpr int I = decltype(ic)::value, m = I / NT, j = I % NT;
    bf16x8 bf;
    if constexpr (BTR) bf = cat(c.l[j], c.h[j]); else bf = c.b[j];
    const f32x4 ci = ZC ? f32x4{0.f, 0.f, 0.f, 0.f} : acc[m][j];
    acc[m][j] = SWAP ? mfma(bf, c.a[m], ci) : mfma(c.a[m], bf, ci);
    tie4(acc[m][j]);
    rd_item<MT, NT, BTR, AM, BM, AO, BO, I>(n, ab, bb);
    extra(ic);
    SCHED_FENCE();
  });
  static_for<MT * NT, (MT + NT > MT * NT ? MT + NT : MT * NT)>(
      [&](auto ic) { rd_item<MT, NT, BTR, AM, BM, AO, BO, decltype(ic)::value>(n, ab, bb); });
}
struct NoExtra { template <class T> __device__ __forceinline__ void operator()(T) const {} };

struct NoHook { __device__ __forceinline__ void operator()() const {} };
struct NoTail { __device__ __forceinline__ void operator()(int) const {} };
template <int NST, int NS, int V, int TV = 0, bool XB = false, class Issue, class Hook = NoHook, class Tail = NoTail, class Pre = NoHook>
__device__ __forceinline__ void loader_loop(Issue&& issue, Hook&& hook = NoHook{}, Tail&& tail = NoTail{}, Pre&& pre = NoHook{}) {
#pragma unroll
  for (int s = 0; s < NS - 1; ++s) issue(s, s);
  if (XB) BARRIER();
  pre();
  WAIT_VM(V * (NS - 2));
  BARRIER();
  hook();
  int slot = NS - 1;
#pragma unroll 1
  for (int t = 0; t <= NST - NS; ++t) {
    WAIT_VM(V * (NS - 3));
    BARRIER();
    issue(t + NS - 1, slot);
    slot = wrapn(slot + 1, NS);
  }
  static_for<1, NS>([&](auto rc) {
    constexpr int R = decltype(rc)::value;
    WAIT_VM(R == NS - 1 ? 0 : V * (NS - 2 - R) + (R > 1 ? TV : 0));
    BARRIER();
    if constexpr (R == 1) tail(slot);
  });
}

template <int VP, int V, class Issue>
__device__ __forceinline__ void stream12(int& slot, Issue&& issue) {
  WAIT_VM(2 * VP); BARRIER(); issue(0, slot); slot = wrapn(slot + 1, 5);
  WAIT_VM(VP + V); BARRIER(); issue(1, slot); slot = wrapn(slot + 1, 5);
#pragma unroll 1
  for (int t = 2; t < 12; ++t) {
    WAIT_VM(2 * V); BARRIER(); issue(t, slot); slot = wrapn(slot + 1, 5);
  }
}

template <int MW, int NW, bool BTR, bool SWAP, int BM, int BKS, int SLOT, int ASTEP, int FT>
__device__ __forceinline__ void gemm12(f32x4 (&acc)[MW][NW], unsigned a0, unsigned a1, const unsigned (&brd)[NW], int so,
                                       char* __restrict__ ws, const Pending& pend, int lane) {
  Frags<MW, NW> x0, x1, y0, y1;
  unsigned bc[NW];
#pragma unroll
  for (int n = 0; n < NW; ++n) bc[n] = brd[n] + so;
  rd_set<MW, NW, BTR, 2048, BM, 0, 0>(x0, ASTEP ? a0 : a0 + so, bc);
  rd_set<MW, NW, BTR, 2048, BM, 0, BKS>(x1, ASTEP ? a1 : a1 + so, bc);
  auto stage = [&](Frags<MW, NW>& c0, Frags<MW, NW>& c1, Frags<MW, NW>& n0, Frags<MW, NW>& n1) {
    so = wrapn(so + SLOT, 5 * SLOT);
    a0 += ASTEP; a1 += ASTEP;
#pragma unroll
    for (int n = 0; n < NW; ++n) bc[n] = brd[n] + so;
    BARRIER();
    kstep<MW, NW, BTR, SWAP, false, 2048, BM, 0, 0, true>(acc, c0, n0, ASTEP ? a0 : a0 + so, bc, NoExtra{});
    kstep<MW, NW, BTR, SWAP, false, 2048, BM, 0, BKS, true>(acc, c1, n1, ASTEP ? a1 : a1 + so, bc, NoExtra{});
  };
#pragma unroll 1
  for (int t = 0; t < 12; t += 2) {
    if (t == FT) flush(ws, pend, lane);
    stage(x0, x1, y0, y1);
    stage(y0, y1, x0, x1);
  }
  land<MW, NW, BTR>(x0);
  land<MW, NW, BTR>(x1);
}
__device__ __forceinline__ int t96_src(int pc, int lane, int pitch, int odd) {
  const int ks = pc / 6, r = pc % 6, g1 = (lane >> 4) & 1, r16 = 8 * g1 + 4 * (lane >> 5) + ((lane >> 2) & 3);
  return r < 4 ? (ks * 32 + r * 8) * pitch + odd * 64 + timg_src(lane, r & 1, pitch)
               : (ks * 32 + (r - 4) * 16 + r16) * pitch + (odd ? 0 : 128) + (((lane & 3) ^ (2 * g1)) * 16);
}
__device__ __forceinline__ int t96_rd(int lane, int nt, int odd) {
  const int g = lane >> 4, q = (lane & 15) >> 2, p = lane & 3;
  const bool narrow = odd ? nt < 2 : nt >= 4;
  return narrow ? 4096 + (g >> 1) * 1024 + (g & 1) * 256 + q * 64 + (((2 * (nt & 1) + (p >> 1)) ^ (2 * (g & 1))) * 16) + (p & 1) * 8
                : timg_rd<1>(lane, odd ? nt - 2 : nt);
}

constexpr int K0_T1 = 72 * KSPL, K0_T2 = 2 * RT, K0_Z = 8, K0_PF = 144;
__global__ __launch_bounds__(256) void k0_prep(const u16* __restrict__ x_img, const u16* __restrict__ x_txt,
                                               const u16* __restrict__ cvec, const u16* __restrict__ wmod_img,
                                               const u16* __restrict__ wmod_txt, const u16* __restrict__ wqkv_img,
                                               const u16* __restrict__ wqkv_txt, const u16* __restrict__ wo_img,
                                               const u16* __restrict__ wo_txt, char* __restrict__ ws) {
  __shared__ __attribute__((aligned(1024))) char smem_[24576];
  LDS_AS char* smem = (LDS_AS char*)smem_;
  const int bid = blockIdx.x, tid = threadIdx.x, lane = tid & 63;
  const int wave = __builtin_amdgcn_readfirstlane(tid >> 6);
  if (bid >= K0_T1 + K0_T2 + K0_Z) {
    const int i = bid - (K0_T1 + K0_T2 + K0_Z);
    const char* p = i < 18 ? (const char*)wo_img + i * 65536 : (i < 36 ? (const char*)wo_txt + (i - 18) * 65536
                  : (i < 90 ? (const char*)wqkv_img + (i - 36) * 65536 : (const char*)wqkv_txt + (i - 90) * 65536));
    u32x4 v[16];
#pragma unroll
    for (int k = 0; k < 16; ++k) v[k] = *(const u32x4*)(p + k * 4096 + tid * 16);
#pragma unroll
    for (int k = 0; k < 16; ++k) asm volatile("" ::"v"(v[k]));
    return;
  } else if (bid >= K0_T1 + K0_T2) {
    for (unsigned o = ((bid - K0_T1 - K0_T2) * 256 + tid) * 16; o < SZ_CNT; o += K0_Z * 4096) *(u32x4*)(ws + OFF_CNT + o) = u32x4{0u, 0u, 0u, 0u};
    unsigned long long pc;
    int delta, len;
    asm volatile("s_getpc_b64 %0\n.Lmmdit_k0_pc:\n\ts_mov_b32 %1, .Lmmdit_main_begin-.Lmmdit_k0_pc\n\ts_mov_b32 %2, .Lmmdit_main_end-.Lmmdit_main_begin"
                 : "=s"(pc), "=s"(delta), "=s"(len));
    pc += delta;
    unsigned w[3];
#pragma unroll
    for (int k = 0; k < 3; ++k) w[k] = *(const unsigned*)((const char*)pc + min((tid + k * 256) * 128, len - 4));
    asm volatile("" ::"v"(w[0]), "v"(w[1]), "v"(w[2]));
    return;
  } else if (bid < K0_T1) {
    constexpr int ROWS = D / KSPL, PER = ROWS / 32;
    const int i = bid, s = i / (36 * KSPL), rem = i % (36 * KSPL), cb = rem % 36, kh = rem / 36;
    const u16* w = (s ? wmod_txt : wmod_img) + (size_t)(kh * ROWS + (tid >> 3)) * (6 * D) + cb * 64 + (tid & 7) * 8;
    u16 cv[3];
#pragma unroll
    for (int e = 0; e < 3; ++e) { const int z = tid + e * 256; cv[e] = cvec[(z / ROWS) * D + kh * ROWS + z % ROWS]; }
    u32x4 wv[PER];
#pragma unroll
    for (int r = 0; r < PER; ++r) wv[r] = *(const u32x4*)(w + (size_t)r * 32 * (6 * D));
    LDS_AS float* sc = (LDS_AS float*)smem;
    LDS_AS float* red = (LDS_AS float*)(smem + 8192);
#pragma unroll
    for (int e = 0; e < 3; ++e) {
      const int z = tid + e * 256;
      const float v = __uint_as_float((unsigned)cv[e] << 16);
      sc[(z % ROWS) * 2 + z / ROWS] = v / (1.0f + __expf(-v));
    }
    __syncthreads();
    float a0[8] = {}, a1[8] = {};
#pragma unroll
    for (int r = 0; r < PER; ++r) {
      f32x2 sv = *(LDS_AS f32x2*)(sc + ((tid >> 3) + r * 32) * 2);
#pragma unroll
      for (int j = 0; j < 4; ++j) {
        float lo = bflo(wv[r][j]), hi = bfhi(wv[r][j]);
        a0[2 * j] = fmaf(sv[0], lo, a0[2 * j]); a0[2 * j + 1] = fmaf(sv[0], hi, a0[2 * j + 1]);
        a1[2 * j] = fmaf(sv[1], lo, a1[2 * j]); a1[2 * j + 1] = fmaf(sv[1], hi, a1[2 * j + 1]);
      }
    }
    LDS_AS float* rp = red + (tid >> 3) * 128 + (tid & 7) * 8;
    *(LDS_AS f32x4*)(rp) = f32x4{a0[0], a0[1], a0[2], a0[3]};
    *(LDS_AS f32x4*)(rp + 4) = f32x4{a0[4], a0[5], a0[6], a0[7]};
    *(LDS_AS f32x4*)(rp + 64) = f32x4{a1[0], a1[1], a1[2], a1[3]};
    *(LDS_AS f32x4*)(rp + 68) = f32x4{a1[4], a1[5], a1[6], a1[7]};
    __syncthreads();
    {
      const int o = tid >> 1, hf = tid & 1;
      float t0 = 0.f, t1 = 0.f;
#pragma unroll
      for (int g = 0; g < 16; g += 2) { t0 += red[(hf * 16 + g) * 128 + o]; t1 += red[(hf * 16 + g + 1) * 128 + o]; }
      float t = t0 + t1;
      t += __shfl_xor(t, 1);
      float* mod = (float*)(ws + OFF_MOD);
      if (hf == 0) mod[((kh * 2 + s) * 2 + (o >> 6)) * 2304 + cb * 64 + (o & 63)] = t;
    }
  } else {
    const int i = bid - K0_T1, b = i / RT, rt = i % RT;
    const u16* src = rt < 64 ? x_img + (size_t)(b * NI + rt * 16) * D : x_txt + (size_t)(b * NTX + (rt - 64) * 16) * D;
    src += (lane & 15) * D + wave * 192 + (lane >> 4) * 8;
    u32x4 v[6];
#pragma unroll
    for (int k = 0; k < 6; ++k) v[k] = *(const u32x4*)(src + k * 32);
    LDS_AS float* red = (LDS_AS float*)smem;
    float s0 = 0.f, s1 = 0.f;
#pragma unroll
    for (int k = 0; k < 6; ++k)
#pragma unroll
      for (int j = 0; j < 4; ++j) { s0 += bflo(v[k][j]); s1 += bfhi(v[k][j]); }
    const float s = red4_sum(s0 + s1);
    if (lane < 16) red[wave * 16 + lane] = s;
    __syncthreads();
    const int r = lane & 15;
    const float mean = ((red[r] + red[16 + r]) + (red[32 + r] + red[48 + r])) * (1.0f / D);
    float q0 = 0.f, q1 = 0.f;
#pragma unroll
    for (int k = 0; k < 6; ++k)
#pragma unroll
      for (int j = 0; j < 4; ++j) {
        float lo = bflo(v[k][j]) - mean, hi = bfhi(v[k][j]) - mean;
        q0 = fmaf(lo, lo, q0); q1 = fmaf(hi, hi, q1);
      }
    const float q = red4_sum(q0 + q1);
    if (lane < 16) red[64 + wave * 16 + lane] = q;
    __syncthreads();
    const float var = ((red[64 + r] + red[80 + r]) + (red[96 + r] + red[112 + r])) * (1.0f / D);
    const float rstd = 1.0f / sqrtf(var + 1e-5f);
    char* dst = ws + OFF_XN + (size_t)(i * KD + wave * 6) * 1024 + lane * 16;
#pragma unroll
    for (int k = 0; k < 6; ++k) {
      u32x4 o;
#pragma unroll
      for (int j = 0; j < 4; ++j) {
        typedef __attribute__((ext_vector_type(2))) _Float16 f16x2;
        const f16x2 h = {(_Float16)((bflo(v[k][j]) - mean) * rstd), (_Float16)((bfhi(v[k][j]) - mean) * rstd)};
        o[j] = __builtin_bit_cast(unsigned, h);
      }
      *(u32x4*)(dst + k * 1024) = o;
    }
  }
}

constexpr int FTX = 2;
constexpr int SLOT = 30 * 1024;
constexpr int P1_SB = 98304, P1_SGS = P1_SB + 4 * 12288;
constexpr int RED = P1_SB + 5 * 12288, ARR = RED + 1024, MAIN_LDS = ARR + 32;
constexpr int WP = 3 * D * 2;

__device__ __forceinline__ void fmix_lo(float& d, unsigned v, float g, float s) {
  asm volatile("v_fma_mix_f32 %0, %1, %2, %3 op_sel_hi:[1,0,0]" : "=v"(d) : "v"(v), "v"(g), "v"(s));
}
__device__ __forceinline__ void fmix_hi(float& d, unsigned v, float g, float s) {
  asm volatile("v_fma_mix_f32 %0, %1, %2, %3 op_sel:[1,0,0] op_sel_hi:[1,0,0]" : "=v"(d) : "v"(v), "v"(g), "v"(s));
}
template <int OFF>
__device__ __forceinline__ void rd_f4(f32x4& r, unsigned addr) {
  asm volatile("ds_read_b128 %0, %1 offset:%c2" : "=v"(r) : "v"(addr), "i"(OFF));
}
__device__ __forceinline__ u32x4 modulate(u32x4 x, const f32x4 (&g)[4]) {
  float t[8];
#pragma unroll
  for (int d = 0; d < 4; ++d) {
    fmix_lo(t[2 * d], x[d], g[2 + (d >> 1)][(d & 1) * 2], g[d >> 1][(d & 1) * 2]);
    fmix_hi(t[2 * d + 1], x[d], g[2 + (d >> 1)][(d & 1) * 2 + 1], g[d >> 1][(d & 1) * 2 + 1]);
  }
  return u32x4{pack2(t[0], t[1]), pack2(t[2], t[3]), pack2(t[4], t[5]), pack2(t[6], t[7])};
}

struct P2Tile { int b, ti, tj; };
__device__ __forceinline__ P2Tile p2_tile(int bid) {
  const int x = bid & 7, idx = bid >> 3, qd = x >> 1;
  P2Tile t;
  t.b = x & 1;
  if (idx == 30) { t.ti = 5; t.tj = 5; }
  else if (qd == 0) { t.ti = idx / 6; t.tj = idx % 6; }
  else if (qd == 1) { t.ti = idx / 5; t.tj = 6 + idx % 5; }
  else if (qd == 2) { t.ti = 6 + idx / 6; t.tj = 5 + idx % 6; }
  else { t.ti = 5 + idx / 5; t.tj = idx % 5; }
  return t;
}
__device__ __forceinline__ int p2_counter(const P2Tile& t, int lane) {
  const int u = min(lane, 11), sec = u < 6 ? 1 : 0, rt = (u < 6 ? t.tj * 6 + u : t.ti * 6 + u - 6);
  return rt < 64 ? C_QK + (t.b * 2 + sec) * 8 + rt / 9 : C_T + sec;
}
__device__ __forceinline__ void p2_issue(LDS_AS char* smem, rsrc_t rs, const P2Tile& t, int l, int lane, int s, int slot) {
#pragma unroll
  for (int i = 0; i < 4; ++i) {
    const int u = l * 4 + i;
    const int so = (u < 6 ? (int)OFF_K + (t.b * RT + t.tj * 6 + u) * KD * 1024 : (int)OFF_Q + (t.b * RT + t.ti * 6 + u - 6) * KD * 1024) + s * 2048;
    dma_sc1<0>(rs, smem + slot * SLOT + u * 2048, lane * 16, so);
    dma_sc1<1024>(rs, smem + slot * SLOT + u * 2048, lane * 16, so);
  }
}
__device__ __forceinline__ void p2_mfma(LDS_AS char* smem, char* __restrict__ ws, const P2Tile& t, int so, int wave, int lane,
                                        LDS_AS unsigned* arr, const Pending& pend) {
  rsrc_t rs = make_rsrc(ws, WS_END);
  const int wk = wave >> 1, wq = wave & 1, b = t.b, ti = t.ti, tj = t.tj;
  const unsigned krd = lds_addr(smem) + wk * 6144 + lane * 16;
  const unsigned qrd[3] = {lds_addr(smem) + 12288 + wq * 6144 + lane * 16, 0, 0};
  const int g = lane >> 4;
  int pso[3];
#pragma unroll
  for (int j = 0; j < 3; ++j) pso[j] = (int)OFF_P + (b * RT + ti * 6 + wq * 3 + j) * KK * 1024 + (tj * 6 + wk * 3) * 512;
  int po16 = (int)tiled_lane16(lane) + (g & 1) * 512, po8 = (int)tiled_lane(lane) + 1024;
  int sso = (int)OFF_ST + ((b * RT + ti * 6 + wq * 3) * 11 + tj) * 256 + wk * 8, svo = g < 3 ? g * 2816 + (lane & 15) * 16 : OOB;
  unsigned rw = lds_addr(smem) + RED + (wk * 64 + lane) * 4 + wq * 512, rr = lds_addr(smem) + RED + ((wk ^ 1) * 64 + (lane & 15)) * 4 + wq * 512;
  asm volatile("" : "+s"(pso[0]), "+s"(pso[1]), "+s"(pso[2]), "+s"(sso), "+v"(po16), "+v"(po8), "+v"(svo), "+v"(rw), "+v"(rr));
  f32x4 acc[3][3] = {};
  gemm12<3, 3, false, false, 2048, 1024, SLOT, 0, FTX>(acc, krd, krd + 1024, qrd, so, ws, pend, lane);
  asm volatile("s_nop 7");
  SCHED_FENCE();
  constexpr float SC = 0.036084391824351615f * 1.4426950408889634f;
  float mx[3], ls[3];
#pragma unroll
  for (int j = 0; j < 3; ++j) {
    const float m0 = max3(acc[0][j][0], acc[0][j][1], acc[0][j][2]), m1 = max3(acc[0][j][3], acc[1][j][0], acc[1][j][1]);
    const float m2 = max3(acc[1][j][2], acc[1][j][3], acc[2][j][0]), m3 = max3(acc[2][j][1], acc[2][j][2], acc[2][j][3]);
    mx[j] = red4_max(max2(max3(m0, m1, m2), m3));
  }
  {
    const float mine = g == 0 ? mx[0] : (g == 1 ? mx[1] : mx[2]);
    asm volatile("ds_write_b32 %0, %1\n\ts_waitcnt lgkmcnt(0)" ::"v"(rw), "v"(mine) : "memory");
    BARRIER();
    float o0, o1, o2;
    asm volatile("ds_read_b32 %0, %3\n\tds_read_b32 %1, %3 offset:64\n\tds_read_b32 %2, %3 offset:128\n\ts_waitcnt lgkmcnt(0)"
                 : "=&v"(o0), "=&v"(o1), "=&v"(o2) : "v"(rr) : "memory");
    mx[0] = max2(mx[0], o0) * SC; mx[1] = max2(mx[1], o1) * SC; mx[2] = max2(mx[2], o2) * SC;
  }
#pragma unroll
  for (int j = 0; j < 3; ++j) {
#pragma unroll
    for (int i = 0; i < 3; ++i)
#pragma unroll
      for (int r = 0; r < 4; ++r) acc[i][j][r] = __builtin_amdgcn_exp2f(fmaf(acc[i][j][r], SC, -mx[j]));
    __builtin_amdgcn_raw_buffer_store_b128(pair16(acc[0][j], acc[1][j]), rs, po16, pso[j], 16);
    __builtin_amdgcn_raw_buffer_store_b64(pack4(acc[2][j]), rs, po8, pso[j], 16);
    const f32x4 t4 = (acc[0][j] + acc[1][j]) + acc[2][j];
    ls[j] = red4_sum((t4[0] + t4[1]) + (t4[2] + t4[3]));
  }
  {
    const u32x2 v = {__float_as_uint(g == 0 ? mx[0] : (g == 1 ? mx[1] : mx[2])), __float_as_uint(g == 0 ? ls[0] : (g == 1 ? ls[1] : ls[2]))};
    __builtin_amdgcn_raw_buffer_store_b64(v, rs, svo, sso, 16);
  }
  BARRIER();
  if (last_wave(arr, 4, lane)) signal(ws, C_P + (b * 11 + ti) * 3 + (tj < 5 ? 0 : (tj == 5 ? 1 : 2)));
}

template <int MW>
__device__ __forceinline__ void big_p1(LDS_AS char* smem, char* __restrict__ ws, int gA, int g, int col, int m0, int wn, int lane,
                                       LDS_AS unsigned* arr, const Pending& pw, Pending& pv) {
  rsrc_t rs = make_rsrc(ws, WS_END);
  const unsigned l0 = lds_addr(smem), aa = l0 + m0 * 2048 + lane * 16;
  const int b = gA >= RT ? 1 : 0, sec = col >> 3, nt0 = (col & 7) * 6 + wn * 3;
  {
    unsigned brd[3];
#pragma unroll
    for (int n = 0; n < 3; ++n) brd[n] = l0 + 18432 + t96_rd(lane, wn * 3 + n, col & 1);
    f32x4 acc[MW][3] = {};
    gemm12<MW, 3, true, true, 0, 6144, SLOT, 0, FTX>(acc, aa, aa + 1024, brd, 2 * SLOT, ws, pw, lane);
    store_tiles<MW * 3, 16>(rs, (int)(sec ? OFF_K : OFF_Q), lane, [&](int i) { return tiled_blk(gA + m0 + i / 3, KD, nt0 + i % 3); },
                            [&](int i) { return acc[i / 3][i % 3]; });
  }
  {
    const unsigned brd[3] = {l0 + 18432 + wn * 3072 + lane * 16, 0, 0};
    f32x4 acc[MW][3] = {};
    gemm12<MW, 3, false, false, 1024, 6144, SLOT, 0, FTX>(acc, aa, aa + 1024, brd, 4 * SLOT, ws,
                                                         Pending{C_QK + (b * 2 + sec) * 8 + g, -1, arr + 2}, lane);
    store_tiles<MW * 3, 16>(rs, (int)(sec ? OFF_VT2 : OFF_VT1), lane,
                            [&](int i) { return tiled_blk(b * 48 + nt0 + i % 3, KK, gA - b * RT + m0 + i / 3); },
                            [&](int i) { return acc[i / 3][i % 3]; });
    pv = Pending{C_V + (b * 2 + sec) * 8 + (col & 7), -1, arr + 3};
  }
}

__device__ __forceinline__ void big_wg(LDS_AS char* smem, char* __restrict__ ws, const u16* __restrict__ wqkv_img,
                                       const u16* __restrict__ wo_img, const u16* __restrict__ wo_txt, int bid, int wave8, int lane) {
  const int x = bid & 7, idx = bid >> 3, b = x & 1, g = idx % 7, col = (x >> 1) * 4 + idx / 7;
  const int gA = b * RT + g * 9, grp = b * 7 + g, hb = (int)OFF_H + gA * KD * 1024;
  const int wi = idx < 24 ? idx : idx - 4, wmb = b * 6 + wi % 6, wcol = (x >> 1) * 4 + wi / 6;
  const P2Tile t2 = p2_tile(bid);
  const u32x4 srd_ws = make_srd(ws, WS_END);
  rsrc_t rs = make_rsrc(ws, WS_END);
  LDS_AS unsigned* arr = (LDS_AS unsigned*)(smem + ARR);
  if (wave8 == 7) {
    const int c2 = p2_counter(t2, lane);
    barriers(7);
    unsigned f = peek(srd_ws, C_H + grp);
    barriers(2);
    check(f, srd_ws, C_H + grp, 12);
    barriers(10);
    f = peek(srd_ws, C_W + col);
    barriers(2);
    check(f, srd_ws, C_W + col, 12);
    barriers(10);
    f = peek(srd_ws, c2);
    barriers(2);
    check(f, srd_ws, c2, 8);
    barriers(18);
    return;
  }
  if (wave8 >= 4) {
    const int l = wave8 - 4;
    rsrc_t rw = make_rsrc(wqkv_img, D * 3 * D * 2);
    rsrc_t ro = make_rsrc(wcol < 8 ? wo_img : wo_txt, D * D * 2);
    int bo[4], bw[4], av[3];
#pragma unroll
    for (int i = 0; i < 4; ++i) {
      bo[i] = (wcol & 7) * 192 + t96_src(l * 4 + i, lane, D * 2, wcol & 1);
      bw[i] = col * 192 + t96_src(l * 4 + i, lane, WP, col & 1);
    }
#pragma unroll
    for (int i = 0; i < 3; ++i) {
      const int u = min(l * 3 + i, 7), r = u * 8 + (lane >> 3);
      av[i] = (wmb * 64 + r) * WP + 2 * D * 2 + (((lane & 7) ^ ((r >> 1) & 7)) * 16);
    }
    const int wz = idx < 24 ? 0 : OOB;
    auto issueW = [&](int s, int slot) {
#pragma unroll
      for (int i = 0; i < 3; ++i) dma<0>(rw, smem + slot * SLOT + min(l * 3 + i, 7) * 1024, av[i], wz + s * 128);
#pragma unroll
      for (int i = 0; i < 4; ++i) dma<0>(ro, smem + slot * SLOT + 18432 + (l * 4 + i) * 1024, bo[i], wz + s * 64 * (D * 2));
    };
    auto issueA = [&](int s, int slot) {
#pragma unroll
      for (int i = 0; i < 3; ++i) {
        LDS_AS char* sa = smem + slot * SLOT + (l * 3 + i) * 2048;
        dma_sc1<0>(rs, sa, lane * 16, hb + ((l * 3 + i) * KD + 2 * s) * 1024);
        dma_sc1<1024>(rs, sa, lane * 16, hb + ((l * 3 + i) * KD + 2 * s) * 1024);
      }
    };
    issueW(0, 0);
    __builtin_amdgcn_s_sleep(4);
#pragma unroll
    for (int s = 1; s < 4; ++s) issueW(s, s);
    WAIT_VM(21); BARRIER();
    int slot = 4;
#pragma unroll 1
    for (int s = 4; s < 12; ++s) {
      WAIT_VM(14); BARRIER(); issueW(s, slot); slot = wrapn(slot + 1, 5);
    }
    stream12<7, 10>(slot, [&](int s, int sl) {
#pragma unroll
      for (int i = 0; i < 4; ++i) dma<0>(rw, smem + sl * SLOT + 18432 + (l * 4 + i) * 1024, bw[i], s * 64 * WP);
      issueA(s, sl);
    });
    stream12<10, 10>(slot, [&](int s, int sl) {
#pragma unroll
      for (int i = 0; i < 4; ++i) {
        const int pc = l * 4 + i;
        dma_sc1(rs, smem + sl * SLOT + 18432 + pc * 1024, lane * 16, (int)OFF_WVO + ((col * 6 + pc % 6) * KD + 2 * s + pc / 6) * 1024);
      }
      issueA(s, sl);
    });
    stream12<10, 8>(slot, [&](int s, int sl) { p2_issue(smem, rs, t2, l, lane, s, sl); });
    WAIT_VM(16); BARRIER();
    WAIT_VM(8); BARRIER();
    WAIT_VM(0); BARRIER();
    barriers(3);
    return;
  }
  const int c = wave8, wm = c >> 1, wn = c & 1;
  Pending ph{-1, -1, arr};
  if (col < 12) {
    const int W = col * 4 + c, ks = W >> 1, t0 = (W & 1) * 5;
    u32x4 xr[5], mp[4][KSPL];
#pragma unroll
    for (int i = 0; i < 4; ++i)
#pragma unroll
      for (int k = 0; k < KSPL; ++k)
        mp[i][k] = aload(srd_ws, (lane >> 4) * 32, (int)OFF_MOD + ((k * 4 + b) * 2304 + (i >> 1) * 768 + ks * 32 + (i & 1) * 4) * 4);
#pragma unroll
    for (int i = 0; i < 5; ++i) xr[i] = aload(srd_ws, lane * 16, (int)OFF_XN + ((gA + min(t0 + i, 8)) * KD + ks) * 1024);
    WAIT_VM(0);
    f32x4 gs[4];
#pragma unroll
    for (int i = 0; i < 4; ++i) {
      TIE(mp[i][0]); TIE(mp[i][1]);
      gs[i] = __builtin_bit_cast(f32x4, mp[i][0]) + __builtin_bit_cast(f32x4, mp[i][1]);
      if (i >= 2) gs[i] += 1.0f;
    }
#pragma unroll
    for (int i = 0; i < 5; ++i) {
      TIE(xr[i]);
      __builtin_amdgcn_raw_buffer_store_b128(modulate(xr[i], gs), rs, lane * 16, hb + (min(t0 + i, 8) * KD + ks) * 1024, 16);
    }
    ph.i0 = C_H + grp;
  }
  Pending pw{-1, -1, arr + 1};
  {
    const int sw = (lane & 15) >> 1, gq = lane >> 4;
    const unsigned ab = lds_addr(smem) + wm * 4096 + (lane & 15) * 128;
    unsigned brd[3];
#pragma unroll
    for (int n = 0; n < 3; ++n) brd[n] = lds_addr(smem) + 18432 + t96_rd(lane, wn * 3 + n, wcol & 1);
    f32x4 acc[2][3] = {};
    BARRIER();
    gemm12<2, 3, true, false, 0, 6144, SLOT, 0, 2>(acc, ab + ((gq ^ sw) * 16), ab + (((4 + gq) ^ sw) * 16), brd, 0, ws, ph, lane);
    if (idx < 24) {
      store_tiles<6, 16>(rs, (int)OFF_WVO, lane, [&](int i) { return tiled_blk(wcol * 6 + wn * 3 + i % 3, KD, wmb * 4 + wm * 2 + i / 3); },
                         [&](int i) { return acc[i / 3][i % 3]; });
      pw.i0 = C_W + wcol;
    }
  }
  Pending pv;
  if (wm == 0) big_p1<5>(smem, ws, gA, g, col, 0, wn, lane, arr, pw, pv);
  else big_p1<4>(smem, ws, gA, g, col, 5, wn, lane, arr, pw, pv);
  p2_mfma(smem, ws, t2, 1 * SLOT, c, lane, arr + 4, pv);
}

template <int MT, bool VT, bool FIRST>
__device__ __forceinline__ void small_p1(LDS_AS char* smem, char* __restrict__ ws, const u16* __restrict__ wsrc, int wcol, int obase,
                                         int gA, int col, int smod, int wave8, int lane, const Pending& pend) {
  constexpr int MH = MT / 2;
  const u32x4 srd_ws = make_srd(ws, WS_END);
  rsrc_t rs = make_rsrc(ws, WS_END);
  auto gtile = [&](int lt) { return lt < MH ? gA + lt : gA + RT + (lt - MH); };
  if (wave8 == 7) {
    if (FIRST) {
      u32x4 mp[12][KSPL];
#pragma unroll
      for (int h = 0; h < 12; ++h)
#pragma unroll
        for (int k = 0; k < KSPL; ++k) mp[h][k] = aload(srd_ws, lane * 16, (int)OFF_MOD + (((k * 2 + smod) * 2 + h / 6) * 2304 + (h % 6) * 256) * 4);
      WAIT_VM(0);
#pragma unroll
      for (int h = 0; h < 12; ++h) {
        TIE(mp[h][0]); TIE(mp[h][1]);
        f32x4 v = __builtin_bit_cast(f32x4, mp[h][0]) + __builtin_bit_cast(f32x4, mp[h][1]);
        if (h % 6 >= 3) v += 1.0f;
        *(LDS_AS f32x4*)(smem + P1_SGS + h * 1024 + lane * 16) = v;
      }
      WAIT_LGKM(0);
    }
    barriers(FIRST ? 15 : 14);
    return;
  }
  if (wave8 >= 4) {
    const int l = wave8 - 4;
    rsrc_t rb = VT ? rs : make_rsrc(wsrc, D * 3 * D * 2);
    int bv[4];
#pragma unroll
    for (int i = 0; i < 4; ++i) {
      const int pc = l * 4 + i;
      bv[i] = VT ? ((col * 6 + pc % 6) * KD + pc / 6) * 1024 + lane * 16 : wcol + t96_src(pc, lane, WP, col & 1);
    }
    if (VT) poll(srd_ws, C_W + col, 12);
    loader_loop<12, 5, 4, 0, FIRST>([&](int s, int slot) {
#pragma unroll
      for (int i = 0; i < 4; ++i) {
        if (VT) dma_sc1(rb, smem + P1_SB + slot * 12288 + (l * 4 + i) * 1024, bv[i], (int)OFF_WVO + s * 2048);
        else dma<0>(rb, smem + P1_SB + slot * 12288 + (l * 4 + i) * 1024, bv[i], s * 64 * WP);
      }
    });
    BARRIER();
    return;
  }
  const int c = wave8, wm = c >> 1, wn = c & 1;
  auto aoff = [&](int st) { return st * (MT * 2048); };
  if (FIRST) {
    u32x4 xr[6][MT];
#pragma unroll
    for (int k = 0; k < 6; ++k)
#pragma unroll
      for (int i = 0; i < MT; ++i) xr[k][i] = aload(srd_ws, lane * 16, (int)OFF_XN + (gtile(i) * KD + c * 6 + k) * 1024);
    BARRIER();
    WAIT_VM(0);
#pragma unroll
    for (int k = 0; k < 6; ++k) {
      f32x4 gs[2][4];
#pragma unroll
      for (int z = 0; z < 2; ++z) {
        const unsigned gp = lds_addr(smem) + P1_SGS + z * 6144 + (c * 6 + k) * 128 + (lane >> 4) * 32;
        rd_f4<0>(gs[z][0], gp); rd_f4<16>(gs[z][1], gp); rd_f4<3072>(gs[z][2], gp); rd_f4<3088>(gs[z][3], gp);
      }
      WAIT_LGKM(0);
#pragma unroll
      for (int z = 0; z < 2; ++z)
#pragma unroll
        for (int i = 0; i < 4; ++i) TIE(gs[z][i]);
#pragma unroll
      for (int i = 0; i < MT; ++i) {
        TIE(xr[k][i]);
        *(LDS_AS u32x4*)(smem + aoff(c * 3 + (k >> 1)) + i * 2048 + (k & 1) * 1024 + lane * 16) = modulate(xr[k][i], gs[i < MH ? 0 : 1]);
      }
    }
    WAIT_LGKM(0);
  }
  const unsigned aa = lds_addr(smem) + wm * MH * 2048 + lane * 16;
  unsigned brd[3];
#pragma unroll
  for (int n = 0; n < 3; ++n) brd[n] = lds_addr(smem) + P1_SB + (VT ? wn * 3072 + lane * 16 : t96_rd(lane, wn * 3 + n, col & 1));
  f32x4 acc[MH][3] = {};
  BARRIER();
  gemm12<MH, 3, !VT, !VT, 1024, 6144, 12288, MT * 2048, 4>(acc, aa, aa + 1024, brd, 0, ws, pend, lane);
  BARRIER();
  const int nt0 = (col & 7) * 6 + wn * 3;
  if (VT) store_tiles<MH * 3, 16>(rs, obase, lane, [&](int i) { return tiled_blk(wm * 48 + nt0 + i % 3, KK, gA + i / 3); },
                                  [&](int i) { return acc[i / 3][i % 3]; });
  else store_tiles<MH * 3, 16>(rs, obase, lane, [&](int i) { return tiled_blk((FIRST ? gA + wm * RT : wm * MH) + i / 3, KD, nt0 + i % 3); },
                               [&](int i) { return acc[i / 3][i % 3]; });
}

__device__ __forceinline__ void small_wg(LDS_AS char* smem, char* __restrict__ ws, const u16* __restrict__ wqkv_img,
                                         const u16* __restrict__ wqkv_txt, const u16* __restrict__ wo_img,
                                         const u16* __restrict__ wo_txt, int bid, int wave8, int lane) {
  const int x = bid & 7, idx = bid >> 3;
  const u32x4 srd_ws = make_srd(ws, WS_END);
  rsrc_t rs = make_rsrc(ws, WS_END);
  LDS_AS unsigned* arr = (LDS_AS unsigned*)(smem + ARR);
  Pending pv{-1, -1, arr + 1};
  if (bid < 240) {
    const int col = (x >> 1) * 4 + (x & 1) * 2 + (idx - 28), sec = col >> 3;
    small_p1<2, false, true>(smem, ws, wqkv_img, col * 192, (int)(sec ? OFF_K : OFF_Q), 63, col, 0, wave8, lane, Pending{-1, -1, arr});
    small_p1<2, true, false>(smem, ws, wqkv_img, 0, (int)(sec ? OFF_VT2 : OFF_VT1), 63, col, 0, wave8, lane,
                             Pending{C_QK + sec * 8 + 7, C_QK + 16 + sec * 8 + 7, arr});
    pv.i0 = C_V + sec * 8 + (col & 7); pv.i1 = pv.i0 + 16;
  } else {
    const int col = bid - 240;
    small_p1<4, false, true>(smem, ws, wqkv_txt, col * 192, (int)(col >> 3 ? OFF_K : OFF_Q), 64, col, 1, wave8, lane, Pending{-1, -1, arr});
    const Pending pq{C_T + (col >> 3), -1, arr};
    if (col < 8) {
      small_p1<4, false, false>(smem, ws, wqkv_txt, 3072 + col * 192, (int)OFF_VX, 64, col, 1, wave8, lane, pq);
      pv.i0 = C_T + 2;
    } else if (wave8 < 4) {
      flush(ws, pq, lane);
    }
  }
  if (wave8 < 4) flush(ws, pv, lane);
  if (bid < 242) {
    const P2Tile t2 = p2_tile(bid);
    if (wave8 == 7) barriers(15);
    else if (wave8 >= 4) {
      poll(srd_ws, p2_counter(t2, lane), 8);
      loader_loop<12, 5, 8>([&](int s, int slot) { p2_issue(smem, rs, t2, wave8 - 4, lane, s, slot); });
      barriers(2);
    } else {
      BARRIER();
      p2_mfma(smem, ws, t2, 0, wave8, lane, arr + 4, Pending{-1, -1, arr});
    }
  } else if (bid < 254) {
    const int e = bid - 242, wave = wave8 & 3;
    if (wave8 >= 4) {
      rsrc_t rb = make_rsrc(e < 6 ? wo_img : wo_txt, D * D * 2);
      const int bv0 = (e % 6) * 256 + wave * 16 * (D * 2) + timg_src(lane, 0, D * 2);
      const int bv1 = (e % 6) * 256 + (wave * 16 + 8) * (D * 2) + timg_src(lane, 1, D * 2);
      poll(srd_ws, C_T + 2, 8);
      loader_loop<12, 5, 6>([&](int s, int slot) {
        LDS_AS char* sb = smem + slot * SLOT;
        dma_sc1<0>(rs, sb + wave * 2048, lane * 16, (int)OFF_VX + (wave * KD + 2 * s) * 1024);
        dma_sc1<1024>(rs, sb + wave * 2048, lane * 16, (int)OFF_VX + (wave * KD + 2 * s) * 1024);
#pragma unroll
        for (int i = 0; i < 4; ++i) dma<0>(rb, sb + 8192 + (wave * 4 + i) * 1024, ((i >> 1) ? bv1 : bv0) + (i & 1) * 128, s * 64 * (D * 2));
      });
    } else {
      const int wm = wave >> 1, wn = wave & 1;
      const unsigned aa = lds_addr(smem) + wm * 4096 + lane * 16;
      unsigned brd[4];
#pragma unroll
      for (int n = 0; n < 4; ++n) brd[n] = lds_addr(smem) + 8192 + timg_rd<2>(lane, wn * 4 + n);
      f32x4 acc[2][4] = {};
      BARRIER();
      gemm12<2, 4, true, false, 0, 8192, SLOT, 0, FTX>(acc, aa, aa + 1024, brd, 0, ws, Pending{-1, -1, arr}, lane);
      store_tiles<8, 16>(rs, (int)(e < 6 ? OFF_VT1 : OFF_VT2), lane,
                         [&](int i) { return tiled_blk(wm * 48 + (e % 6) * 8 + wn * 4 + (i & 3), KK, 64 + (i >> 2)); },
                         [&](int i) { return acc[i >> 2][i & 3]; });
      if (last_wave(arr + 4, 4, lane)) signal(ws, C_X + e);
    }
  }
}

constexpr int K3_NS = 4, K3_STAGE = 35 * 1024, K3_ST = 33 * 1024;
__device__ __forceinline__ void fmac(f32x4& a, const f32x4& x, float f, int e) {
  float a0 = a[e], a1 = a[e + 1];
  asm volatile("v_fmac_f32 %0, %2, %4\n\tv_fmac_f32 %1, %3, %4" : "+v"(a0), "+v"(a1) : "v"(x[e]), "v"(x[e + 1]), "v"(f));
  a[e] = a0; a[e + 1] = a1;
}
template <int QT>
__device__ __forceinline__ void k3_mfma(LDS_AS char* smem, int wn, int qoff, int nb, int lane, u16* __restrict__ outp) {
  const unsigned vrd = lds_addr(smem) + wn * 9216 + lane * 16, prd = lds_addr(smem) + 18432 + qoff * 3072 + lane * 16;
  unsigned srd[QT];
#pragma unroll
  for (int j = 0; j < QT; ++j) srd[j] = lds_addr(smem) + K3_ST + (qoff + j) * 256 + (qoff + j >= 3 ? 256 : 0) + (lane & 15) * 16;
  f32x4 acc[3][QT] = {}, t0[3][QT], t1[3][QT] = {};
  f32x4 st[QT];
  float R[QT], L[QT], w[QT];
#pragma unroll
  for (int j = 0; j < QT; ++j) { R[j] = -1e30f; L[j] = 0.f; st[j] = f32x4{-1e30f, 0.f, 0.f, 0.f}; }
  Frags<3, QT> f0, f1, f2;
  unsigned bc[QT];
  bc[0] = prd;
  BARRIER();
  rd_set<3, QT, false, 3072, 3072, 0, 0>(f0, vrd, bc);
  rd_set<3, QT, false, 3072, 3072, 1024, 1024>(f1, vrd, bc);
  int so = 0;
  auto weigh = [&](auto sc, const f32x4 (&sp)[QT]) {
    constexpr int S = decltype(sc)::value;
    if constexpr (S == 0) {
      bool big = false;
#pragma unroll
      for (int j = 0; j < QT; ++j) { w[j] = sp[j][0] - R[j]; big |= w[j] > 64.f; }
      if (__builtin_amdgcn_ballot_w64(big) != 0) {
#pragma unroll
        for (int j = 0; j < QT; ++j) {
          const bool up = w[j] > 64.f;
          const float sc2 = up ? __builtin_amdgcn_exp2f(-w[j]) : 1.0f;
          R[j] = up ? sp[j][0] : R[j];
          w[j] = up ? 0.f : w[j];
          L[j] *= sc2;
#pragma unroll
          for (int i = 0; i < 3; ++i) acc[i][j] *= sc2;
        }
      }
    } else if constexpr (S <= QT) {
      w[S - 1] = __builtin_amdgcn_exp2f(w[S - 1]);
      tie1(w[S - 1]);
    } else if constexpr (S <= 2 * QT) {
      L[S - QT - 1] = fmaf(w[S - QT - 1], sp[S - QT - 1][1] + sp[S - QT - 1][3], L[S - QT - 1]);
      tie1(L[S - QT - 1]);
    }
  };
  auto stage = [&](f32x4 (&cur)[3][QT], f32x4 (&prv)[3][QT]) {
    BARRIER();
    f32x4 sp[QT];
#pragma unroll
    for (int j = 0; j < QT; ++j) { sp[j] = st[j]; rd_f4<0>(st[j], srd[j] + so); }
    kstep<3, QT, false, false, true, 3072, 3072, 2048, 2048, true>(cur, f0, f2, vrd + so, bc, [&](auto ic) { weigh(ic, sp); });
    so = wrapn(so + K3_STAGE, K3_NS * K3_STAGE);
    bc[0] = prd + so;
    auto fold = [&](auto sc) {
      constexpr int S = decltype(sc)::value, T = S / 2, E = (S % 2) * 2;
      fmac(acc[T / QT][T % QT], prv[T / QT][T % QT], w[T % QT], E);
    };
    kstep<3, QT, false, false, false, 3072, 3072, 0, 0, true>(cur, f1, f0, vrd + so, bc, [&](auto ic) {
      if constexpr (decltype(ic)::value == 0) {
#pragma unroll
        for (int j = 0; j < QT; ++j) tie4(st[j]);
      }
      fold(ic);
    });
    kstep<3, QT, false, false, false, 3072, 3072, 1024, 1024, true>(cur, f2, f1, vrd + so, bc, [&](auto ic) {
      fold(std::integral_constant<int, 3 * QT + decltype(ic)::value>{});
    });
  };
#pragma unroll 1
  for (int t = 0; t < 10; t += 2) {
    stage(t0, t1);
    stage(t1, t0);
  }
  stage(t0, t1);
  land<3, QT, false>(f0);
  land<3, QT, false>(f1);
  static_for<0, 2 * QT + 1>([&](auto sc) { weigh(sc, st); });
  LDS_AS char* xs = smem + 3 * K3_STAGE;
  const int cw = wn * 48 + (lane >> 4) * 4;
  f32x4 o[QT * 3];
#pragma unroll
  for (int j = 0; j < QT; ++j) L[j] = 1.0f / L[j];
#pragma unroll
  for (int i = 0; i < 3; ++i) {
    const f32x4 gate = *(LDS_AS f32x4*)(xs + 15360 + (cw + i * 16) * 4) + *(LDS_AS f32x4*)(xs + 15360 + 384 + (cw + i * 16) * 4);
#pragma unroll
    for (int j = 0; j < QT; ++j) {
      const u32x2 xv = *(LDS_AS u32x2*)(xs + ((qoff + j) * 16 + (lane & 15)) * 192 + (cw + i * 16) * 2);
      const f32x4 a = (acc[i][j] + t0[i][j] * w[j]) * L[j];
      o[j * 3 + i] = f32x4{fmaf(gate[0], a[0], bflo(xv[0])), fmaf(gate[1], a[1], bfhi(xv[0])),
                           fmaf(gate[2], a[2], bflo(xv[1])), fmaf(gate[3], a[3], bfhi(xv[1]))};
    }
  }
  const bool odd = (lane >> 4) & 1;
  u16* ob = outp + (size_t)(qoff * 16 + (lane & 15)) * D + nb * 96 + wn * 48;
  auto toff = [&](int i) { return (i / 3) * 16 * D + (i % 3) * 16; };
#pragma unroll
  for (int i = 0; i + 1 < QT * 3; i += 2) *(u32x4*)(ob + (odd ? toff(i + 1) : toff(i)) + (lane >> 5) * 8) = pair16(o[i], o[i + 1]);
  if (QT & 1) *(u32x2*)(ob + toff(QT * 3 - 1) + (lane >> 4) * 4) = pack4(o[QT * 3 - 1]);
}

__device__ __forceinline__ void phase3(LDS_AS char* smem, char* __restrict__ ws, const u16* __restrict__ x_img,
                                       const u16* __restrict__ x_txt, u16* __restrict__ out, int bid, int wave8, int lane) {
  const int wave = wave8 & 3, x = bid & 7, idx = bid >> 3, b = x & 1, qd = x >> 1;
  int rt0, nrt, nb, txt;
  if (idx < 26) {
    const int u = (qd >> 1) * 26 + idx, rb = u >> 2;
    nb = (qd & 1) * 4 + (u & 3); rt0 = rb * 5; nrt = rb == 12 ? 4 : 5; txt = 0;
  } else {
    nb = qd * 2 + idx - 26; rt0 = 64; nrt = 2; txt = 1;
  }
  if (wave8 >= 4) {
    rsrc_t rs = make_rsrc(ws, WS_END);
    const u32x4 srd_ws = make_srd(ws, WS_END);
    const bool rev = qd == 1 || qd == 2;
    auto ktile = [&](int t) { return rev ? (t < 5 ? 6 + t : (t == 5 ? 5 : t - 6)) : t; };
    auto tail = [&](int slot) {
      const u16* xres = txt ? x_txt + (size_t)b * NTX * D : x_img + (size_t)(b * NI + rt0 * 16) * D;
      rsrc_t rx = make_rsrc(xres, nrt * 16 * D * 2);
      LDS_AS char* sb = smem + slot * K3_STAGE + wave * 4096;
#pragma unroll
      for (int i = 0; i < 4; ++i) {
        const int c = (wave * 4 + i) * 64 + lane;
        if (wave * 4 + i < 15) dma<0>(rx, sb + i * 1024, (c / 12) * (D * 2) + nb * 192 + (c % 12) * 16, 0);
        else dma<0>(rs, sb + i * 1024, lane < 24 * KSPL ? (int)OFF_MOD + ((((lane / 24) * 2 + txt) * 2 + b) * 2304 + 1536 + nb * 96 + (lane % 24) * 4) * 4 : OOB, 0);
      }
    };
    if (wave < 2) {
      poll(srd_ws, C_V + (b * 2 + txt) * 8 + nb, 8);
      const int vb = (int)(txt ? OFF_VT2 : OFF_VT1) + (b * 48 + nb * 6 + wave * 3) * KK * 1024;
      loader_loop<11, K3_NS, 9, 4>([&](int t, int slot) {
        const int kt = ktile(t);
        if (kt == 10) poll(srd_ws, C_X + txt * 6 + (nb * 96 + (lane & 1) * 95) / 128, 1);
        LDS_AS char* sb = smem + slot * K3_STAGE + wave * 9216;
#pragma unroll
        for (int i = 0; i < 3; ++i) {
          const int so = vb + i * KK * 1024 + kt * 3072;
          dma_sc1<0>(rs, sb + i * 3072, lane * 16, so);
          dma_sc1<1024>(rs, sb + i * 3072, lane * 16, so);
          dma_sc1<2048>(rs, sb + i * 3072, lane * 16, so);
        }
      }, NoHook{}, tail);
    } else {
      const int cp = C_P + (b * 11 + (rt0 * 16 + (lane & 1) * (nrt * 16 - 1)) / 96) * 3;
      poll(srd_ws, cp + (rev ? 2 : 0), 5);
      const int pb = (int)OFF_P + (b * RT + rt0) * KK * 1024;
      const int su = (wave - 2) * 3 + (lane >> 4);
      const int sv = (su < nrt && (lane >> 4) < 3) ? (int)OFF_ST + (b * RT + rt0 + su) * 2816 + (lane & 15) * 16 : OOB;
      loader_loop<11, K3_NS, 9, 4>([&](int t, int slot) {
        if (t == 5) poll(srd_ws, cp + ((lane & 2) ? 1 : (rev ? 0 : 2)), (lane & 2) ? 1 : 5);
        const int kt = ktile(t);
#pragma unroll
        for (int i = 0; i < 8; ++i) {
          const int pc = (wave - 2) * 8 + i, u = pc / 3, ks = pc % 3;
          if (i < 7 || wave == 2)
            dma_sc1(rs, smem + slot * K3_STAGE + 18432 + pc * 1024, lane * 16, u < nrt ? pb + (u * KK + kt * 3 + ks) * 1024 : OOB);
          else
            dma_sc1(rs, smem + RED, OOB, 0);
        }
        dma_sc1(rs, smem + slot * K3_STAGE + K3_ST + (wave - 2) * 1024, sv, kt * 256);
      }, NoHook{}, tail);
    }
    return;
  }
  u16* outp = txt ? out + (size_t)2 * NI * D + (size_t)b * NTX * D : out + (size_t)(b * NI + rt0 * 16) * D;
  const int wq = wave >> 1, q0 = (nrt + 1) >> 1;
  const int qt = wq ? nrt - q0 : q0, qoff = wq ? q0 : 0;
  if (qt == 3) k3_mfma<3>(smem, wave & 1, qoff, nb, lane, outp);
  else if (qt == 2) k3_mfma<2>(smem, wave & 1, qoff, nb, lane, outp);
  else k3_mfma<1>(smem, wave & 1, qoff, nb, lane, outp);
}

__global__ __launch_bounds__(512, 2) void kmain(const u16* __restrict__ x_img, const u16* __restrict__ x_txt,
                                                const u16* __restrict__ wqkv_img, const u16* __restrict__ wqkv_txt,
                                                const u16* __restrict__ wo_img, const u16* __restrict__ wo_txt,
                                                char* __restrict__ ws, u16* __restrict__ out) {
  __shared__ __attribute__((aligned(1024))) char smem_[MAIN_LDS];
  LDS_AS char* smem = (LDS_AS char*)smem_;
  const int bid = blockIdx.x, tid = threadIdx.x, lane = tid & 63;
  const int wave = __builtin_amdgcn_readfirstlane(tid >> 6);
  asm volatile(".Lmmdit_main_begin:");
  if (tid < 8) ((LDS_AS unsigned*)(smem + ARR))[tid] = 0u;
  WAIT_LGKM(0);
  if (bid < 224) {
    big_wg(smem, ws, wqkv_img, wo_img, wo_txt, bid, wave, lane);
    phase3(smem, ws, x_img, x_txt, out, bid, wave, lane);
  } else {
    small_wg(smem, ws, wqkv_img, wqkv_txt, wo_img, wo_txt, bid, wave, lane);
  }
  asm volatile(".Lmmdit_main_end:");
}

extern "C" void kernel_launch(void* const* d_in, const int* in_sizes, int n_in, void* d_out, int out_size, void* d_ws,
                              size_t ws_size, hipStream_t stream) {
  const u16* x_img = (const u16*)d_in[0];
  const u16* x_txt = (const u16*)d_in[1];
  const u16* cvec = (const u16*)d_in[2];
  const u16* wqkv_img = (const u16*)d_in[3];
  const u16* wqkv_txt = (const u16*)d_in[4];
  const u16* wo_img = (const u16*)d_in[5];
  const u16* wo_txt = (const u16*)d_in[6];
  const u16* wmod_img = (const u16*)d_in[7];
  const u16* wmod_txt = (const u16*)d_in[8];
  char* ws = (char*)d_ws;
  k0_prep<<<K0_T1 + K0_T2 + K0_Z + K0_PF, 256, 0, stream>>>(x_img, x_txt, cvec, wmod_img, wmod_txt, wqkv_img, wqkv_txt, wo_img, wo_txt, ws);
  kmain<<<256, 512, 0, stream>>>(x_img, x_txt, wqkv_img, wqkv_txt, wo_img, wo_txt, ws, (u16*)d_out);
}
```

```cpp
#include <hip/hip_runtime.h>
#include <hip/hip_bf16.h>
#include <stdint.h>
#include <type_traits>

typedef __attribute__((ext_vector_type(8))) __bf16 bf16x8;
typedef __attribute__((ext_vector_type(4))) __bf16 bf16x4;
typedef __attribute__((ext_vector_type(2))) __bf16 bf16x2;
typedef __attribute__((ext_vector_type(4))) float f32x4;
typedef __attribute__((ext_vector_type(2))) float f32x2;
typedef __attribute__((ext_vector_type(4))) unsigned u32x4;
typedef __attribute__((ext_vector_type(2))) unsigned u32x2;
#define LDS_AS __attribute__((address_space(3)))
typedef __amdgpu_buffer_rsrc_t rsrc_t;
typedef unsigned short u16;

constexpr int D = 768, NI = 1024, NTX = 32;
constexpr int RT = 66;
constexpr int KD = 24;
constexpr int KK = 33;
constexpr int KSPL = 2;
constexpr int OOB = 0x7f000000;

constexpr unsigned SZ_TOK = 2u * RT * KD * 1024u;
constexpr unsigned SZ_VT = 2u * 48u * KK * 1024u;
constexpr unsigned OFF_XN = 0;
constexpr unsigned OFF_Q = OFF_XN + SZ_TOK;
constexpr unsigned OFF_K = OFF_Q + SZ_TOK;
constexpr unsigned OFF_VX = OFF_K + SZ_TOK;
constexpr unsigned OFF_VT1 = OFF_VX + 4u * KD * 1024u;
constexpr unsigned OFF_VT2 = OFF_VT1 + SZ_VT;
constexpr unsigned OFF_P = OFF_VT2 + SZ_VT;
constexpr unsigned OFF_WVO = OFF_P + 2u * RT * KK * 1024u;
constexpr unsigned OFF_MOD = OFF_WVO + 96u * KD * 1024u;
constexpr unsigned OFF_ST = OFF_MOD + KSPL * 4u * 2304u * 4u;
constexpr unsigned OFF_CNT = OFF_ST + 2u * RT * 11u * 256u;
constexpr unsigned OFF_H = OFF_CNT + 24576u;
constexpr unsigned WS_END = OFF_H + SZ_TOK;
constexpr int C_H = 0;
constexpr int C_QK = 14;
constexpr int C_T = 46;
constexpr int C_V = 49;
constexpr int C_X = 81;
constexpr int C_P = 93;
constexpr int C_W = 159;
constexpr unsigned SZ_CNT = 24576u;

#define WAIT_VM(n) asm volatile("s_waitcnt vmcnt(%0)" ::"n"(n) : "memory")
#define WAIT_LGKM(n) asm volatile("s_waitcnt lgkmcnt(%0)" ::"n"(n) : "memory")
#define BARRIER() __builtin_amdgcn_s_barrier()
#define TIE(x) asm volatile("" : "+v"(x))
#define SCHED_FENCE() __builtin_amdgcn_sched_barrier(0)

template <int I, int N, class F>
__device__ __forceinline__ void static_for(F&& f) {
  if constexpr (I < N) { f(std::integral_constant<int, I>{}); static_for<I + 1, N>(f); }
}
__device__ __forceinline__ rsrc_t make_rsrc(const void* p, unsigned bytes) {
  return __builtin_amdgcn_make_buffer_rsrc((void*)p, 0, (int)bytes, 0x00020000);
}
__device__ __forceinline__ u32x4 make_srd(const void* p, unsigned bytes) {
  unsigned long long a = (unsigned long long)p;
  u32x4 r;
  r[0] = (unsigned)a; r[1] = (unsigned)(a >> 32) & 0xffffu; r[2] = bytes; r[3] = 0x00020000u;
  return r;
}
template <int IMM>
__device__ __forceinline__ void dma(rsrc_t rs, LDS_AS char* lds, int voff, int soff) {
  __builtin_amdgcn_raw_ptr_buffer_load_lds(rs, (LDS_AS void*)lds, 16, voff, soff, IMM, 0);
}
template <int IMM = 0>
__device__ __forceinline__ void dma_sc1(rsrc_t rs, LDS_AS char* lds, int voff, int soff) {
  __builtin_amdgcn_raw_ptr_buffer_load_lds(rs, (LDS_AS void*)lds, 16, voff, soff, IMM, 16);
}
__device__ __forceinline__ u32x4 aload(const u32x4& srd, int voff, int soff) {
  u32x4 r;
  asm volatile("buffer_load_dwordx4 %0, %1, %2, %3 offen" : "=v"(r) : "v"(voff), "s"(srd), "s"(soff));
  return r;
}
__device__ __forceinline__ u32x4 aload_sc1(const u32x4& srd, int voff, int soff) {
  u32x4 r;
  asm volatile("buffer_load_dwordx4 %0, %1, %2, %3 offen sc1" : "=v"(r) : "v"(voff), "s"(srd), "s"(soff));
  return r;
}
__device__ __forceinline__ void signal(char* ws, int idx) {
  asm volatile("global_atomic_add %0, %1, %2" ::"v"((int)OFF_CNT + idx * 128), "v"(1u), "s"(ws) : "memory");
}
__device__ __forceinline__ bool last_wave(LDS_AS unsigned* word, unsigned n, int lane) {
  WAIT_VM(0);
  unsigned old = 0;
  if (lane == 0) asm volatile("ds_add_rtn_u32 %0, %1, %2\n\ts_waitcnt lgkmcnt(0)" : "=v"(old) : "v"((unsigned)(size_t)word), "v"(1u) : "memory");
  return lane == 0 && old == n - 1;
}
struct Pending { int i0, i1; LDS_AS unsigned* arr; };
__device__ __forceinline__ void flush(char* ws, const Pending& p, int lane) {
  if (p.i0 >= 0 && last_wave(p.arr, 4, lane)) { signal(ws, p.i0); if (p.i1 >= 0) signal(ws, p.i1); }
}
__device__ __forceinline__ void poll(const u32x4& srd, int idx, unsigned target) {
#pragma unroll 1
  for (int spin = 0; spin < (1 << 20); ++spin) {
    unsigned f;
    asm volatile("buffer_load_dword %0, %1, %2, 0 offen sc1\n\ts_waitcnt vmcnt(0)" : "=v"(f) : "v"((int)OFF_CNT + idx * 128), "s"(srd) : "memory");
    if (__builtin_amdgcn_ballot_w64(f < target) == 0) break;
    __builtin_amdgcn_s_sleep(1);
  }
}
__device__ __forceinline__ unsigned peek(const u32x4& srd, int idx) {
  unsigned f;
  asm volatile("buffer_load_dword %0, %1, %2, 0 offen sc1" : "=v"(f) : "v"((int)OFF_CNT + idx * 128), "s"(srd) : "memory");
  return f;
}
__device__ __forceinline__ void check(unsigned f, const u32x4& srd, int idx, unsigned target) {
  WAIT_VM(0);
  TIE(f);
  if (__builtin_amdgcn_ballot_w64(f < target) != 0) poll(srd, idx, target);
}
__device__ __forceinline__ void barriers(int n) {
#pragma unroll 1
  for (int t = 0; t < n; ++t) BARRIER();
}
template <int OFF>
__device__ __forceinline__ void rd_tr(bf16x4& lo, bf16x4& hi, unsigned addr) {
  asm volatile("ds_read_b64_tr_b16 %0, %2 offset:%c3\n\tds_read_b64_tr_b16 %1, %2 offset:%c4"
               : "=&v"(lo), "=&v"(hi) : "v"(addr), "i"(OFF), "i"(OFF + 512));
}
template <int OFF>
__device__ __forceinline__ void rd_128(bf16x8& r, unsigned addr) {
  asm volatile("ds_read_b128 %0, %1 offset:%c2" : "=v"(r) : "v"(addr), "i"(OFF));
}
template <int OFF>
__device__ __forceinline__ void rd_32(float& r, unsigned addr) {
  asm volatile("ds_read_b32 %0, %1 offset:%c2" : "=v"(r) : "v"(addr), "i"(OFF));
}
__device__ __forceinline__ void tie1(float& x) { TIE(x); }
__device__ __forceinline__ void tie4(f32x4& x) { TIE(x); }
__device__ __forceinline__ void tie8(bf16x8& x) { TIE(x); }
__device__ __forceinline__ void tie4h(bf16x4& x) { TIE(x); }
__device__ __forceinline__ bf16x8 cat(bf16x4 lo, bf16x4 hi) { return __builtin_shufflevector(lo, hi, 0, 1, 2, 3, 4, 5, 6, 7); }
__device__ __forceinline__ unsigned lds_addr(LDS_AS char* p) { return (unsigned)(size_t)p; }
__device__ __forceinline__ int wrapn(int v, int n) { return v >= n ? v - n : v; }
__device__ __forceinline__ f32x4 mfma(bf16x8 a, bf16x8 b, f32x4 c) {
  return __builtin_amdgcn_mfma_f32_16x16x32_bf16(a, b, c, 0, 0, 0);
}
__device__ __forceinline__ unsigned pack2(float a, float b) {
  f32x2 f = {a, b};
  bf16x2 h = __builtin_convertvector(f, bf16x2);
  return __builtin_bit_cast(unsigned, h);
}
__device__ __forceinline__ u32x2 pack4(f32x4 v) { return u32x2{pack2(v[0], v[1]), pack2(v[2], v[3])}; }
__device__ __forceinline__ float bflo(unsigned v) { return __uint_as_float(v << 16); }
__device__ __forceinline__ float bfhi(unsigned v) { return __uint_as_float(v & 0xffff0000u); }
__device__ __forceinline__ float max2(float a, float b) { float r; asm("v_max_f32 %0, %1, %2" : "=v"(r) : "v"(a), "v"(b)); return r; }
__device__ __forceinline__ float max3(float a, float b, float c) { float r; asm("v_max3_f32 %0, %1, %2, %3" : "=v"(r) : "v"(a), "v"(b), "v"(c)); return r; }
__device__ __forceinline__ float red4_max(float x) {
  auto a = __builtin_amdgcn_permlane16_swap(__float_as_uint(x), __float_as_uint(x), false, false);
  x = max2(__uint_as_float(a[0]), __uint_as_float(a[1]));
  auto b = __builtin_amdgcn_permlane32_swap(__float_as_uint(x), __float_as_uint(x), false, false);
  return max2(__uint_as_float(b[0]), __uint_as_float(b[1]));
}
__device__ __forceinline__ float red4_sum(float x) {
  auto a = __builtin_amdgcn_permlane16_swap(__float_as_uint(x), __float_as_uint(x), false, false);
  x = __uint_as_float(a[0]) + __uint_as_float(a[1]);
  auto b = __builtin_amdgcn_permlane32_swap(__float_as_uint(x), __float_as_uint(x), false, false);
  return __uint_as_float(b[0]) + __uint_as_float(b[1]);
}
__device__ __forceinline__ unsigned tiled_lane(int lane) {
  int g = lane >> 4;
  return (unsigned)(((lane & 15) + 16 * (g >> 1)) * 16 + (g & 1) * 8);
}
__device__ __forceinline__ unsigned tiled_blk(int rowtile, int KS, int T16) {
  return (unsigned)(rowtile * KS * 1024 + T16 * 512);
}
__device__ __forceinline__ u32x4 pair16(f32x4 x, f32x4 y) {
  const u32x2 px = pack4(x), py = pack4(y);
  auto a = __builtin_amdgcn_permlane16_swap(px[0], py[0], false, false);
  auto b = __builtin_amdgcn_permlane16_swap(px[1], py[1], false, false);
  return u32x4{a[0], b[0], a[1], b[1]};
}
__device__ __forceinline__ unsigned tiled_lane16(int lane) { return (unsigned)(((lane & 15) + 16 * (lane >> 5)) * 16); }
template <int NTILE, int AUX, class Blk, class Val>
__device__ __forceinline__ void store_tiles(rsrc_t rs, int base, int lane, Blk&& blk, Val&& val) {
  const bool odd = (lane >> 4) & 1;
  const int l16 = (int)tiled_lane16(lane);
  const int sb = __builtin_amdgcn_readfirstlane(base);
#pragma unroll
  for (int i = 0; i + 1 < NTILE; i += 2) {
    const int so = __builtin_amdgcn_readfirstlane(sb - 0x100000 + (int)blk(i));
    const int d = __builtin_amdgcn_readfirstlane((int)(blk(i + 1) - blk(i)));
    __builtin_amdgcn_raw_buffer_store_b128(pair16(val(i), val(i + 1)), rs, l16 + 0x100000 + (odd ? d : 0), so, AUX);
  }
  if (NTILE & 1)
    __builtin_amdgcn_raw_buffer_store_b64(pack4(val(NTILE - 1)), rs, (int)tiled_lane(lane),
                                          __builtin_amdgcn_readfirstlane(sb + (int)blk(NTILE - 1)), AUX);
}
__device__ __forceinline__ int timg_src(int lane, int sigma, int pitch) {
  int r = lane >> 3, c = (lane & 7) ^ (2 * ((r >> 1) & 1) + 4 * sigma);
  return r * pitch + c * 16;
}
template <int NB>
__device__ __forceinline__ int timg_rd(int lane, int nt) {
  int g = lane >> 4, q = (lane & 15) >> 2, p = lane & 3;
  int f = 2 * ((q >> 1) & 1) + 4 * (g & 1);
  return (g * NB + (nt >> 2)) * 1024 + q * 128 + (((2 * (nt & 3)) ^ f) + (p >> 1)) * 16 + (p & 1) * 8;
}

template <int MT, int NT>
struct Frags { bf16x8 a[MT]; bf16x8 b[NT]; bf16x4 l[NT], h[NT]; };

template <int MT, int NT, bool BTR, int AM, int BM, int AO, int BO, int I>
__device__ __forceinline__ void rd_item(Frags<MT, NT>& n, unsigned ab, const unsigned (&bb)[NT]) {
  if constexpr (I < NT) {
    if constexpr (BTR) rd_tr<BO>(n.l[I], n.h[I], bb[I]);
    else rd_128<BO + I * BM>(n.b[I], bb[0]);
  } else if constexpr (I < NT + MT) {
    rd_128<AO + (I - NT) * AM>(n.a[I - NT], ab);
  }
}
template <int MT, int NT, bool BTR, int AM, int BM, int AO, int BO>
__device__ __forceinline__ void rd_set(Frags<MT, NT>& n, unsigned ab, const unsigned (&bb)[NT]) {
  static_for<0, MT + NT>([&](auto ic) { rd_item<MT, NT, BTR, AM, BM, AO, BO, decltype(ic)::value>(n, ab, bb); });
}
template <int MT, int NT, bool BTR, bool AH = false>
__device__ __forceinline__ void land(Frags<MT, NT>& c) {
  WAIT_LGKM(AH ? MT + (BTR ? 2 : 1) * NT : 0);
#pragma unroll
  for (int j = 0; j < NT; ++j) {
    if constexpr (BTR) { TIE(c.l[j]); TIE(c.h[j]); } else TIE(c.b[j]);
  }
#pragma unroll
  for (int m = 0; m < MT; ++m) TIE(c.a[m]);
  SCHED_FENCE();
}
template <int MT, int NT, bool BTR, bool SWAP, bool ZC, int AM, int BM, int AO, int BO, bool AH = false, class X>
__device__ __forceinline__ void kstep(f32x4 (&acc)[MT][NT], Frags<MT, NT>& c, Frags<MT, NT>& n, unsigned ab,
                                      const unsigned (&bb)[NT], X&& extra) {
  land<MT, NT, BTR, AH>(c);
  static_for<0, MT * NT>([&](auto ic) {
    constexpr int I = decltype(ic)::value, m = I / NT, j = I % NT;
    bf16x8 bf;
    if constexpr (BTR) bf = cat(c.l[j], c.h[j]); else bf = c.b[j];
    const f32x4 ci = ZC ? f32x4{0.f, 0.f, 0.f, 0.f} : acc[m][j];
    acc[m][j] = SWAP ? mfma(bf, c.a[m], ci) : mfma(c.a[m], bf, ci);
    tie4(acc[m][j]);
    rd_item<MT, NT, BTR, AM, BM, AO, BO, I>(n, ab, bb);
    extra(ic);
    SCHED_FENCE();
  });
  static_for<MT * NT, (MT + NT > MT * NT ? MT + NT : MT * NT)>(
      [&](auto ic) { rd_item<MT, NT, BTR, AM, BM, AO, BO, decltype(ic)::value>(n, ab, bb); });
}
struct NoExtra { template <class T> __device__ __forceinline__ void operator()(T) const {} };

struct NoHook { __device__ __forceinline__ void operator()() const {} };
struct NoTail { __device__ __forceinline__ void operator()(int) const {} };
template <int NST, int NS, int V, int TV = 0, bool XB = false, class Issue, class Hook = NoHook, class Tail = NoTail, class Pre = NoHook>
__device__ __forceinline__ void loader_loop(Issue&& issue, Hook&& hook = NoHook{}, Tail&& tail = NoTail{}, Pre&& pre = NoHook{}) {
#pragma unroll
  for (int s = 0; s < NS - 1; ++s) issue(s, s);
  if (XB) BARRIER();
  pre();
  WAIT_VM(V * (NS - 2));
  BARRIER();
  hook();
  int slot = NS - 1;
#pragma unroll 1
  for (int t = 0; t <= NST - NS; ++t) {
    WAIT_VM(V * (NS - 3));
    BARRIER();
    issue(t + NS - 1, slot);
    slot = wrapn(slot + 1, NS);
  }
  static_for<1, NS>([&](auto rc) {
    constexpr int R = decltype(rc)::value;
    WAIT_VM(R == NS - 1 ? 0 : V * (NS - 2 - R) + (R > 1 ? TV : 0));
    BARRIER();
    if constexpr (R == 1) tail(slot);
  });
}

template <int VP, int V, class Issue>
__device__ __forceinline__ void stream12(int& slot, Issue&& issue) {
  WAIT_VM(2 * VP); BARRIER(); issue(0, slot); slot = wrapn(slot + 1, 5);
  WAIT_VM(VP + V); BARRIER(); issue(1, slot); slot = wrapn(slot + 1, 5);
#pragma unroll 1
  for (int t = 2; t < 12; ++t) {
    WAIT_VM(2 * V); BARRIER(); issue(t, slot); slot = wrapn(slot + 1, 5);
  }
}

template <int MW, int NW, bool BTR, bool SWAP, int BM, int BKS, int SLOT, int ASTEP, int FT, class Prev = NoHook>
__device__ __forceinline__ void gemm12(f32x4 (&acc)[MW][NW], unsigned a0, unsigned a1, const unsigned (&brd)[NW], int so,
                                       char* __restrict__ ws, const Pending& pend, int lane, Prev&& prev = NoHook{}) {
  Frags<MW, NW> x0, x1, y0, y1;
  unsigned bc[NW];
#pragma unroll
  for (int n = 0; n < NW; ++n) bc[n] = brd[n] + so;
  rd_set<MW, NW, BTR, 2048, BM, 0, 0>(x0, ASTEP ? a0 : a0 + so, bc);
  rd_set<MW, NW, BTR, 2048, BM, 0, BKS>(x1, ASTEP ? a1 : a1 + so, bc);
  prev();
#pragma unroll
  for (int m = 0; m < MW; ++m)
#pragma unroll
    for (int n = 0; n < NW; ++n) acc[m][n] = f32x4{0.f, 0.f, 0.f, 0.f};
  auto stage = [&](Frags<MW, NW>& c0, Frags<MW, NW>& c1, Frags<MW, NW>& n0, Frags<MW, NW>& n1) {
    so = wrapn(so + SLOT, 5 * SLOT);
    a0 += ASTEP; a1 += ASTEP;
#pragma unroll
    for (int n = 0; n < NW; ++n) bc[n] = brd[n] + so;
    BARRIER();
    kstep<MW, NW, BTR, SWAP, false, 2048, BM, 0, 0, true>(acc, c0, n0, ASTEP ? a0 : a0 + so, bc, NoExtra{});
    kstep<MW, NW, BTR, SWAP, false, 2048, BM, 0, BKS, true>(acc, c1, n1, ASTEP ? a1 : a1 + so, bc, NoExtra{});
  };
#pragma unroll 1
  for (int t = 0; t < 12; t += 2) {
    if (t == FT) flush(ws, pend, lane);
    stage(x0, x1, y0, y1);
    stage(y0, y1, x0, x1);
  }
  land<MW, NW, BTR>(x0);
  land<MW, NW, BTR>(x1);
}
__device__ __forceinline__ int t96_src(int pc, int lane, int pitch, int odd) {
  const int ks = pc / 6, r = pc % 6, g1 = (lane >> 4) & 1, r16 = 8 * g1 + 4 * (lane >> 5) + ((lane >> 2) & 3);
  return r < 4 ? (ks * 32 + r * 8) * pitch + odd * 64 + timg_src(lane, r & 1, pitch)
               : (ks * 32 + (r - 4) * 16 + r16) * pitch + (odd ? 0 : 128) + (((lane & 3) ^ (2 * g1)) * 16);
}
__device__ __forceinline__ int t96_rd(int lane, int nt, int odd) {
  const int g = lane >> 4, q = (lane & 15) >> 2, p = lane & 3;
  const bool narrow = odd ? nt < 2 : nt >= 4;
  return narrow ? 4096 + (g >> 1) * 1024 + (g & 1) * 256 + q * 64 + (((2 * (nt & 1) + (p >> 1)) ^ (2 * (g & 1))) * 16) + (p & 1) * 8
                : timg_rd<1>(lane, odd ? nt - 2 : nt);
}

constexpr int K0_T1 = 72 * KSPL, K0_T2 = 2 * RT, K0_Z = 8, K0_PF = 108;
__global__ __launch_bounds__(256) void k0_prep(const u16* __restrict__ x_img, const u16* __restrict__ x_txt,
                                               const u16* __restrict__ cvec, const u16* __restrict__ wmod_img,
                                               const u16* __restrict__ wmod_txt, const u16* __restrict__ wqkv_img,
                                               const u16* __restrict__ wqkv_txt, const u16* __restrict__ wo_img,
                                               const u16* __restrict__ wo_txt, char* __restrict__ ws) {
  __shared__ __attribute__((aligned(1024))) char smem_[24576];
  LDS_AS char* smem = (LDS_AS char*)smem_;
  const int bid = blockIdx.x, tid = threadIdx.x, lane = tid & 63;
  const int wave = __builtin_amdgcn_readfirstlane(tid >> 6);
  if (bid >= K0_T1 + K0_T2 + K0_Z) {
    const int i = bid - (K0_T1 + K0_T2 + K0_Z);
    const char* p = i < 18 ? (const char*)wo_img + i * 65536 : (i < 36 ? (const char*)wo_txt + (i - 18) * 65536
                  : (const char*)wqkv_txt + (i - 54) * 65536);
    u32x4 v[16];
#pragma unroll
    for (int k = 0; k < 16; ++k) {
      const int c = ((i - 36) * 16 + k) * 256 + tid;
      v[k] = (i >= 36 && i < 54) ? *(const u32x4*)((const char*)wqkv_img + (c / 96) * (3 * D * 2) + 2 * D * 2 + (c % 96) * 16)
                                 : *(const u32x4*)(p + k * 4096 + tid * 16);
    }
#pragma unroll
    for (int k = 0; k < 16; ++k) asm volatile("" ::"v"(v[k]));
    return;
  } else if (bid >= K0_T1 + K0_T2) {
    for (unsigned o = ((bid - K0_T1 - K0_T2) * 256 + tid) * 16; o < SZ_CNT; o += K0_Z * 4096) *(u32x4*)(ws + OFF_CNT + o) = u32x4{0u, 0u, 0u, 0u};
    unsigned long long pc;
    int delta, len;
    asm volatile("s_getpc_b64 %0\n.Lmmdit_k0_pc:\n\ts_mov_b32 %1, .Lmmdit_main_begin-.Lmmdit_k0_pc\n\ts_mov_b32 %2, .Lmmdit_main_end-.Lmmdit_main_begin"
                 : "=s"(pc), "=s"(delta), "=s"(len));
    pc += delta;
    unsigned w[3];
#pragma unroll
    for (int k = 0; k < 3; ++k) w[k] = *(const unsigned*)((const char*)pc + min((tid + k * 256) * 128, len - 4));
    asm volatile("" ::"v"(w[0]), "v"(w[1]), "v"(w[2]));
    return;
  } else if (bid < K0_T1) {
    constexpr int ROWS = D / KSPL, PER = ROWS / 32;
    const int i = bid, s = i / (36 * KSPL), rem = i % (36 * KSPL), cb = rem % 36, kh = rem / 36;
    const u16* w = (s ? wmod_txt : wmod_img) + (size_t)(kh * ROWS + (tid >> 3)) * (6 * D) + cb * 64 + (tid & 7) * 8;
    u16 cv[3];
#pragma unroll
    for (int e = 0; e < 3; ++e) { const int z = tid + e * 256; cv[e] = cvec[(z / ROWS) * D + kh * ROWS + z % ROWS]; }
    u32x4 wv[PER];
#pragma unroll
    for (int r = 0; r < PER; ++r) wv[r] = *(const u32x4*)(w + (size_t)r * 32 * (6 * D));
    LDS_AS float* sc = (LDS_AS float*)smem;
    LDS_AS float* red = (LDS_AS float*)(smem + 8192);
#pragma unroll
    for (int e = 0; e < 3; ++e) {
      const int z = tid + e * 256;
      const float v = __uint_as_float((unsigned)cv[e] << 16);
      sc[(z % ROWS) * 2 + z / ROWS] = v / (1.0f + __expf(-v));
    }
    __syncthreads();
    float a0[8] = {}, a1[8] = {};
#pragma unroll
    for (int r = 0; r < PER; ++r) {
      f32x2 sv = *(LDS_AS f32x2*)(sc + ((tid >> 3) + r * 32) * 2);
#pragma unroll
      for (int j = 0; j < 4; ++j) {
        float lo = bflo(wv[r][j]), hi = bfhi(wv[r][j]);
        a0[2 * j] = fmaf(sv[0], lo, a0[2 * j]); a0[2 * j + 1] = fmaf(sv[0], hi, a0[2 * j + 1]);
        a1[2 * j] = fmaf(sv[1], lo, a1[2 * j]); a1[2 * j + 1] = fmaf(sv[1], hi, a1[2 * j + 1]);
      }
    }
    LDS_AS float* rp = red + (tid >> 3) * 128 + (tid & 7) * 8;
    *(LDS_AS f32x4*)(rp) = f32x4{a0[0], a0[1], a0[2], a0[3]};
    *(LDS_AS f32x4*)(rp + 4) = f32x4{a0[4], a0[5], a0[6], a0[7]};
    *(LDS_AS f32x4*)(rp + 64) = f32x4{a1[0], a1[1], a1[2], a1[3]};
    *(LDS_AS f32x4*)(rp + 68) = f32x4{a1[4], a1[5], a1[6], a1[7]};
    __syncthreads();
    {
      const int o = tid >> 1, hf = tid & 1;
      float t0 = 0.f, t1 = 0.f;
#pragma unroll
      for (int g = 0; g < 16; g += 2) { t0 += red[(hf * 16 + g) * 128 + o]; t1 += red[(hf * 16 + g + 1) * 128 + o]; }
      float t = t0 + t1;
      t += __shfl_xor(t, 1);
      float* mod = (float*)(ws + OFF_MOD);
      if (hf == 0) mod[((kh * 2 + s) * 2 + (o >> 6)) * 2304 + cb * 64 + (o & 63)] = t;
    }
  } else {
    const int i = bid - K0_T1, b = i / RT, rt = i % RT;
    const u16* src = rt < 64 ? x_img + (size_t)(b * NI + rt * 16) * D : x_txt + (size_t)(b * NTX + (rt - 64) * 16) * D;
    src += (lane & 15) * D + wave * 192 + (lane >> 4) * 8;
    u32x4 v[6];
#pragma unroll
    for (int k = 0; k < 6; ++k) v[k] = *(const u32x4*)(src + k * 32);
    LDS_AS float* red = (LDS_AS float*)smem;
    float s0 = 0.f, s1 = 0.f;
#pragma unroll
    for (int k = 0; k < 6; ++k)
#pragma unroll
      for (int j = 0; j < 4; ++j) { s0 += bflo(v[k][j]); s1 += bfhi(v[k][j]); }
    const float s = red4_sum(s0 + s1);
    if (lane < 16) red[wave * 16 + lane] = s;
    __syncthreads();
    const int r = lane & 15;
    const float mean = ((red[r] + red[16 + r]) + (red[32 + r] + red[48 + r])) * (1.0f / D);
    float q0 = 0.f, q1 = 0.f;
#pragma unroll
    for (int k = 0; k < 6; ++k)
#pragma unroll
      for (int j = 0; j < 4; ++j) {
        float lo = bflo(v[k][j]) - mean, hi = bfhi(v[k][j]) - mean;
        q0 = fmaf(lo, lo, q0); q1 = fmaf(hi, hi, q1);
      }
    const float q = red4_sum(q0 + q1);
    if (lane < 16) red[64 + wave * 16 + lane] = q;
    __syncthreads();
    const float var = ((red[64 + r] + red[80 + r]) + (red[96 + r] + red[112 + r])) * (1.0f / D);
    const float rstd = 1.0f / sqrtf(var + 1e-5f);
    char* dst = ws + OFF_XN + (size_t)(i * KD + wave * 6) * 1024 + lane * 16;
#pragma unroll
    for (int k = 0; k < 6; ++k) {
      u32x4 o;
#pragma unroll
      for (int j = 0; j < 4; ++j) {
        typedef __attribute__((ext_vector_type(2))) _Float16 f16x2;
        const f16x2 h = {(_Float16)((bflo(v[k][j]) - mean) * rstd), (_Float16)((bfhi(v[k][j]) - mean) * rstd)};
        o[j] = __builtin_bit_cast(unsigned, h);
      }
      *(u32x4*)(dst + k * 1024) = o;
    }
  }
}

constexpr int FTX = 2;
constexpr int SLOT = 30 * 1024;
constexpr int P1_SB = 98304, P1_SGS = P1_SB + 4 * 12288;
constexpr int RED = P1_SB + 5 * 12288, ARR = RED + 1024, MAIN_LDS = ARR + 32;
constexpr int WP = 3 * D * 2;

__device__ __forceinline__ void fmix_lo(float& d, unsigned v, float g, float s) {
  asm volatile("v_fma_mix_f32 %0, %1, %2, %3 op_sel_hi:[1,0,0]" : "=v"(d) : "v"(v), "v"(g), "v"(s));
}
__device__ __forceinline__ void fmix_hi(float& d, unsigned v, float g, float s) {
  asm volatile("v_fma_mix_f32 %0, %1, %2, %3 op_sel:[1,0,0] op_sel_hi:[1,0,0]" : "=v"(d) : "v"(v), "v"(g), "v"(s));
}
template <int OFF>
__device__ __forceinline__ void rd_f4(f32x4& r, unsigned addr) {
  asm volatile("ds_read_b128 %0, %1 offset:%c2" : "=v"(r) : "v"(addr), "i"(OFF));
}
__device__ __forceinline__ u32x4 modulate(u32x4 x, const f32x4 (&g)[4]) {
  float t[8];
#pragma unroll
  for (int d = 0; d < 4; ++d) {
    fmix_lo(t[2 * d], x[d], g[2 + (d >> 1)][(d & 1) * 2], g[d >> 1][(d & 1) * 2]);
    fmix_hi(t[2 * d + 1], x[d], g[2 + (d >> 1)][(d & 1) * 2 + 1], g[d >> 1][(d & 1) * 2 + 1]);
  }
  return u32x4{pack2(t[0], t[1]), pack2(t[2], t[3]), pack2(t[4], t[5]), pack2(t[6], t[7])};
}

struct P2Tile { int b, ti, tj; };
__device__ __forceinline__ P2Tile p2_tile(int bid) {
  const int x = bid & 7, idx = bid >> 3, qd = x >> 1;
  P2Tile t;
  t.b = x & 1;
  if (idx == 30) { t.ti = 5; t.tj = 5; }
  else if (qd == 0) { t.ti = idx / 6; t.tj = idx % 6; }
  else if (qd == 1) { t.ti = idx / 5; t.tj = 6 + idx % 5; }
  else if (qd == 2) { t.ti = 6 + idx / 6; t.tj = 5 + idx % 6; }
  else { t.ti = 5 + idx / 5; t.tj = idx % 5; }
  return t;
}
__device__ __forceinline__ int p2_counter(const P2Tile& t, int lane) {
  const int u = min(lane, 11), sec = u < 6 ? 1 : 0, rt = (u < 6 ? t.tj * 6 + u : t.ti * 6 + u - 6);
  return rt < 64 ? C_QK + (t.b * 2 + sec) * 8 + rt / 9 : C_T + sec;
}
__device__ __forceinline__ void p2_issue(LDS_AS char* smem, rsrc_t rs, const P2Tile& t, int l, int lane, int s, int slot) {
#pragma unroll
  for (int i = 0; i < 4; ++i) {
    const int u = l * 4 + i;
    const int so = (u < 6 ? (int)OFF_K + (t.b * RT + t.tj * 6 + u) * KD * 1024 : (int)OFF_Q + (t.b * RT + t.ti * 6 + u - 6) * KD * 1024) + s * 2048;
    dma_sc1<0>(rs, smem + slot * SLOT + u * 2048, lane * 16, so);
    dma_sc1<1024>(rs, smem + slot * SLOT + u * 2048, lane * 16, so);
  }
}
__device__ __forceinline__ void p2_mfma(LDS_AS char* smem, char* __restrict__ ws, const P2Tile& t, int so, int wave, int lane,
                                        LDS_AS unsigned* arr, const Pending& pend) {
  rsrc_t rs = make_rsrc(ws, WS_END);
  const int wk = wave >> 1, wq = wave & 1, b = t.b, ti = t.ti, tj = t.tj;
  const unsigned krd = lds_addr(smem) + wk * 6144 + lane * 16;
  const unsigned qrd[3] = {lds_addr(smem) + 12288 + wq * 6144 + lane * 16, 0, 0};
  const int g = lane >> 4;
  int pso[3];
#pragma unroll
  for (int j = 0; j < 3; ++j) pso[j] = (int)OFF_P + (b * RT + ti * 6 + wq * 3 + j) * KK * 1024 + (tj * 6 + wk * 3) * 512;
  int po16 = (int)tiled_lane16(lane) + (g & 1) * 512, po8 = (int)tiled_lane(lane) + 1024;
  int sso = (int)OFF_ST + ((b * RT + ti * 6 + wq * 3) * 11 + tj) * 256 + wk * 8, svo = g < 3 ? g * 2816 + (lane & 15) * 16 : OOB;
  unsigned rw = lds_addr(smem) + RED + (wk * 64 + lane) * 4 + wq * 512, rr = lds_addr(smem) + RED + ((wk ^ 1) * 64 + (lane & 15)) * 4 + wq * 512;
  asm volatile("" : "+s"(pso[0]), "+s"(pso[1]), "+s"(pso[2]), "+s"(sso), "+v"(po16), "+v"(po8), "+v"(svo), "+v"(rw), "+v"(rr));
  f32x4 acc[3][3] = {};
  gemm12<3, 3, false, false, 2048, 1024, SLOT, 0, FTX>(acc, krd, krd + 1024, qrd, so, ws, pend, lane);
  asm volatile("s_nop 7");
  SCHED_FENCE();
  constexpr float SC = 0.036084391824351615f * 1.4426950408889634f;
  float mx[3], ls[3];
#pragma unroll
  for (int j = 0; j < 3; ++j) {
    const float m0 = max3(acc[0][j][0], acc[0][j][1], acc[0][j][2]), m1 = max3(acc[0][j][3], acc[1][j][0], acc[1][j][1]);
    const float m2 = max3(acc[1][j][2], acc[1][j][3], acc[2][j][0]), m3 = max3(acc[2][j][1], acc[2][j][2], acc[2][j][3]);
    mx[j] = red4_max(max2(max3(m0, m1, m2), m3));
  }
  {
    const float mine = g == 0 ? mx[0] : (g == 1 ? mx[1] : mx[2]);
    asm volatile("ds_write_b32 %0, %1\n\ts_waitcnt lgkmcnt(0)" ::"v"(rw), "v"(mine) : "memory");
    BARRIER();
    float o0, o1, o2;
    asm volatile("ds_read_b32 %0, %3\n\tds_read_b32 %1, %3 offset:64\n\tds_read_b32 %2, %3 offset:128\n\ts_waitcnt lgkmcnt(0)"
                 : "=&v"(o0), "=&v"(o1), "=&v"(o2) : "v"(rr) : "memory");
    mx[0] = max2(mx[0], o0) * SC; mx[1] = max2(mx[1], o1) * SC; mx[2] = max2(mx[2], o2) * SC;
  }
#pragma unroll
  for (int j = 0; j < 3; ++j) {
#pragma unroll
    for (int i = 0; i < 3; ++i)
#pragma unroll
      for (int r = 0; r < 4; ++r) acc[i][j][r] = __builtin_amdgcn_exp2f(fmaf(acc[i][j][r], SC, -mx[j]));
    __builtin_amdgcn_raw_buffer_store_b128(pair16(acc[0][j], acc[1][j]), rs, po16, pso[j], 16);
    __builtin_amdgcn_raw_buffer_store_b64(pack4(acc[2][j]), rs, po8, pso[j], 16);
    const f32x4 t4 = (acc[0][j] + acc[1][j]) + acc[2][j];
    ls[j] = red4_sum((t4[0] + t4[1]) + (t4[2] + t4[3]));
  }
  {
    const u32x2 v = {__float_as_uint(g == 0 ? mx[0] : (g == 1 ? mx[1] : mx[2])), __float_as_uint(g == 0 ? ls[0] : (g == 1 ? ls[1] : ls[2]))};
    __builtin_amdgcn_raw_buffer_store_b64(v, rs, svo, sso, 16);
  }
  BARRIER();
  if (last_wave(arr, 4, lane)) signal(ws, C_P + (b * 11 + ti) * 3 + (tj < 5 ? 0 : (tj == 5 ? 1 : 2)));
}

template <int MW, class Prev>
__device__ __forceinline__ void big_p1(LDS_AS char* smem, char* __restrict__ ws, int gA, int g, int col, int m0, int wn, int lane,
                                       LDS_AS unsigned* arr, const Pending& pw, Pending& pv, Prev&& prev) {
  rsrc_t rs = make_rsrc(ws, WS_END);
  const unsigned l0 = lds_addr(smem), aa = l0 + m0 * 2048 + lane * 16;
  const int b = gA >= RT ? 1 : 0, sec = col >> 3, nt0 = (col & 7) * 6 + wn * 3;
  f32x4 aq[MW][3], av[MW][3];
  {
    unsigned brd[3];
#pragma unroll
    for (int n = 0; n < 3; ++n) brd[n] = l0 + 18432 + t96_rd(lane, wn * 3 + n, col & 1);
    gemm12<MW, 3, true, true, 0, 6144, SLOT, 0, FTX>(aq, aa, aa + 1024, brd, 2 * SLOT, ws, pw, lane, prev);
  }
  {
    const unsigned brd[3] = {l0 + 18432 + wn * 3072 + lane * 16, 0, 0};
    gemm12<MW, 3, false, false, 1024, 6144, SLOT, 0, FTX>(av, aa, aa + 1024, brd, 4 * SLOT, ws,
                                                           Pending{C_QK + (b * 2 + sec) * 8 + g, -1, arr + 2}, lane, [&]() {
      store_tiles<MW * 3, 16>(rs, (int)((sec ? OFF_K : OFF_Q) + tiled_blk(gA + m0, KD, nt0)), lane,
                              [&](int i) { return tiled_blk(i / 3, KD, i % 3); }, [&](int i) { return aq[i / 3][i % 3]; });
    });
  }
  store_tiles<MW * 3, 16>(rs, (int)((sec ? OFF_VT2 : OFF_VT1) + tiled_blk(b * 48 + nt0, KK, gA - b * RT + m0)), lane,
                          [&](int i) { return tiled_blk(i % 3, KK, i / 3); }, [&](int i) { return av[i / 3][i % 3]; });
  pv = Pending{C_V + (b * 2 + sec) * 8 + (col & 7), -1, arr + 3};
}

__device__ __forceinline__ void big_wg(LDS_AS char* smem, char* __restrict__ ws, const u16* __restrict__ wqkv_img,
                                       const u16* __restrict__ wo_img, const u16* __restrict__ wo_txt, int bid, int wave8, int lane) {
  const int x = bid & 7, idx = bid >> 3, b = x & 1, g = idx % 7, col = (x >> 1) * 4 + idx / 7;
  const int gA = b * RT + g * 9, grp = b * 7 + g, hb = (int)OFF_H + gA * KD * 1024;
  const int wi = idx < 24 ? idx : idx - 4, wmb = b * 6 + wi % 6, wcol = (x >> 1) * 4 + wi / 6;
  const P2Tile t2 = p2_tile(bid);
  const u32x4 srd_ws = make_srd(ws, WS_END);
  rsrc_t rs = make_rsrc(ws, WS_END);
  LDS_AS unsigned* arr = (LDS_AS unsigned*)(smem + ARR);
  if (wave8 == 7) {
    const int c2 = p2_counter(t2, lane);
    {
      rsrc_t rw = make_rsrc(wqkv_img, D * 3 * D * 2);
#pragma unroll
      for (int k = 0; k < 11; ++k) {
        const int c = (bid * 11 + k) * 64 + lane;
        dma<0>(rw, smem + RED, (c / 192) * WP + (c % 192) * 16, 0);
      }
    }
    barriers(7);
    unsigned f = peek(srd_ws, C_H + grp);
    barriers(2);
    check(f, srd_ws, C_H + grp, 12);
    barriers(10);
    f = peek(srd_ws, C_W + col);
    barriers(2);
    check(f, srd_ws, C_W + col, 12);
    barriers(10);
    f = peek(srd_ws, c2);
    barriers(2);
    check(f, srd_ws, c2, 8);
    barriers(18);
    return;
  }
  if (wave8 >= 4) {
    const int l = wave8 - 4;
    rsrc_t rw = make_rsrc(wqkv_img, D * 3 * D * 2);
    rsrc_t ro = make_rsrc(wcol < 8 ? wo_img : wo_txt, D * D * 2);
    int bo[4], bw[4], av[3];
#pragma unroll
    for (int i = 0; i < 4; ++i) {
      bo[i] = (wcol & 7) * 192 + t96_src(l * 4 + i, lane, D * 2, wcol & 1);
      bw[i] = col * 192 + t96_src(l * 4 + i, lane, WP, col & 1);
    }
#pragma unroll
    for (int i = 0; i < 3; ++i) {
      const int u = min(l * 3 + i, 7), r = u * 8 + (lane >> 3);
      av[i] = (wmb * 64 + r) * WP + 2 * D * 2 + (((lane & 7) ^ ((r >> 1) & 7)) * 16);
    }
    const int wz = idx < 24 ? 0 : OOB;
    auto issueW = [&](int s, int slot) {
#pragma unroll
      for (int i = 0; i < 3; ++i) dma<0>(rw, smem + slot * SLOT + min(l * 3 + i, 7) * 1024, av[i], wz + s * 128);
#pragma unroll
      for (int i = 0; i < 4; ++i) dma<0>(ro, smem + slot * SLOT + 18432 + (l * 4 + i) * 1024, bo[i], wz + s * 64 * (D * 2));
    };
    auto issueA = [&](int s, int slot) {
#pragma unroll
      for (int i = 0; i < 3; ++i) {
        LDS_AS char* sa = smem + slot * SLOT + (l * 3 + i) * 2048;
        dma_sc1<0>(rs, sa, lane * 16, hb + ((l * 3 + i) * KD + 2 * s) * 1024);
        dma_sc1<1024>(rs, sa, lane * 16, hb + ((l * 3 + i) * KD + 2 * s) * 1024);
      }
    };
    issueW(0, 0);
    __builtin_amdgcn_s_sleep(4);
#pragma unroll
    for (int s = 1; s < 4; ++s) issueW(s, s);
    WAIT_VM(21); BARRIER();
    int slot = 4;
#pragma unroll 1
    for (int s = 4; s < 12; ++s) {
      WAIT_VM(14); BARRIER(); issueW(s, slot); slot = wrapn(slot + 1, 5);
    }
    stream12<7, 10>(slot, [&](int s, int sl) {
#pragma unroll
      for (int i = 0; i < 4; ++i) dma<0>(rw, smem + sl * SLOT + 18432 + (l * 4 + i) * 1024, bw[i], s * 64 * WP);
      issueA(s, sl);
    });
#pragma unroll 1
    for (int k = 0; k < 12; ++k) {
      if (k == 0 || k >= 7) WAIT_VM(20);
      else if (k == 1 || k == 6) WAIT_VM(14);
      else WAIT_VM(8);
      BARRIER();
      const int s = k < 5 ? k + 7 : k - 5;
#pragma unroll
      for (int i = 0; i < 4; ++i) {
        const int pc = l * 4 + i;
        dma_sc1(rs, smem + slot * SLOT + 18432 + pc * 1024, lane * 16, (int)OFF_WVO + ((col * 6 + pc % 6) * KD + 2 * s + pc / 6) * 1024);
      }
      if (k >= 5) issueA(s, slot);
      slot = wrapn(slot + 1, 5);
    }
    stream12<10, 8>(slot, [&](int s, int sl) { p2_issue(smem, rs, t2, l, lane, s, sl); });
    WAIT_VM(16); BARRIER();
    WAIT_VM(8); BARRIER();
    WAIT_VM(0); BARRIER();
    barriers(3);
    return;
  }
  const int c = wave8, wm = c >> 1, wn = c & 1;
  Pending ph{-1, -1, arr};
  if (col < 12) {
    const int W = col * 4 + c, ks = W >> 1, t0 = (W & 1) * 5;
    u32x4 xr[5], mp[4][KSPL];
#pragma unroll
    for (int i = 0; i < 4; ++i)
#pragma unroll
      for (int k = 0; k < KSPL; ++k)
        mp[i][k] = aload(srd_ws, (lane >> 4) * 32, (int)OFF_MOD + ((k * 4 + b) * 2304 + (i >> 1) * 768 + ks * 32 + (i & 1) * 4) * 4);
#pragma unroll
    for (int i = 0; i < 5; ++i) xr[i] = aload(srd_ws, lane * 16, (int)OFF_XN + ((gA + min(t0 + i, 8)) * KD + ks) * 1024);
    WAIT_VM(0);
    f32x4 gs[4];
#pragma unroll
    for (int i = 0; i < 4; ++i) {
      TIE(mp[i][0]); TIE(mp[i][1]);
      gs[i] = __builtin_bit_cast(f32x4, mp[i][0]) + __builtin_bit_cast(f32x4, mp[i][1]);
      if (i >= 2) gs[i] += 1.0f;
    }
#pragma unroll
    for (int i = 0; i < 5; ++i) {
      TIE(xr[i]);
      __builtin_amdgcn_raw_buffer_store_b128(modulate(xr[i], gs), rs, lane * 16, hb + (min(t0 + i, 8) * KD + ks) * 1024, 16);
    }
    ph.i0 = C_H + grp;
  }
  const int sw = (lane & 15) >> 1, gq = lane >> 4;
  const unsigned ab = lds_addr(smem) + wm * 4096 + (lane & 15) * 128;
  unsigned brd[3];
#pragma unroll
  for (int n = 0; n < 3; ++n) brd[n] = lds_addr(smem) + 18432 + t96_rd(lane, wn * 3 + n, wcol & 1);
  f32x4 aw[2][3];
  BARRIER();
  gemm12<2, 3, true, false, 0, 6144, SLOT, 0, 2>(aw, ab + ((gq ^ sw) * 16), ab + (((4 + gq) ^ sw) * 16), brd, 0, ws, ph, lane);
  const Pending pw{idx < 24 ? C_W + wcol : -1, -1, arr + 1};
  auto store_w = [&]() {
    store_tiles<6, 16>(rs, idx < 24 ? (int)(OFF_WVO + tiled_blk(wcol * 6 + wn * 3, KD, wmb * 4 + wm * 2)) : OOB, lane,
                       [&](int i) { return tiled_blk(i % 3, KD, i / 3); }, [&](int i) { return aw[i / 3][i % 3]; });
  };
  Pending pv;
  if (wm == 0) big_p1<5>(smem, ws, gA, g, col, 0, wn, lane, arr, pw, pv, store_w);
  else big_p1<4>(smem, ws, gA, g, col, 5, wn, lane, arr, pw, pv, store_w);
  p2_mfma(smem, ws, t2, 1 * SLOT, c, lane, arr + 4, pv);
}

template <int MT, bool VT, bool FIRST>
__device__ __forceinline__ void small_p1(LDS_AS char* smem, char* __restrict__ ws, const u16* __restrict__ wsrc, int wcol, int obase,
                                         int gA, int col, int smod, int wave8, int lane, const Pending& pend) {
  constexpr int MH = MT / 2;
  const u32x4 srd_ws = make_srd(ws, WS_END);
  rsrc_t rs = make_rsrc(ws, WS_END);
  auto gtile = [&](int lt) { return lt < MH ? gA + lt : gA + RT + (lt - MH); };
  if (wave8 == 7) {
    if (FIRST) {
      u32x4 mp[12][KSPL];
#pragma unroll
      for (int h = 0; h < 12; ++h)
#pragma unroll
        for (int k = 0; k < KSPL; ++k) mp[h][k] = aload(srd_ws, lane * 16, (int)OFF_MOD + (((k * 2 + smod) * 2 + h / 6) * 2304 + (h % 6) * 256) * 4);
      WAIT_VM(0);
#pragma unroll
      for (int h = 0; h < 12; ++h) {
        TIE(mp[h][0]); TIE(mp[h][1]);
        f32x4 v = __builtin_bit_cast(f32x4, mp[h][0]) + __builtin_bit_cast(f32x4, mp[h][1]);
        if (h % 6 >= 3) v += 1.0f;
        *(LDS_AS f32x4*)(smem + P1_SGS + h * 1024 + lane * 16) = v;
      }
      WAIT_LGKM(0);
    }
    barriers(FIRST ? 15 : 14);
    return;
  }
  if (wave8 >= 4) {
    const int l = wave8 - 4;
    rsrc_t rb = VT ? rs : make_rsrc(wsrc, D * 3 * D * 2);
    int bv[4];
#pragma unroll
    for (int i = 0; i < 4; ++i) {
      const int pc = l * 4 + i;
      bv[i] = VT ? ((col * 6 + pc % 6) * KD + pc / 6) * 1024 + lane * 16 : wcol + t96_src(pc, lane, WP, col & 1);
    }
    if (VT) poll(srd_ws, C_W + col, 12);
    loader_loop<12, 5, 4, 0, FIRST>([&](int s, int slot) {
#pragma unroll
      for (int i = 0; i < 4; ++i) {
        if (VT) dma_sc1(rb, smem + P1_SB + slot * 12288 + (l * 4 + i) * 1024, bv[i], (int)OFF_WVO + s * 2048);
        else dma<0>(rb, smem + P1_SB + slot * 12288 + (l * 4 + i) * 1024, bv[i], s * 64 * WP);
      }
    });
    BARRIER();
    return;
  }
  const int c = wave8, wm = c >> 1, wn = c & 1;
  auto aoff = [&](int st) { return st * (MT * 2048); };
  if (FIRST) {
    u32x4 xr[6][MT];
#pragma unroll
    for (int k = 0; k < 6; ++k)
#pragma unroll
      for (int i = 0; i < MT; ++i) xr[k][i] = aload(srd_ws, lane * 16, (int)OFF_XN + (gtile(i) * KD + c * 6 + k) * 1024);
    BARRIER();
    WAIT_VM(0);
#pragma unroll
    for (int k = 0; k < 6; ++k) {
      f32x4 gs[2][4];
#pragma unroll
      for (int z = 0; z < 2; ++z) {
        const unsigned gp = lds_addr(smem) + P1_SGS + z * 6144 + (c * 6 + k) * 128 + (lane >> 4) * 32;
        rd_f4<0>(gs[z][0], gp); rd_f4<16>(gs[z][1], gp); rd_f4<3072>(gs[z][2], gp); rd_f4<3088>(gs[z][3], gp);
      }
      WAIT_LGKM(0);
#pragma unroll
      for (int z = 0; z < 2; ++z)
#pragma unroll
        for (int i = 0; i < 4; ++i) TIE(gs[z][i]);
#pragma unroll
      for (int i = 0; i < MT; ++i) {
        TIE(xr[k][i]);
        *(LDS_AS u32x4*)(smem + aoff(c * 3 + (k >> 1)) + i * 2048 + (k & 1) * 1024 + lane * 16) = modulate(xr[k][i], gs[i < MH ? 0 : 1]);
      }
    }
    WAIT_LGKM(0);
  }
  const unsigned aa = lds_addr(smem) + wm * MH * 2048 + lane * 16;
  unsigned brd[3];
#pragma unroll
  for (int n = 0; n < 3; ++n) brd[n] = lds_addr(smem) + P1_SB + (VT ? wn * 3072 + lane * 16 : t96_rd(lane, wn * 3 + n, col & 1));
  f32x4 acc[MH][3] = {};
  BARRIER();
  gemm12<MH, 3, !VT, !VT, 1024, 6144, 12288, MT * 2048, 4>(acc, aa, aa + 1024, brd, 0, ws, pend, lane);
  BARRIER();
  const int nt0 = (col & 7) * 6 + wn * 3;
  if (VT) store_tiles<MH * 3, 16>(rs, obase, lane, [&](int i) { return tiled_blk(wm * 48 + nt0 + i % 3, KK, gA + i / 3); },
                                  [&](int i) { return acc[i / 3][i % 3]; });
  else store_tiles<MH * 3, 16>(rs, obase, lane, [&](int i) { return tiled_blk((FIRST ? gA + wm * RT : wm * MH) + i / 3, KD, nt0 + i % 3); },
                               [&](int i) { return acc[i / 3][i % 3]; });
}

__device__ __forceinline__ void small_wg(LDS_AS char* smem, char* __restrict__ ws, const u16* __restrict__ wqkv_img,
                                         const u16* __restrict__ wqkv_txt, const u16* __restrict__ wo_img,
                                         const u16* __restrict__ wo_txt, int bid, int wave8, int lane) {
  const int x = bid & 7, idx = bid >> 3;
  const u32x4 srd_ws = make_srd(ws, WS_END);
  rsrc_t rs = make_rsrc(ws, WS_END);
  LDS_AS unsigned* arr = (LDS_AS unsigned*)(smem + ARR);
  Pending pv{-1, -1, arr + 1};
  if (bid < 240) {
    const int col = (x >> 1) * 4 + (x & 1) * 2 + (idx - 28), sec = col >> 3;
    small_p1<2, false, true>(smem, ws, wqkv_img, col * 192, (int)(sec ? OFF_K : OFF_Q), 63, col, 0, wave8, lane, Pending{-1, -1, arr});
    small_p1<2, true, false>(smem, ws, wqkv_img, 0, (int)(sec ? OFF_VT2 : OFF_VT1), 63, col, 0, wave8, lane,
                             Pending{C_QK + sec * 8 + 7, C_QK + 16 + sec * 8 + 7, arr});
    pv.i0 = C_V + sec * 8 + (col & 7); pv.i1 = pv.i0 + 16;
  } else {
    const int col = bid - 240;
    small_p1<4, false, true>(smem, ws, wqkv_txt, col * 192, (int)(col >> 3 ? OFF_K : OFF_Q), 64, col, 1, wave8, lane, Pending{-1, -1, arr});
    const Pending pq{C_T + (col >> 3), -1, arr};
    if (col < 8) {
      small_p1<4, false, false>(smem, ws, wqkv_txt, 3072 + col * 192, (int)OFF_VX, 64, col, 1, wave8, lane, pq);
      pv.i0 = C_T + 2;
    } else if (wave8 < 4) {
      flush(ws, pq, lane);
    }
  }
  if (wave8 < 4) flush(ws, pv, lane);
  if (bid < 242) {
    const P2Tile t2 = p2_tile(bid);
    if (wave8 == 7) barriers(15);
    else if (wave8 >= 4) {
      poll(srd_ws, p2_counter(t2, lane), 8);
      loader_loop<12, 5, 8>([&](int s, int slot) { p2_issue(smem, rs, t2, wave8 - 4, lane, s, slot); });
      barriers(2);
    } else {
      BARRIER();
      p2_mfma(smem, ws, t2, 0, wave8, lane, arr + 4, Pending{-1, -1, arr});
    }
  } else if (bid < 254) {
    const int e = bid - 242, wave = wave8 & 3;
    if (wave8 >= 4) {
      rsrc_t rb = make_rsrc(e < 6 ? wo_img : wo_txt, D * D * 2);
      const int bv0 = (e % 6) * 256 + wave * 16 * (D * 2) + timg_src(lane, 0, D * 2);
      const int bv1 = (e % 6) * 256 + (wave * 16 + 8) * (D * 2) + timg_src(lane, 1, D * 2);
      poll(srd_ws, C_T + 2, 8);
      loader_loop<12, 5, 6>([&](int s, int slot) {
        LDS_AS char* sb = smem + slot * SLOT;
        dma_sc1<0>(rs, sb + wave * 2048, lane * 16, (int)OFF_VX + (wave * KD + 2 * s) * 1024);
        dma_sc1<1024>(rs, sb + wave * 2048, lane * 16, (int)OFF_VX + (wave * KD + 2 * s) * 1024);
#pragma unroll
        for (int i = 0; i < 4; ++i) dma<0>(rb, sb + 8192 + (wave * 4 + i) * 1024, ((i >> 1) ? bv1 : bv0) + (i & 1) * 128, s * 64 * (D * 2));
      });
    } else {
      const int wm = wave >> 1, wn = wave & 1;
      const unsigned aa = lds_addr(smem) + wm * 4096 + lane * 16;
      unsigned brd[4];
#pragma unroll
      for (int n = 0; n < 4; ++n) brd[n] = lds_addr(smem) + 8192 + timg_rd<2>(lane, wn * 4 + n);
      f32x4 acc[2][4] = {};
      BARRIER();
      gemm12<2, 4, true, false, 0, 8192, SLOT, 0, FTX>(acc, aa, aa + 1024, brd, 0, ws, Pending{-1, -1, arr}, lane);
      store_tiles<8, 16>(rs, (int)(e < 6 ? OFF_VT1 : OFF_VT2), lane,
                         [&](int i) { return tiled_blk(wm * 48 + (e % 6) * 8 + wn * 4 + (i & 3), KK, 64 + (i >> 2)); },
                         [&](int i) { return acc[i >> 2][i & 3]; });
      if (last_wave(arr + 4, 4, lane)) signal(ws, C_X + e);
    }
  }
}

constexpr int K3_NS = 4, K3_STAGE = 35 * 1024, K3_ST = 33 * 1024;
__device__ __forceinline__ void fmac(f32x4& a, const f32x4& x, float f, int e) {
  float a0 = a[e], a1 = a[e + 1];
  asm volatile("v_fmac_f32 %0, %2, %4\n\tv_fmac_f32 %1, %3, %4" : "+v"(a0), "+v"(a1) : "v"(x[e]), "v"(x[e + 1]), "v"(f));
  a[e] = a0; a[e + 1] = a1;
}
template <int QT>
__device__ __forceinline__ void k3_mfma(LDS_AS char* smem, int wn, int qoff, int nb, int lane, u16* __restrict__ outp) {
  const unsigned vrd = lds_addr(smem) + wn * 9216 + lane * 16, prd = lds_addr(smem) + 18432 + qoff * 3072 + lane * 16;
  unsigned srd[QT];
#pragma unroll
  for (int j = 0; j < QT; ++j) srd[j] = lds_addr(smem) + K3_ST + (qoff + j) * 256 + (qoff + j >= 3 ? 256 : 0) + (lane & 15) * 16;
  f32x4 acc[3][QT] = {}, t0[3][QT], t1[3][QT] = {};
  f32x4 st[QT];
  float R[QT], L[QT], w[QT];
#pragma unroll
  for (int j = 0; j < QT; ++j) { R[j] = -1e30f; L[j] = 0.f; st[j] = f32x4{-1e30f, 0.f, 0.f, 0.f}; }
  Frags<3, QT> f0, f1, f2;
  unsigned bc[QT];
  bc[0] = prd;
  BARRIER();
  rd_set<3, QT, false, 3072, 3072, 0, 0>(f0, vrd, bc);
  rd_set<3, QT, false, 3072, 3072, 1024, 1024>(f1, vrd, bc);
  int so = 0;
  auto weigh = [&](auto sc, const f32x4 (&sp)[QT]) {
    constexpr int S = decltype(sc)::value;
    if constexpr (S == 0) {
      bool big = false;
#pragma unroll
      for (int j = 0; j < QT; ++j) { w[j] = sp[j][0] - R[j]; big |= w[j] > 64.f; }
      if (__builtin_amdgcn_ballot_w64(big) != 0) {
#pragma unroll
        for (int j = 0; j < QT; ++j) {
          const bool up = w[j] > 64.f;
          const float sc2 = up ? __builtin_amdgcn_exp2f(-w[j]) : 1.0f;
          R[j] = up ? sp[j][0] : R[j];
          w[j] = up ? 0.f : w[j];
          L[j] *= sc2;
#pragma unroll
          for (int i = 0; i < 3; ++i) acc[i][j] *= sc2;
        }
      }
    } else if constexpr (S <= QT) {
      w[S - 1] = __builtin_amdgcn_exp2f(w[S - 1]);
      tie1(w[S - 1]);
    } else if constexpr (S <= 2 * QT) {
      L[S - QT - 1] = fmaf(w[S - QT - 1], sp[S - QT - 1][1] + sp[S - QT - 1][3], L[S - QT - 1]);
      tie1(L[S - QT - 1]);
    }
  };
  auto stage = [&](f32x4 (&cur)[3][QT], f32x4 (&prv)[3][QT]) {
    BARRIER();
    f32x4 sp[QT];
#pragma unroll
    for (int j = 0; j < QT; ++j) { sp[j] = st[j]; rd_f4<0>(st[j], srd[j] + so); }
    kstep<3, QT, false, false, true, 3072, 3072, 2048, 2048, true>(cur, f0, f2, vrd + so, bc, [&](auto ic) { weigh(ic, sp); });
    so = wrapn(so + K3_STAGE, K3_NS * K3_STAGE);
    bc[0] = prd + so;
    auto fold = [&](auto sc) {
      constexpr int S = decltype(sc)::value, T = S / 2, E = (S % 2) * 2;
      fmac(acc[T / QT][T % QT], prv[T / QT][T % QT], w[T % QT], E);
    };
    kstep<3, QT, false, false, false, 3072, 3072, 0, 0, true>(cur, f1, f0, vrd + so, bc, [&](auto ic) {
      if constexpr (decltype(ic)::value == 0) {
#pragma unroll
        for (int j = 0; j < QT; ++j) tie4(st[j]);
      }
      fold(ic);
    });
    kstep<3, QT, false, false, false, 3072, 3072, 1024, 1024, true>(cur, f2, f1, vrd + so, bc, [&](auto ic) {
      fold(std::integral_constant<int, 3 * QT + decltype(ic)::value>{});
    });
  };
#pragma unroll 1
  for (int t = 0; t < 10; t += 2) {
    stage(t0, t1);
    stage(t1, t0);
  }
  stage(t0, t1);
  land<3, QT, false>(f0);
  land<3, QT, false>(f1);
  static_for<0, 2 * QT + 1>([&](auto sc) { weigh(sc, st); });
  LDS_AS char* xs = smem + 3 * K3_STAGE;
  const int cw = wn * 48 + (lane >> 4) * 4;
  f32x4 o[QT * 3];
#pragma unroll
  for (int j = 0; j < QT; ++j) L[j] = 1.0f / L[j];
#pragma unroll
  for (int i = 0; i < 3; ++i) {
    const f32x4 gate = *(LDS_AS f32x4*)(xs + 15360 + (cw + i * 16) * 4) + *(LDS_AS f32x4*)(xs + 15360 + 384 + (cw + i * 16) * 4);
#pragma unroll
    for (int j = 0; j < QT; ++j) {
      const u32x2 xv = *(LDS_AS u32x2*)(xs + ((qoff + j) * 16 + (lane & 15)) * 192 + (cw + i * 16) * 2);
      const f32x4 a = (acc[i][j] + t0[i][j] * w[j]) * L[j];
      o[j * 3 + i] = f32x4{fmaf(gate[0], a[0], bflo(xv[0])), fmaf(gate[1], a[1], bfhi(xv[0])),
                           fmaf(gate[2], a[2], bflo(xv[1])), fmaf(gate[3], a[3], bfhi(xv[1]))};
    }
  }
  const bool odd = (lane >> 4) & 1;
  u16* ob = outp + (size_t)(qoff * 16 + (lane & 15)) * D + nb * 96 + wn * 48;
  auto toff = [&](int i) { return (i / 3) * 16 * D + (i % 3) * 16; };
#pragma unroll
  for (int i = 0; i + 1 < QT * 3; i += 2) *(u32x4*)(ob + (odd ? toff(i + 1) : toff(i)) + (lane >> 5) * 8) = pair16(o[i], o[i + 1]);
  if (QT & 1) *(u32x2*)(ob + toff(QT * 3 - 1) + (lane >> 4) * 4) = pack4(o[QT * 3 - 1]);
}

__device__ __forceinline__ void phase3(LDS_AS char* smem, char* __restrict__ ws, const u16* __restrict__ x_img,
                                       const u16* __restrict__ x_txt, u16* __restrict__ out, int bid, int wave8, int lane) {
  const int wave = wave8 & 3, x = bid & 7, idx = bid >> 3, b = x & 1, qd = x >> 1;
  int rt0, nrt, nb, txt;
  if (idx < 26) {
    const int u = (qd >> 1) * 26 + idx, rb = u >> 2;
    nb = (qd & 1) * 4 + (u & 3); rt0 = rb * 5; nrt = rb == 12 ? 4 : 5; txt = 0;
  } else {
    nb = qd * 2 + idx - 26; rt0 = 64; nrt = 2; txt = 1;
  }
  if (wave8 >= 4) {
    rsrc_t rs = make_rsrc(ws, WS_END);
    const u32x4 srd_ws = make_srd(ws, WS_END);
    const bool rev = qd == 1 || qd == 2;
    auto ktile = [&](int t) { return rev ? (t < 5 ? 6 + t : (t == 5 ? 5 : t - 6)) : t; };
    auto tail = [&](int slot) {
      const u16* xres = txt ? x_txt + (size_t)b * NTX * D : x_img + (size_t)(b * NI + rt0 * 16) * D;
      rsrc_t rx = make_rsrc(xres, nrt * 16 * D * 2);
      LDS_AS char* sb = smem + slot * K3_STAGE + wave * 4096;
#pragma unroll
      for (int i = 0; i < 4; ++i) {
        const int c = (wave * 4 + i) * 64 + lane;
        if (wave * 4 + i < 15) dma<0>(rx, sb + i * 1024, (c / 12) * (D * 2) + nb * 192 + (c % 12) * 16, 0);
        else dma<0>(rs, sb + i * 1024, lane < 24 * KSPL ? (int)OFF_MOD + ((((lane / 24) * 2 + txt) * 2 + b) * 2304 + 1536 + nb * 96 + (lane % 24) * 4) * 4 : OOB, 0);
      }
    };
    if (wave < 2) {
      poll(srd_ws, C_V + (b * 2 + txt) * 8 + nb, 8);
      const int vb = (int)(txt ? OFF_VT2 : OFF_VT1) + (b * 48 + nb * 6 + wave * 3) * KK * 1024;
      loader_loop<11, K3_NS, 9, 4>([&](int t, int slot) {
        const int kt = ktile(t);
        if (kt == 10) poll(srd_ws, C_X + txt * 6 + (nb * 96 + (lane & 1) * 95) / 128, 1);
        LDS_AS char* sb = smem + slot * K3_STAGE + wave * 9216;
#pragma unroll
        for (int i = 0; i < 3; ++i) {
          const int so = vb + i * KK * 1024 + kt * 3072;
          dma_sc1<0>(rs, sb + i * 3072, lane * 16, so);
          dma_sc1<1024>(rs, sb + i * 3072, lane * 16, so);
          dma_sc1<2048>(rs, sb + i * 3072, lane * 16, so);
        }
      }, NoHook{}, tail);
    } else {
      const int cp = C_P + (b * 11 + (rt0 * 16 + (lane & 1) * (nrt * 16 - 1)) / 96) * 3;
      poll(srd_ws, cp + (rev ? 2 : 0), 5);
      const int pb = (int)OFF_P + (b * RT + rt0) * KK * 1024;
      const int su = (wave - 2) * 3 + (lane >> 4);
      const int sv = (su < nrt && (lane >> 4) < 3) ? (int)OFF_ST + (b * RT + rt0 + su) * 2816 + (lane & 15) * 16 : OOB;
      loader_loop<11, K3_NS, 9, 4>([&](int t, int slot) {
        if (t == 5) poll(srd_ws, cp + ((lane & 2) ? 1 : (rev ? 0 : 2)), (lane & 2) ? 1 : 5);
        const int kt = ktile(t);
#pragma unroll
        for (int i = 0; i < 8; ++i) {
          const int pc = (wave - 2) * 8 + i, u = pc / 3, ks = pc % 3;
          if (i < 7 || wave == 2)
            dma_sc1(rs, smem + slot * K3_STAGE + 18432 + pc * 1024, lane * 16, u < nrt ? pb + (u * KK + kt * 3 + ks) * 1024 : OOB);
          else
            dma_sc1(rs, smem + RED, OOB, 0);
        }
        dma_sc1(rs, smem + slot * K3_STAGE + K3_ST + (wave - 2) * 1024, sv, kt * 256);
      }, NoHook{}, tail);
    }
    return;
  }
  u16* outp = txt ? out + (size_t)2 * NI * D + (size_t)b * NTX * D : out + (size_t)(b * NI + rt0 * 16) * D;
  const int wq = wave >> 1, q0 = (nrt + 1) >> 1;
  const int qt = wq ? nrt - q0 : q0, qoff = wq ? q0 : 0;
  if (qt == 3) k3_mfma<3>(smem, wave & 1, qoff, nb, lane, outp);
  else if (qt == 2) k3_mfma<2>(smem, wave & 1, qoff, nb, lane, outp);
  else k3_mfma<1>(smem, wave & 1, qoff, nb, lane, outp);
}

__global__ __launch_bounds__(512, 2) void kmain(const u16* __restrict__ x_img, const u16* __restrict__ x_txt,
                                                const u16* __restrict__ wqkv_img, const u16* __restrict__ wqkv_txt,
                                                const u16* __restrict__ wo_img, const u16* __restrict__ wo_txt,
                                                char* __restrict__ ws, u16* __restrict__ out) {
  __shared__ __attribute__((aligned(1024))) char smem_[MAIN_LDS];
  LDS_AS char* smem = (LDS_AS char*)smem_;
  const int bid = blockIdx.x, tid = threadIdx.x, lane = tid & 63;
  const int wave = __builtin_amdgcn_readfirstlane(tid >> 6);
  asm volatile(".Lmmdit_main_begin:");
  if (tid < 8) ((LDS_AS unsigned*)(smem + ARR))[tid] = 0u;
  WAIT_LGKM(0);
  if (bid < 224) {
    big_wg(smem, ws, wqkv_img, wo_img, wo_txt, bid, wave, lane);
    phase3(smem, ws, x_img, x_txt, out, bid, wave, lane);
  } else {
    small_wg(smem, ws, wqkv_img, wqkv_txt, wo_img, wo_txt, bid, wave, lane);
  }
  asm volatile(".Lmmdit_main_end:");
}

extern "C" void kernel_launch(void* const* d_in, const int* in_sizes, int n_in, void* d_out, int out_size, void* d_ws,
                              size_t ws_size, hipStream_t stream) {
  const u16* x_img = (const u16*)d_in[0];
  const u16* x_txt = (const u16*)d_in[1];
  const u16* cvec = (const u16*)d_in[2];
  const u16* wqkv_img = (const u16*)d_in[3];
  const u16* wqkv_txt = (const u16*)d_in[4];
  const u16* wo_img = (const u16*)d_in[5];
  const u16* wo_txt = (const u16*)d_in[6];
  const u16* wmod_img = (const u16*)d_in[7];
  const u16* wmod_txt = (const u16*)d_in[8];
  char* ws = (char*)d_ws;
  k0_prep<<<K0_T1 + K0_T2 + K0_Z + K0_PF, 256, 0, stream>>>(x_img, x_txt, cvec, wmod_img, wmod_txt, wqkv_img, wqkv_txt, wo_img, wo_txt, ws);
  kmain<<<256, 512, 0, stream>>>(x_img, x_txt, wqkv_img, wqkv_txt, wo_img, wo_txt, ws, (u16*)d_out);
}
```

```cpp
#include <hip/hip_runtime.h>
#include <hip/hip_bf16.h>
#include <stdint.h>
#include <type_traits>

typedef __attribute__((ext_vector_type(8))) __bf16 bf16x8;
typedef __attribute__((ext_vector_type(4))) __bf16 bf16x4;
typedef __attribute__((ext_vector_type(2))) __bf16 bf16x2;
typedef __attribute__((ext_vector_type(4))) float f32x4;
typedef __attribute__((ext_vector_type(2))) float f32x2;
typedef __attribute__((ext_vector_type(4))) unsigned u32x4;
typedef __attribute__((ext_vector_type(2))) unsigned u32x2;
#define LDS_AS __attribute__((address_space(3)))
typedef __amdgpu_buffer_rsrc_t rsrc_t;
typedef unsigned short u16;

constexpr int D = 768, NI = 1024, NTX = 32;
constexpr int RT = 66;
constexpr int KD = 24;
constexpr int KK = 33;
constexpr int KSPL = 2;
constexpr int OOB = 0x7f000000;

constexpr unsigned SZ_TOK = 2u * RT * KD * 1024u;
constexpr unsigned SZ_VT = 2u * 48u * KK * 1024u;
constexpr unsigned OFF_XN = 0;
constexpr unsigned OFF_Q = OFF_XN + SZ_TOK;
constexpr unsigned OFF_K = OFF_Q + SZ_TOK;
constexpr unsigned OFF_VX = OFF_K + SZ_TOK;
constexpr unsigned OFF_VT1 = OFF_VX + 4u * KD * 1024u;
constexpr unsigned OFF_VT2 = OFF_VT1 + SZ_VT;
constexpr unsigned OFF_P = OFF_VT2 + SZ_VT;
constexpr unsigned OFF_WVO = OFF_P + 2u * RT * KK * 1024u;
constexpr unsigned OFF_MOD = OFF_WVO + 96u * KD * 1024u;
constexpr unsigned OFF_ST = OFF_MOD + KSPL * 4u * 2304u * 4u;
constexpr unsigned OFF_CNT = OFF_ST + 2u * RT * 11u * 256u;
constexpr unsigned OFF_H = OFF_CNT + 24576u;
constexpr unsigned WS_END = OFF_H + SZ_TOK;
constexpr int C_H = 0;
constexpr int C_QK = 14;
constexpr int C_T = 46;
constexpr int C_V = 49;
constexpr int C_X = 81;
constexpr int C_P = 93;
constexpr int C_W = 159;
constexpr unsigned SZ_CNT = 24576u;

#define WAIT_VM(n) asm volatile("s_waitcnt vmcnt(%0)" ::"n"(n) : "memory")
#define WAIT_LGKM(n) asm volatile("s_waitcnt lgkmcnt(%0)" ::"n"(n) : "memory")
#define BARRIER() __builtin_amdgcn_s_barrier()
#define TIE(x) asm volatile("" : "+v"(x))
#define SCHED_FENCE() __builtin_amdgcn_sched_barrier(0)

template <int I, int N, class F>
__device__ __forceinline__ void static_for(F&& f) {
  if constexpr (I < N) { f(std::integral_constant<int, I>{}); static_for<I + 1, N>(f); }
}
__device__ __forceinline__ rsrc_t make_rsrc(const void* p, unsigned bytes) {
  return __builtin_amdgcn_make_buffer_rsrc((void*)p, 0, (int)bytes, 0x00020000);
}
__device__ __forceinline__ u32x4 make_srd(const void* p, unsigned bytes) {
  unsigned long long a = (unsigned long long)p;
  u32x4 r;
  r[0] = (unsigned)a; r[1] = (unsigned)(a >> 32) & 0xffffu; r[2] = bytes; r[3] = 0x00020000u;
  return r;
}
template <int IMM>
__device__ __forceinline__ void dma(rsrc_t rs, LDS_AS char* lds, int voff, int soff) {
  __builtin_amdgcn_raw_ptr_buffer_load_lds(rs, (LDS_AS void*)lds, 16, voff, soff, IMM, 0);
}
template <int IMM = 0>
__device__ __forceinline__ void dma_sc1(rsrc_t rs, LDS_AS char* lds, int voff, int soff) {
  __builtin_amdgcn_raw_ptr_buffer_load_lds(rs, (LDS_AS void*)lds, 16, voff, soff, IMM, 16);
}
__device__ __forceinline__ u32x4 aload(const u32x4& srd, int voff, int soff) {
  u32x4 r;
  asm volatile("buffer_load_dwordx4 %0, %1, %2, %3 offen" : "=v"(r) : "v"(voff), "s"(srd), "s"(soff));
  return r;
}
__device__ __forceinline__ u32x4 aload_sc1(const u32x4& srd, int voff, int soff) {
  u32x4 r;
  asm volatile("buffer_load_dwordx4 %0, %1, %2, %3 offen sc1" : "=v"(r) : "v"(voff), "s"(srd), "s"(soff));
  return r;
}
__device__ __forceinline__ void signal(char* ws, int idx) {
  asm volatile("global_atomic_add %0, %1, %2" ::"v"((int)OFF_CNT + idx * 128), "v"(1u), "s"(ws) : "memory");
}
__device__ __forceinline__ bool last_wave(LDS_AS unsigned* word, unsigned n, int lane) {
  WAIT_VM(0);
  unsigned old = 0;
  if (lane == 0) asm volatile("ds_add_rtn_u32 %0, %1, %2\n\ts_waitcnt lgkmcnt(0)" : "=v"(old) : "v"((unsigned)(size_t)word), "v"(1u) : "memory");
  return lane == 0 && old == n - 1;
}
struct Pending { int i0, i1; LDS_AS unsigned* arr; };
__device__ __forceinline__ void flush(char* ws, const Pending& p, int lane) {
  if (p.i0 >= 0 && last_wave(p.arr, 4, lane)) { signal(ws, p.i0); if (p.i1 >= 0) signal(ws, p.i1); }
}
__device__ __forceinline__ void poll(const u32x4& srd, int idx, unsigned target) {
#pragma unroll 1
  for (int spin = 0; spin < (1 << 20); ++spin) {
    unsigned f;
    asm volatile("buffer_load_dword %0, %1, %2, 0 offen sc1\n\ts_waitcnt vmcnt(0)" : "=v"(f) : "v"((int)OFF_CNT + idx * 128), "s"(srd) : "memory");
    if (__builtin_amdgcn_ballot_w64(f < target) == 0) break;
    __builtin_amdgcn_s_sleep(1);
  }
}
__device__ __forceinline__ unsigned peek(const u32x4& srd, int idx) {
  unsigned f;
  asm volatile("buffer_load_dword %0, %1, %2, 0 offen sc1" : "=v"(f) : "v"((int)OFF_CNT + idx * 128), "s"(srd) : "memory");
  return f;
}
__device__ __forceinline__ void check(unsigned f, const u32x4& srd, int idx, unsigned target) {
  WAIT_VM(0);
  TIE(f);
  if (__builtin_amdgcn_ballot_w64(f < target) != 0) poll(srd, idx, target);
}
__device__ __forceinline__ void barriers(int n) {
#pragma unroll 1
  for (int t = 0; t < n; ++t) BARRIER();
}
template <int OFF>
__device__ __forceinline__ void rd_tr(bf16x4& lo, bf16x4& hi, unsigned addr) {
  asm volatile("ds_read_b64_tr_b16 %0, %2 offset:%c3\n\tds_read_b64_tr_b16 %1, %2 offset:%c4"
               : "=&v"(lo), "=&v"(hi) : "v"(addr), "i"(OFF), "i"(OFF + 512));
}
template <int OFF>
__device__ __forceinline__ void rd_128(bf16x8& r, unsigned addr) {
  asm volatile("ds_read_b128 %0, %1 offset:%c2" : "=v"(r) : "v"(addr), "i"(OFF));
}
template <int OFF>
__device__ __forceinline__ void rd_32(float& r, unsigned addr) {
  asm volatile("ds_read_b32 %0, %1 offset:%c2" : "=v"(r) : "v"(addr), "i"(OFF));
}
__device__ __forceinline__ void tie1(float& x) { TIE(x); }
__device__ __forceinline__ void tie4(f32x4& x) { TIE(x); }
__device__ __forceinline__ void tie8(bf16x8& x) { TIE(x); }
__device__ __forceinline__ void tie4h(bf16x4& x) { TIE(x); }
__device__ __forceinline__ bf16x8 cat(bf16x4 lo, bf16x4 hi) { return __builtin_shufflevector(lo, hi, 0, 1, 2, 3, 4, 5, 6, 7); }
__device__ __forceinline__ unsigned lds_addr(LDS_AS char* p) { return (unsigned)(size_t)p; }
__device__ __forceinline__ int wrapn(int v, int n) { return v >= n ? v - n : v; }
__device__ __forceinline__ f32x4 mfma(bf16x8 a, bf16x8 b, f32x4 c) {
  return __builtin_amdgcn_mfma_f32_16x16x32_bf16(a, b, c, 0, 0, 0);
}
__device__ __forceinline__ unsigned pack2(float a, float b) {
  f32x2 f = {a, b};
  bf16x2 h = __builtin_convertvector(f, bf16x2);
  return __builtin_bit_cast(unsigned, h);
}
__device__ __forceinline__ u32x2 pack4(f32x4 v) { return u32x2{pack2(v[0], v[1]), pack2(v[2], v[3])}; }
__device__ __forceinline__ float bflo(unsigned v) { return __uint_as_float(v << 16); }
__device__ __forceinline__ float bfhi(unsigned v) { return __uint_as_float(v & 0xffff0000u); }
__device__ __forceinline__ float max2(float a, float b) { float r; asm("v_max_f32 %0, %1, %2" : "=v"(r) : "v"(a), "v"(b)); return r; }
__device__ __forceinline__ float max3(float a, float b, float c) { float r; asm("v_max3_f32 %0, %1, %2, %3" : "=v"(r) : "v"(a), "v"(b), "v"(c)); return r; }
__device__ __forceinline__ float red4_max(float x) {
  auto a = __builtin_amdgcn_permlane16_swap(__float_as_uint(x), __float_as_uint(x), false, false);
  x = max2(__uint_as_float(a[0]), __uint_as_float(a[1]));
  auto b = __builtin_amdgcn_permlane32_swap(__float_as_uint(x), __float_as_uint(x), false, false);
  return max2(__uint_as_float(b[0]), __uint_as_float(b[1]));
}
__device__ __forceinline__ float red4_sum(float x) {
  auto a = __builtin_amdgcn_permlane16_swap(__float_as_uint(x), __float_as_uint(x), false, false);
  x = __uint_as_float(a[0]) + __uint_as_float(a[1]);
  auto b = __builtin_amdgcn_permlane32_swap(__float_as_uint(x), __float_as_uint(x), false, false);
  return __uint_as_float(b[0]) + __uint_as_float(b[1]);
}
__device__ __forceinline__ unsigned tiled_lane(int lane) {
  int g = lane >> 4;
  return (unsigned)(((lane & 15) + 16 * (g >> 1)) * 16 + (g & 1) * 8);
}
__device__ __forceinline__ unsigned tiled_blk(int rowtile, int KS, int T16) {
  return (unsigned)(rowtile * KS * 1024 + T16 * 512);
}
__device__ __forceinline__ u32x4 pair16(f32x4 x, f32x4 y) {
  const u32x2 px = pack4(x), py = pack4(y);
  auto a = __builtin_amdgcn_permlane16_swap(px[0], py[0], false, false);
  auto b = __builtin_amdgcn_permlane16_swap(px[1], py[1], false, false);
  return u32x4{a[0], b[0], a[1], b[1]};
}
__device__ __forceinline__ unsigned tiled_lane16(int lane) { return (unsigned)(((lane & 15) + 16 * (lane >> 5)) * 16); }
template <int NTILE, int AUX, class Blk, class Val>
__device__ __forceinline__ void store_tiles(rsrc_t rs, int base, int lane, Blk&& blk, Val&& val) {
  const bool odd = (lane >> 4) & 1;
  const int l16 = (int)tiled_lane16(lane);
  const int sb = __builtin_amdgcn_readfirstlane(base);
#pragma unroll
  for (int i = 0; i + 1 < NTILE; i += 2) {
    const int so = __builtin_amdgcn_readfirstlane(sb - 0x100000 + (int)blk(i));
    const int d = __builtin_amdgcn_readfirstlane((int)(blk(i + 1) - blk(i)));
    __builtin_amdgcn_raw_buffer_store_b128(pair16(val(i), val(i + 1)), rs, l16 + 0x100000 + (odd ? d : 0), so, AUX);
  }
  if (NTILE & 1)
    __builtin_amdgcn_raw_buffer_store_b64(pack4(val(NTILE - 1)), rs, (int)tiled_lane(lane),
                                          __builtin_amdgcn_readfirstlane(sb + (int)blk(NTILE - 1)), AUX);
}
__device__ __forceinline__ int timg_src(int lane, int sigma, int pitch) {
  int r = lane >> 3, c = (lane & 7) ^ (2 * ((r >> 1) & 1) + 4 * sigma);
  return r * pitch + c * 16;
}
template <int NB>
__device__ __forceinline__ int timg_rd(int lane, int nt) {
  int g = lane >> 4, q = (lane & 15) >> 2, p = lane & 3;
  int f = 2 * ((q >> 1) & 1) + 4 * (g & 1);
  return (g * NB + (nt >> 2)) * 1024 + q * 128 + (((2 * (nt & 3)) ^ f) + (p >> 1)) * 16 + (p & 1) * 8;
}

template <int MT, int NT>
struct Frags { bf16x8 a[MT]; bf16x8 b[NT]; bf16x4 l[NT], h[NT]; };

template <int MT, int NT, bool BTR, int AM, int BM, int AO, int BO, int I>
__device__ __forceinline__ void rd_item(Frags<MT, NT>& n, unsigned ab, const unsigned (&bb)[NT]) {
  if constexpr (I < NT) {
    if constexpr (BTR) rd_tr<BO>(n.l[I], n.h[I], bb[I]);
    else rd_128<BO + I * BM>(n.b[I], bb[0]);
  } else if constexpr (I < NT + MT) {
    rd_128<AO + (I - NT) * AM>(n.a[I - NT], ab);
  }
}
template <int MT, int NT, bool BTR, int AM, int BM, int AO, int BO>
__device__ __forceinline__ void rd_set(Frags<MT, NT>& n, unsigned ab, const unsigned (&bb)[NT]) {
  static_for<0, MT + NT>([&](auto ic) { rd_item<MT, NT, BTR, AM, BM, AO, BO, decltype(ic)::value>(n, ab, bb); });
}
template <int MT, int NT, bool BTR, bool AH = false>
__device__ __forceinline__ void land(Frags<MT, NT>& c) {
  WAIT_LGKM(AH ? MT + (BTR ? 2 : 1) * NT : 0);
#pragma unroll
  for (int j = 0; j < NT; ++j) {
    if constexpr (BTR) { TIE(c.l[j]); TIE(c.h[j]); } else TIE(c.b[j]);
  }
#pragma unroll
  for (int m = 0; m < MT; ++m) TIE(c.a[m]);
  SCHED_FENCE();
}
template <int MT, int NT, bool BTR, bool SWAP, bool ZC, int AM, int BM, int AO, int BO, bool AH = false, class X>
__device__ __forceinline__ void kstep(f32x4 (&acc)[MT][NT], Frags<MT, NT>& c, Frags<MT, NT>& n, unsigned ab,
                                      const unsigned (&bb)[NT], X&& extra) {
  land<MT, NT, BTR, AH>(c);
  static_for<0, MT * NT>([&](auto ic) {
    constexpr int I = decltype(ic)::value, m = I / NT, j = I % NT;
    bf16x8 bf;
    if constexpr (BTR) bf = cat(c.l[j], c.h[j]); else bf = c.b[j];
    const f32x4 ci = ZC ? f32x4{0.f, 0.f, 0.f, 0.f} : acc[m][j];
    acc[m][j] = SWAP ? mfma(bf, c.a[m], ci) : mfma(c.a[m], bf, ci);
    tie4(acc[m][j]);
    rd_item<MT, NT, BTR, AM, BM, AO, BO, I>(n, ab, bb);
    extra(ic);
    SCHED_FENCE();
  });
  static_for<MT * NT, (MT + NT > MT * NT ? MT + NT : MT * NT)>(
      [&](auto ic) { rd_item<MT, NT, BTR, AM, BM, AO, BO, decltype(ic)::value>(n, ab, bb); });
}
struct NoExtra { template <class T> __device__ __forceinline__ void operator()(T) const {} };

struct NoHook { __device__ __forceinline__ void operator()() const {} };
struct NoTail { __device__ __forceinline__ void operator()(int) const {} };
template <int NST, int NS, int V, int TV = 0, bool XB = false, class Issue, class Hook = NoHook, class Tail = NoTail, class Pre = NoHook>
__device__ __forceinline__ void loader_loop(Issue&& issue, Hook&& hook = NoHook{}, Tail&& tail = NoTail{}, Pre&& pre = NoHook{}) {
#pragma unroll
  for (int s = 0; s < NS - 1; ++s) issue(s, s);
  if (XB) BARRIER();
  pre();
  WAIT_VM(V * (NS - 2));
  BARRIER();
  hook();
  int slot = NS - 1;
#pragma unroll 1
  for (int t = 0; t <= NST - NS; ++t) {
    WAIT_VM(V * (NS - 3));
    BARRIER();
    issue(t + NS - 1, slot);
    slot = wrapn(slot + 1, NS);
  }
  static_for<1, NS>([&](auto rc) {
    constexpr int R = decltype(rc)::value;
    WAIT_VM(R == NS - 1 ? 0 : V * (NS - 2 - R) + (R > 1 ? TV : 0));
    BARRIER();
    if constexpr (R == 1) tail(slot);
  });
}

template <int VP, int V, class Issue>
__device__ __forceinline__ void stream12(int& slot, Issue&& issue) {
  WAIT_VM(2 * VP); BARRIER(); issue(0, slot); slot = wrapn(slot + 1, 5);
  WAIT_VM(VP + V); BARRIER(); issue(1, slot); slot = wrapn(slot + 1, 5);
#pragma unroll 1
  for (int t = 2; t < 12; ++t) {
    WAIT_VM(2 * V); BARRIER(); issue(t, slot); slot = wrapn(slot + 1, 5);
  }
}

template <int MW, int NW, bool BTR, bool SWAP, int BM, int BKS, int SLOT, int ASTEP, int FT, class Prev = NoHook>
__device__ __forceinline__ void gemm12(f32x4 (&acc)[MW][NW], unsigned a0, unsigned a1, const unsigned (&brd)[NW], int so,
                                       char* __restrict__ ws, const Pending& pend, int lane, Prev&& prev = NoHook{}) {
  Frags<MW, NW> x0, x1, y0, y1;
  unsigned bc[NW];
#pragma unroll
  for (int n = 0; n < NW; ++n) bc[n] = brd[n] + so;
  rd_set<MW, NW, BTR, 2048, BM, 0, 0>(x0, ASTEP ? a0 : a0 + so, bc);
  rd_set<MW, NW, BTR, 2048, BM, 0, BKS>(x1, ASTEP ? a1 : a1 + so, bc);
  prev();
#pragma unroll
  for (int m = 0; m < MW; ++m)
#pragma unroll
    for (int n = 0; n < NW; ++n) acc[m][n] = f32x4{0.f, 0.f, 0.f, 0.f};
  auto stage = [&](Frags<MW, NW>& c0, Frags<MW, NW>& c1, Frags<MW, NW>& n0, Frags<MW, NW>& n1) {
    so = wrapn(so + SLOT, 5 * SLOT);
    a0 += ASTEP; a1 += ASTEP;
#pragma unroll
    for (int n = 0; n < NW; ++n) bc[n] = brd[n] + so;
    BARRIER();
    kstep<MW, NW, BTR, SWAP, false, 2048, BM, 0, 0, true>(acc, c0, n0, ASTEP ? a0 : a0 + so, bc, NoExtra{});
    kstep<MW, NW, BTR, SWAP, false, 2048, BM, 0, BKS, true>(acc, c1, n1, ASTEP ? a1 : a1 + so, bc, NoExtra{});
  };
#pragma unroll 1
  for (int t = 0; t < 12; t += 2) {
    if (t == FT) flush(ws, pend, lane);
    stage(x0, x1, y0, y1);
    stage(y0, y1, x0, x1);
  }
  land<MW, NW, BTR>(x0);
  land<MW, NW, BTR>(x1);
}
__device__ __forceinline__ int t96_src(int pc, int lane, int pitch, int odd) {
  const int ks = pc / 6, r = pc % 6, g1 = (lane >> 4) & 1, r16 = 8 * g1 + 4 * (lane >> 5) + ((lane >> 2) & 3);
  return r < 4 ? (ks * 32 + r * 8) * pitch + odd * 64 + timg_src(lane, r & 1, pitch)
               : (ks * 32 + (r - 4) * 16 + r16) * pitch + (odd ? 0 : 128) + (((lane & 3) ^ (2 * g1)) * 16);
}
__device__ __forceinline__ int t96_rd(int lane, int nt, int odd) {
  const int g = lane >> 4, q = (lane & 15) >> 2, p = lane & 3;
  const bool narrow = odd ? nt < 2 : nt >= 4;
  return narrow ? 4096 + (g >> 1) * 1024 + (g & 1) * 256 + q * 64 + (((2 * (nt & 1) + (p >> 1)) ^ (2 * (g & 1))) * 16) + (p & 1) * 8
                : timg_rd<1>(lane, odd ? nt - 2 : nt);
}

constexpr int K0_T1 = 72 * KSPL, K0_T2 = 2 * RT, K0_Z = 8, K0_PF = 144;
__global__ __launch_bounds__(256) void k0_prep(const u16* __restrict__ x_img, const u16* __restrict__ x_txt,
                                               const u16* __restrict__ cvec, const u16* __restrict__ wmod_img,
                                               const u16* __restrict__ wmod_txt, const u16* __restrict__ wqkv_img,
                                               const u16* __restrict__ wqkv_txt, const u16* __restrict__ wo_img,
                                               const u16* __restrict__ wo_txt, char* __restrict__ ws) {
  __shared__ __attribute__((aligned(1024))) char smem_[24576];
  LDS_AS char* smem = (LDS_AS char*)smem_;
  const int bid = blockIdx.x, tid = threadIdx.x, lane = tid & 63;
  const int wave = __builtin_amdgcn_readfirstlane(tid >> 6);
  if (bid >= K0_T1 + K0_T2 + K0_Z) {
    const int i = bid - (K0_T1 + K0_T2 + K0_Z);
    const char* p = i < 18 ? (const char*)wo_img + i * 65536 : (i < 36 ? (const char*)wo_txt + (i - 18) * 65536
                  : (i < 90 ? (const char*)wqkv_img + (i - 36) * 65536 : (const char*)wqkv_txt + (i - 90) * 65536));
    u32x4 v[16];
#pragma unroll
    for (int k = 0; k < 16; ++k) v[k] = *(const u32x4*)(p + k * 4096 + tid * 16);
#pragma unroll
    for (int k = 0; k < 16; ++k) asm volatile("" ::"v"(v[k]));
    return;
  } else if (bid >= K0_T1 + K0_T2) {
    for (unsigned o = ((bid - K0_T1 - K0_T2) * 256 + tid) * 16; o < SZ_CNT; o += K0_Z * 4096) *(u32x4*)(ws + OFF_CNT + o) = u32x4{0u, 0u, 0u, 0u};
    unsigned long long pc;
    int delta, len;
    asm volatile("s_getpc_b64 %0\n.Lmmdit_k0_pc:\n\ts_mov_b32 %1, .Lmmdit_main_begin-.Lmmdit_k0_pc\n\ts_mov_b32 %2, .Lmmdit_main_end-.Lmmdit_main_begin"
                 : "=s"(pc), "=s"(delta), "=s"(len));
    pc += delta;
    unsigned w[3];
#pragma unroll
    for (int k = 0; k < 3; ++k) w[k] = *(const unsigned*)((const char*)pc + min((tid + k * 256) * 128, len - 4));
    asm volatile("" ::"v"(w[0]), "v"(w[1]), "v"(w[2]));
    return;
  } else if (bid < K0_T1) {
    constexpr int ROWS = D / KSPL, PER = ROWS / 32;
    const int i = bid, s = i / (36 * KSPL), rem = i % (36 * KSPL), cb = rem % 36, kh = rem / 36;
    const u16* w = (s ? wmod_txt : wmod_img) + (size_t)(kh * ROWS + (tid >> 3)) * (6 * D) + cb * 64 + (tid & 7) * 8;
    u16 cv[3];
#pragma unroll
    for (int e = 0; e < 3; ++e) { const int z = tid + e * 256; cv[e] = cvec[(z / ROWS) * D + kh * ROWS + z % ROWS]; }
    u32x4 wv[PER];
#pragma unroll
    for (int r = 0; r < PER; ++r) wv[r] = *(const u32x4*)(w + (size_t)r * 32 * (6 * D));
    LDS_AS float* sc = (LDS_AS float*)smem;
    LDS_AS float* red = (LDS_AS float*)(smem + 8192);
#pragma unroll
    for (int e = 0; e < 3; ++e) {
      const int z = tid + e * 256;
      const float v = __uint_as_float((unsigned)cv[e] << 16);
      sc[(z % ROWS) * 2 + z / ROWS] = v / (1.0f + __expf(-v));
    }
    __syncthreads();
    float a0[8] = {}, a1[8] = {};
#pragma unroll
    for (int r = 0; r < PER; ++r) {
      f32x2 sv = *(LDS_AS f32x2*)(sc + ((tid >> 3) + r * 32) * 2);
#pragma unroll
      for (int j = 0; j < 4; ++j) {
        float lo = bflo(wv[r][j]), hi = bfhi(wv[r][j]);
        a0[2 * j] = fmaf(sv[0], lo, a0[2 * j]); a0[2 * j + 1] = fmaf(sv[0], hi, a0[2 * j + 1]);
        a1[2 * j] = fmaf(sv[1], lo, a1[2 * j]); a1[2 * j + 1] = fmaf(sv[1], hi, a1[2 * j + 1]);
      }
    }
    LDS_AS float* rp = red + (tid >> 3) * 128 + (tid & 7) * 8;
    *(LDS_AS f32x4*)(rp) = f32x4{a0[0], a0[1], a0[2], a0[3]};
    *(LDS_AS f32x4*)(rp + 4) = f32x4{a0[4], a0[5], a0[6], a0[7]};
    *(LDS_AS f32x4*)(rp + 64) = f32x4{a1[0], a1[1], a1[2], a1[3]};
    *(LDS_AS f32x4*)(rp + 68) = f32x4{a1[4], a1[5], a1[6], a1[7]};
    __syncthreads();
    {
      const int o = tid >> 1, hf = tid & 1;
      float t0 = 0.f, t1 = 0.f;
#pragma unroll
      for (int g = 0; g < 16; g += 2) { t0 += red[(hf * 16 + g) * 128 + o]; t1 += red[(hf * 16 + g + 1) * 128 + o]; }
      float t = t0 + t1;
      t += __shfl_xor(t, 1);
      float* mod = (float*)(ws + OFF_MOD);
      if (hf == 0) mod[((kh * 2 + s) * 2 + (o >> 6)) * 2304 + cb * 64 + (o & 63)] = t;
    }
  } else {
    const int i = bid - K0_T1, b = i / RT, rt = i % RT;
    const u16* src = rt < 64 ? x_img + (size_t)(b * NI + rt * 16) * D : x_txt + (size_t)(b * NTX + (rt - 64) * 16) * D;
    src += (lane & 15) * D + wave * 192 + (lane >> 4) * 8;
    u32x4 v[6];
#pragma unroll
    for (int k = 0; k < 6; ++k) v[k] = *(const u32x4*)(src + k * 32);
    LDS_AS float* red = (LDS_AS float*)smem;
    float s0 = 0.f, s1 = 0.f;
#pragma unroll
    for (int k = 0; k < 6; ++k)
#pragma unroll
      for (int j = 0; j < 4; ++j) { s0 += bflo(v[k][j]); s1 += bfhi(v[k][j]); }
    const float s = red4_sum(s0 + s1);
    if (lane < 16) red[wave * 16 + lane] = s;
    __syncthreads();
    const int r = lane & 15;
    const float mean = ((red[r] + red[16 + r]) + (red[32 + r] + red[48 + r])) * (1.0f / D);
    float q0 = 0.f, q1 = 0.f;
#pragma unroll
    for (int k = 0; k < 6; ++k)
#pragma unroll
      for (int j = 0; j < 4; ++j) {
        float lo = bflo(v[k][j]) - mean, hi = bfhi(v[k][j]) - mean;
        q0 = fmaf(lo, lo, q0); q1 = fmaf(hi, hi, q1);
      }
    const float q = red4_sum(q0 + q1);
    if (lane < 16) red[64 + wave * 16 + lane] = q;
    __syncthreads();
    const float var = ((red[64 + r] + red[80 + r]) + (red[96 + r] + red[112 + r])) * (1.0f / D);
    const float rstd = 1.0f / sqrtf(var + 1e-5f);
    char* dst = ws + OFF_XN + (size_t)(i * KD + wave * 6) * 1024 + lane * 16;
#pragma unroll
    for (int k = 0; k < 6; ++k) {
      u32x4 o;
#pragma unroll
      for (int j = 0; j < 4; ++j) {
        typedef __attribute__((ext_vector_type(2))) _Float16 f16x2;
        const f16x2 h = {(_Float16)((bflo(v[k][j]) - mean) * rstd), (_Float16)((bfhi(v[k][j]) - mean) * rstd)};
        o[j] = __builtin_bit_cast(unsigned, h);
      }
      *(u32x4*)(dst + k * 1024) = o;
    }
  }
}

constexpr int FTX = 2;
constexpr int SLOT = 30 * 1024;
constexpr int P1_SB = 98304, P1_SGS = P1_SB + 4 * 12288;
constexpr int RED = P1_SB + 5 * 12288, ARR = RED + 1024, MAIN_LDS = ARR + 32;
constexpr int WP = 3 * D * 2;

__device__ __forceinline__ void fmix_lo(float& d, unsigned v, float g, float s) {
  asm volatile("v_fma_mix_f32 %0, %1, %2, %3 op_sel_hi:[1,0,0]" : "=v"(d) : "v"(v), "v"(g), "v"(s));
}
__device__ __forceinline__ void fmix_hi(float& d, unsigned v, float g, float s) {
  asm volatile("v_fma_mix_f32 %0, %1, %2, %3 op_sel:[1,0,0] op_sel_hi:[1,0,0]" : "=v"(d) : "v"(v), "v"(g), "v"(s));
}
template <int OFF>
__device__ __forceinline__ void rd_f4(f32x4& r, unsigned addr) {
  asm volatile("ds_read_b128 %0, %1 offset:%c2" : "=v"(r) : "v"(addr), "i"(OFF));
}
__device__ __forceinline__ u32x4 modulate(u32x4 x, const f32x4 (&g)[4]) {
  float t[8];
#pragma unroll
  for (int d = 0; d < 4; ++d) {
    fmix_lo(t[2 * d], x[d], g[2 + (d >> 1)][(d & 1) * 2], g[d >> 1][(d & 1) * 2]);
    fmix_hi(t[2 * d + 1], x[d], g[2 + (d >> 1)][(d & 1) * 2 + 1], g[d >> 1][(d & 1) * 2 + 1]);
  }
  return u32x4{pack2(t[0], t[1]), pack2(t[2], t[3]), pack2(t[4], t[5]), pack2(t[6], t[7])};
}

struct P2Tile { int b, ti, tj; };
__device__ __forceinline__ P2Tile p2_tile(int bid) {
  const int x = bid & 7, idx = bid >> 3, qd = x >> 1;
  P2Tile t;
  t.b = x & 1;
  if (idx == 30) { t.ti = 5; t.tj = 5; }
  else if (qd == 0) { t.ti = idx / 6; t.tj = idx % 6; }
  else if (qd == 1) { t.ti = idx / 5; t.tj = 6 + idx % 5; }
  else if (qd == 2) { t.ti = 6 + idx / 6; t.tj = 5 + idx % 6; }
  else { t.ti = 5 + idx / 5; t.tj = idx % 5; }
  return t;
}
__device__ __forceinline__ int p2_counter(const P2Tile& t, int lane) {
  const int u = min(lane, 11), sec = u < 6 ? 1 : 0, rt = (u < 6 ? t.tj * 6 + u : t.ti * 6 + u - 6);
  return rt < 64 ? C_QK + (t.b * 2 + sec) * 8 + rt / 9 : C_T + sec;
}
__device__ __forceinline__ void p2_issue(LDS_AS char* smem, rsrc_t rs, const P2Tile& t, int l, int lane, int s, int slot) {
#pragma unroll
  for (int i = 0; i < 4; ++i) {
    const int u = l * 4 + i;
    const int so = (u < 6 ? (int)OFF_K + (t.b * RT + t.tj * 6 + u) * KD * 1024 : (int)OFF_Q + (t.b * RT + t.ti * 6 + u - 6) * KD * 1024) + s * 2048;
    dma_sc1<0>(rs, smem + slot * SLOT + u * 2048, lane * 16, so);
    dma_sc1<1024>(rs, smem + slot * SLOT + u * 2048, lane * 16, so);
  }
}
__device__ __forceinline__ void p2_mfma(LDS_AS char* smem, char* __restrict__ ws, const P2Tile& t, int so, int wave, int lane,
                                        LDS_AS unsigned* arr, const Pending& pend) {
  rsrc_t rs = make_rsrc(ws, WS_END);
  const int wk = wave >> 1, wq = wave & 1, b = t.b, ti = t.ti, tj = t.tj;
  const unsigned krd = lds_addr(smem) + wk * 6144 + lane * 16;
  const unsigned qrd[3] = {lds_addr(smem) + 12288 + wq * 6144 + lane * 16, 0, 0};
  const int g = lane >> 4;
  int pso[3];
#pragma unroll
  for (int j = 0; j < 3; ++j) pso[j] = (int)OFF_P + (b * RT + ti * 6 + wq * 3 + j) * KK * 1024 + (tj * 6 + wk * 3) * 512;
  int po16 = (int)tiled_lane16(lane) + (g & 1) * 512, po8 = (int)tiled_lane(lane) + 1024;
  int sso = (int)OFF_ST + ((b * RT + ti * 6 + wq * 3) * 11 + tj) * 256 + wk * 8, svo = g < 3 ? g * 2816 + (lane & 15) * 16 : OOB;
  unsigned rw = lds_addr(smem) + RED + (wk * 64 + lane) * 4 + wq * 512, rr = lds_addr(smem) + RED + ((wk ^ 1) * 64 + (lane & 15)) * 4 + wq * 512;
  asm volatile("" : "+s"(pso[0]), "+s"(pso[1]), "+s"(pso[2]), "+s"(sso), "+v"(po16), "+v"(po8), "+v"(svo), "+v"(rw), "+v"(rr));
  f32x4 acc[3][3] = {};
  gemm12<3, 3, false, false, 2048, 1024, SLOT, 0, FTX>(acc, krd, krd + 1024, qrd, so, ws, pend, lane);
  asm volatile("s_nop 7");
  SCHED_FENCE();
  constexpr float SC = 0.036084391824351615f * 1.4426950408889634f;
  float mx[3], ls[3];
#pragma unroll
  for (int j = 0; j < 3; ++j) {
    const float m0 = max3(acc[0][j][0], acc[0][j][1], acc[0][j][2]), m1 = max3(acc[0][j][3], acc[1][j][0], acc[1][j][1]);
    const float m2 = max3(acc[1][j][2], acc[1][j][3], acc[2][j][0]), m3 = max3(acc[2][j][1], acc[2][j][2], acc[2][j][3]);
    mx[j] = red4_max(max2(max3(m0, m1, m2), m3));
  }
  {
    const float mine = g == 0 ? mx[0] : (g == 1 ? mx[1] : mx[2]);
    asm volatile("ds_write_b32 %0, %1\n\ts_waitcnt lgkmcnt(0)" ::"v"(rw), "v"(mine) : "memory");
    BARRIER();
    float o0, o1, o2;
    asm volatile("ds_read_b32 %0, %3\n\tds_read_b32 %1, %3 offset:64\n\tds_read_b32 %2, %3 offset:128\n\ts_waitcnt lgkmcnt(0)"
                 : "=&v"(o0), "=&v"(o1), "=&v"(o2) : "v"(rr) : "memory");
    mx[0] = max2(mx[0], o0) * SC; mx[1] = max2(mx[1], o1) * SC; mx[2] = max2(mx[2], o2) * SC;
  }
#pragma unroll
  for (int j = 0; j < 3; ++j) {
#pragma unroll
    for (int i = 0; i < 3; ++i)
#pragma unroll
      for (int r = 0; r < 4; ++r) acc[i][j][r] = __builtin_amdgcn_exp2f(fmaf(acc[i][j][r], SC, -mx[j]));
    __builtin_amdgcn_raw_buffer_store_b128(pair16(acc[0][j], acc[1][j]), rs, po16, pso[j], 16);
    __builtin_amdgcn_raw_buffer_store_b64(pack4(acc[2][j]), rs, po8, pso[j], 16);
    const f32x4 t4 = (acc[0][j] + acc[1][j]) + acc[2][j];
    ls[j] = red4_sum((t4[0] + t4[1]) + (t4[2] + t4[3]));
  }
  {
    const u32x2 v = {__float_as_uint(g == 0 ? mx[0] : (g == 1 ? mx[1] : mx[2])), __float_as_uint(g == 0 ? ls[0] : (g == 1 ? ls[1] : ls[2]))};
    __builtin_amdgcn_raw_buffer_store_b64(v, rs, svo, sso, 16);
  }
  BARRIER();
  if (last_wave(arr, 4, lane)) signal(ws, C_P + (b * 11 + ti) * 3 + (tj < 5 ? 0 : (tj == 5 ? 1 : 2)));
}

template <int MW, class Prev>
__device__ __forceinline__ void big_p1(LDS_AS char* smem, char* __restrict__ ws, int gA, int g, int col, int m0, int wn, int lane,
                                       LDS_AS unsigned* arr, const Pending& pw, Pending& pv, Prev&& prev) {
  rsrc_t rs = make_rsrc(ws, WS_END);
  const unsigned l0 = lds_addr(smem), aa = l0 + m0 * 2048 + lane * 16;
  const int b = gA >= RT ? 1 : 0, sec = col >> 3, nt0 = (col & 7) * 6 + wn * 3;
  f32x4 aq[MW][3], av[MW][3];
  {
    unsigned brd[3];
#pragma unroll
    for (int n = 0; n < 3; ++n) brd[n] = l0 + 18432 + t96_rd(lane, wn * 3 + n, col & 1);
    gemm12<MW, 3, true, true, 0, 6144, SLOT, 0, FTX>(aq, aa, aa + 1024, brd, 2 * SLOT, ws, pw, lane, prev);
  }
  {
    const unsigned brd[3] = {l0 + 18432 + wn * 3072 + lane * 16, 0, 0};
    gemm12<MW, 3, false, false, 1024, 6144, SLOT, 0, FTX>(av, aa, aa + 1024, brd, 4 * SLOT, ws,
                                                           Pending{C_QK + (b * 2 + sec) * 8 + g, -1, arr + 2}, lane, [&]() {
      store_tiles<MW * 3, 16>(rs, (int)((sec ? OFF_K : OFF_Q) + tiled_blk(gA + m0, KD, nt0)), lane,
                              [&](int i) { return tiled_blk(i / 3, KD, i % 3); }, [&](int i) { return aq[i / 3][i % 3]; });
    });
  }
  store_tiles<MW * 3, 16>(rs, (int)((sec ? OFF_VT2 : OFF_VT1) + tiled_blk(b * 48 + nt0, KK, gA - b * RT + m0)), lane,
                          [&](int i) { return tiled_blk(i % 3, KK, i / 3); }, [&](int i) { return av[i / 3][i % 3]; });
  pv = Pending{C_V + (b * 2 + sec) * 8 + (col & 7), -1, arr + 3};
}

__device__ __forceinline__ void big_wg(LDS_AS char* smem, char* __restrict__ ws, const u16* __restrict__ wqkv_img,
                                       const u16* __restrict__ wo_img, const u16* __restrict__ wo_txt, int bid, int wave8, int lane) {
  const int x = bid & 7, idx = bid >> 3, b = x & 1, g = idx % 7, col = (x >> 1) * 4 + idx / 7;
  const int gA = b * RT + g * 9, grp = b * 7 + g, hb = (int)OFF_H + gA * KD * 1024;
  const int wi = idx < 24 ? idx : idx - 4, wmb = b * 6 + wi % 6, wcol = (x >> 1) * 4 + wi / 6;
  const P2Tile t2 = p2_tile(bid);
  const u32x4 srd_ws = make_srd(ws, WS_END);
  rsrc_t rs = make_rsrc(ws, WS_END);
  LDS_AS unsigned* arr = (LDS_AS unsigned*)(smem + ARR);
  if (wave8 == 7) {
    const int c2 = p2_counter(t2, lane);
    barriers(7);
    unsigned f = peek(srd_ws, C_H + grp);
    barriers(2);
    check(f, srd_ws, C_H + grp, 12);
    barriers(10);
    f = peek(srd_ws, C_W + col);
    barriers(2);
    check(f, srd_ws, C_W + col, 12);
    barriers(10);
    f = peek(srd_ws, c2);
    barriers(2);
    check(f, srd_ws, c2, 8);
    barriers(18);
    return;
  }
  if (wave8 >= 4) {
    const int l = wave8 - 4;
    rsrc_t rw = make_rsrc(wqkv_img, D * 3 * D * 2);
    rsrc_t ro = make_rsrc(wcol < 8 ? wo_img : wo_txt, D * D * 2);
    int bo[4], bw[4], av[3];
#pragma unroll
    for (int i = 0; i < 4; ++i) {
      bo[i] = (wcol & 7) * 192 + t96_src(l * 4 + i, lane, D * 2, wcol & 1);
      bw[i] = col * 192 + t96_src(l * 4 + i, lane, WP, col & 1);
    }
#pragma unroll
    for (int i = 0; i < 3; ++i) {
      const int u = min(l * 3 + i, 7), r = u * 8 + (lane >> 3);
      av[i] = (wmb * 64 + r) * WP + 2 * D * 2 + (((lane & 7) ^ ((r >> 1) & 7)) * 16);
    }
    const int wz = idx < 24 ? 0 : OOB;
    auto issueW = [&](int s, int slot) {
#pragma unroll
      for (int i = 0; i < 3; ++i) dma<0>(rw, smem + slot * SLOT + min(l * 3 + i, 7) * 1024, av[i], wz + s * 128);
#pragma unroll
      for (int i = 0; i < 4; ++i) dma<0>(ro, smem + slot * SLOT + 18432 + (l * 4 + i) * 1024, bo[i], wz + s * 64 * (D * 2));
    };
    auto issueA = [&](int s, int slot) {
#pragma unroll
      for (int i = 0; i < 3; ++i) {
        LDS_AS char* sa = smem + slot * SLOT + (l * 3 + i) * 2048;
        dma_sc1<0>(rs, sa, lane * 16, hb + ((l * 3 + i) * KD + 2 * s) * 1024);
        dma_sc1<1024>(rs, sa, lane * 16, hb + ((l * 3 + i) * KD + 2 * s) * 1024);
      }
    };
    issueW(0, 0);
    __builtin_amdgcn_s_sleep(4);
#pragma unroll
    for (int s = 1; s < 4; ++s) issueW(s, s);
    WAIT_VM(21); BARRIER();
    int slot = 4;
#pragma unroll 1
    for (int s = 4; s < 12; ++s) {
      WAIT_VM(14); BARRIER(); issueW(s, slot); slot = wrapn(slot + 1, 5);
    }
    stream12<7, 10>(slot, [&](int s, int sl) {
#pragma unroll
      for (int i = 0; i < 4; ++i) dma<0>(rw, smem + sl * SLOT + 18432 + (l * 4 + i) * 1024, bw[i], s * 64 * WP);
      issueA(s, sl);
    });
#pragma unroll 1
    for (int k = 0; k < 12; ++k) {
      if (k == 0 || k >= 7) WAIT_VM(20);
      else if (k == 1 || k == 6) WAIT_VM(14);
      else WAIT_VM(8);
      BARRIER();
      const int s = k < 5 ? k + 7 : k - 5;
#pragma unroll
      for (int i = 0; i < 4; ++i) {
        const int pc = l * 4 + i;
        dma_sc1(rs, smem + slot * SLOT + 18432 + pc * 1024, lane * 16, (int)OFF_WVO + ((col * 6 + pc % 6) * KD + 2 * s + pc / 6) * 1024);
      }
      if (k >= 5) issueA(s, slot);
      slot = wrapn(slot + 1, 5);
    }
    stream12<10, 8>(slot, [&](int s, int sl) { p2_issue(smem, rs, t2, l, lane, s, sl); });
    WAIT_VM(16); BARRIER();
    WAIT_VM(8); BARRIER();
    WAIT_VM(0); BARRIER();
    barriers(3);
    return;
  }
  const int c = wave8, wm = c >> 1, wn = c & 1;
  Pending ph{-1, -1, arr};
  if (col < 12) {
    const int W = col * 4 + c, ks = W >> 1, t0 = (W & 1) * 5;
    u32x4 xr[5], mp[4][KSPL];
#pragma unroll
    for (int i = 0; i < 4; ++i)
#pragma unroll
      for (int k = 0; k < KSPL; ++k)
        mp[i][k] = aload(srd_ws, (lane >> 4) * 32, (int)OFF_MOD + ((k * 4 + b) * 2304 + (i >> 1) * 768 + ks * 32 + (i & 1) * 4) * 4);
#pragma unroll
    for (int i = 0; i < 5; ++i) xr[i] = aload(srd_ws, lane * 16, (int)OFF_XN + ((gA + min(t0 + i, 8)) * KD + ks) * 1024);
    WAIT_VM(0);
    f32x4 gs[4];
#pragma unroll
    for (int i = 0; i < 4; ++i) {
      TIE(mp[i][0]); TIE(mp[i][1]);
      gs[i] = __builtin_bit_cast(f32x4, mp[i][0]) + __builtin_bit_cast(f32x4, mp[i][1]);
      if (i >= 2) gs[i] += 1.0f;
    }
#pragma unroll
    for (int i = 0; i < 5; ++i) {
      TIE(xr[i]);
      __builtin_amdgcn_raw_buffer_store_b128(modulate(xr[i], gs), rs, lane * 16, hb + (min(t0 + i, 8) * KD + ks) * 1024, 16);
    }
    ph.i0 = C_H + grp;
  }
  const int sw = (lane & 15) >> 1, gq = lane >> 4;
  const unsigned ab = lds_addr(smem) + wm * 4096 + (lane & 15) * 128;
  unsigned brd[3];
#pragma unroll
  for (int n = 0; n < 3; ++n) brd[n] = lds_addr(smem) + 18432 + t96_rd(lane, wn * 3 + n, wcol & 1);
  f32x4 aw[2][3];
  BARRIER();
  gemm12<2, 3, true, false, 0, 6144, SLOT, 0, 2>(aw, ab + ((gq ^ sw) * 16), ab + (((4 + gq) ^ sw) * 16), brd, 0, ws, ph, lane);
  const Pending pw{idx < 24 ? C_W + wcol : -1, -1, arr + 1};
  auto store_w = [&]() {
    store_tiles<6, 16>(rs, idx < 24 ? (int)(OFF_WVO + tiled_blk(wcol * 6 + wn * 3, KD, wmb * 4 + wm * 2)) : OOB, lane,
                       [&](int i) { return tiled_blk(i % 3, KD, i / 3); }, [&](int i) { return aw[i / 3][i % 3]; });
  };
  Pending pv;
  if (wm == 0) big_p1<5>(smem, ws, gA, g, col, 0, wn, lane, arr, pw, pv, store_w);
  else big_p1<4>(smem, ws, gA, g, col, 5, wn, lane, arr, pw, pv, store_w);
  p2_mfma(smem, ws, t2, 1 * SLOT, c, lane, arr + 4, pv);
}

template <int MT, bool VT, bool FIRST>
__device__ __forceinline__ void small_p1(LDS_AS char* smem, char* __restrict__ ws, const u16* __restrict__ wsrc, int wcol, int obase,
                                         int gA, int col, int smod, int wave8, int lane, const Pending& pend) {
  constexpr int MH = MT / 2;
  const u32x4 srd_ws = make_srd(ws, WS_END);
  rsrc_t rs = make_rsrc(ws, WS_END);
  auto gtile = [&](int lt) { return lt < MH ? gA + lt : gA + RT + (lt - MH); };
  if (wave8 == 7) {
    if (FIRST) {
      u32x4 mp[12][KSPL];
#pragma unroll
      for (int h = 0; h < 12; ++h)
#pragma unroll
        for (int k = 0; k < KSPL; ++k) mp[h][k] = aload(srd_ws, lane * 16, (int)OFF_MOD + (((k * 2 + smod) * 2 + h / 6) * 2304 + (h % 6) * 256) * 4);
      WAIT_VM(0);
#pragma unroll
      for (int h = 0; h < 12; ++h) {
        TIE(mp[h][0]); TIE(mp[h][1]);
        f32x4 v = __builtin_bit_cast(f32x4, mp[h][0]) + __builtin_bit_cast(f32x4, mp[h][1]);
        if (h % 6 >= 3) v += 1.0f;
        *(LDS_AS f32x4*)(smem + P1_SGS + h * 1024 + lane * 16) = v;
      }
      WAIT_LGKM(0);
    }
    barriers(FIRST ? 15 : 14);
    return;
  }
  if (wave8 >= 4) {
    const int l = wave8 - 4;
    rsrc_t rb = VT ? rs : make_rsrc(wsrc, D * 3 * D * 2);
    int bv[4];
#pragma unroll
    for (int i = 0; i < 4; ++i) {
      const int pc = l * 4 + i;
      bv[i] = VT ? ((col * 6 + pc % 6) * KD + pc / 6) * 1024 + lane * 16 : wcol + t96_src(pc, lane, WP, col & 1);
    }
    if (VT) poll(srd_ws, C_W + col, 12);
    loader_loop<12, 5, 4, 0, FIRST>([&](int s, int slot) {
#pragma unroll
      for (int i = 0; i < 4; ++i) {
        if (VT) dma_sc1(rb, smem + P1_SB + slot * 12288 + (l * 4 + i) * 1024, bv[i], (int)OFF_WVO + s * 2048);
        else dma<0>(rb, smem + P1_SB + slot * 12288 + (l * 4 + i) * 1024, bv[i], s * 64 * WP);
      }
    });
    BARRIER();
    return;
  }
  const int c = wave8, wm = c >> 1, wn = c & 1;
  auto aoff = [&](int st) { return st * (MT * 2048); };
  if (FIRST) {
    u32x4 xr[6][MT];
#pragma unroll
    for (int k = 0; k < 6; ++k)
#pragma unroll
      for (int i = 0; i < MT; ++i) xr[k][i] = aload(srd_ws, lane * 16, (int)OFF_XN + (gtile(i) * KD + c * 6 + k) * 1024);
    BARRIER();
    WAIT_VM(0);
#pragma unroll
    for (int k = 0; k < 6; ++k) {
      f32x4 gs[2][4];
#pragma unroll
      for (int z = 0; z < 2; ++z) {
        const unsigned gp = lds_addr(smem) + P1_SGS + z * 6144 + (c * 6 + k) * 128 + (lane >> 4) * 32;
        rd_f4<0>(gs[z][0], gp); rd_f4<16>(gs[z][1], gp); rd_f4<3072>(gs[z][2], gp); rd_f4<3088>(gs[z][3], gp);
      }
      WAIT_LGKM(0);
#pragma unroll
      for (int z = 0; z < 2; ++z)
#pragma unroll
        for (int i = 0; i < 4; ++i) TIE(gs[z][i]);
#pragma unroll
      for (int i = 0; i < MT; ++i) {
        TIE(xr[k][i]);
        *(LDS_AS u32x4*)(smem + aoff(c * 3 + (k >> 1)) + i * 2048 + (k & 1) * 1024 + lane * 16) = modulate(xr[k][i], gs[i < MH ? 0 : 1]);
      }
    }
    WAIT_LGKM(0);
  }
  const unsigned aa = lds_addr(smem) + wm * MH * 2048 + lane * 16;
  unsigned brd[3];
#pragma unroll
  for (int n = 0; n < 3; ++n) brd[n] = lds_addr(smem) + P1_SB + (VT ? wn * 3072 + lane * 16 : t96_rd(lane, wn * 3 + n, col & 1));
  f32x4 acc[MH][3] = {};
  BARRIER();
  gemm12<MH, 3, !VT, !VT, 1024, 6144, 12288, MT * 2048, 4>(acc, aa, aa + 1024, brd, 0, ws, pend, lane);
  BARRIER();
  const int nt0 = (col & 7) * 6 + wn * 3;
  if (VT) store_tiles<MH * 3, 16>(rs, obase, lane, [&](int i) { return tiled_blk(wm * 48 + nt0 + i % 3, KK, gA + i / 3); },
                                  [&](int i) { return acc[i / 3][i % 3]; });
  else store_tiles<MH * 3, 16>(rs, obase, lane, [&](int i) { return tiled_blk((FIRST ? gA + wm * RT : wm * MH) + i / 3, KD, nt0 + i % 3); },
                               [&](int i) { return acc[i / 3][i % 3]; });
}

__device__ __forceinline__ void small_wg(LDS_AS char* smem, char* __restrict__ ws, const u16* __restrict__ wqkv_img,
                                         const u16* __restrict__ wqkv_txt, const u16* __restrict__ wo_img,
                                         const u16* __restrict__ wo_txt, int bid, int wave8, int lane) {
  const int x = bid & 7, idx = bid >> 3;
  const u32x4 srd_ws = make_srd(ws, WS_END);
  rsrc_t rs = make_rsrc(ws, WS_END);
  LDS_AS unsigned* arr = (LDS_AS unsigned*)(smem + ARR);
  Pending pv{-1, -1, arr + 1};
  if (bid < 240) {
    const int col = (x >> 1) * 4 + (x & 1) * 2 + (idx - 28), sec = col >> 3;
    small_p1<2, false, true>(smem, ws, wqkv_img, col * 192, (int)(sec ? OFF_K : OFF_Q), 63, col, 0, wave8, lane, Pending{-1, -1, arr});
    small_p1<2, true, false>(smem, ws, wqkv_img, 0, (int)(sec ? OFF_VT2 : OFF_VT1), 63, col, 0, wave8, lane,
                             Pending{C_QK + sec * 8 + 7, C_QK + 16 + sec * 8 + 7, arr});
    pv.i0 = C_V + sec * 8 + (col & 7); pv.i1 = pv.i0 + 16;
  } else {
    const int col = bid - 240;
    small_p1<4, false, true>(smem, ws, wqkv_txt, col * 192, (int)(col >> 3 ? OFF_K : OFF_Q), 64, col, 1, wave8, lane, Pending{-1, -1, arr});
    const Pending pq{C_T + (col >> 3), -1, arr};
    if (col < 8) {
      small_p1<4, false, false>(smem, ws, wqkv_txt, 3072 + col * 192, (int)OFF_VX, 64, col, 1, wave8, lane, pq);
      pv.i0 = C_T + 2;
    } else if (wave8 < 4) {
      flush(ws, pq, lane);
    }
  }
  if (wave8 < 4) flush(ws, pv, lane);
  if (bid < 242) {
    const P2Tile t2 = p2_tile(bid);
    if (wave8 == 7) barriers(15);
    else if (wave8 >= 4) {
      poll(srd_ws, p2_counter(t2, lane), 8);
      loader_loop<12, 5, 8>([&](int s, int slot) { p2_issue(smem, rs, t2, wave8 - 4, lane, s, slot); });
      barriers(2);
    } else {
      BARRIER();
      p2_mfma(smem, ws, t2, 0, wave8, lane, arr + 4, Pending{-1, -1, arr});
    }
  } else if (bid < 254) {
    const int e = bid - 242, wave = wave8 & 3;
    if (wave8 >= 4) {
      rsrc_t rb = make_rsrc(e < 6 ? wo_img : wo_txt, D * D * 2);
      const int bv0 = (e % 6) * 256 + wave * 16 * (D * 2) + timg_src(lane, 0, D * 2);
      const int bv1 = (e % 6) * 256 + (wave * 16 + 8) * (D * 2) + timg_src(lane, 1, D * 2);
      poll(srd_ws, C_T + 2, 8);
      loader_loop<12, 5, 6>([&](int s, int slot) {
        LDS_AS char* sb = smem + slot * SLOT;
        dma_sc1<0>(rs, sb + wave * 2048, lane * 16, (int)OFF_VX + (wave * KD + 2 * s) * 1024);
        dma_sc1<1024>(rs, sb + wave * 2048, lane * 16, (int)OFF_VX + (wave * KD + 2 * s) * 1024);
#pragma unroll
        for (int i = 0; i < 4; ++i) dma<0>(rb, sb + 8192 + (wave * 4 + i) * 1024, ((i >> 1) ? bv1 : bv0) + (i & 1) * 128, s * 64 * (D * 2));
      });
    } else {
      const int wm = wave >> 1, wn = wave & 1;
      const unsigned aa = lds_addr(smem) + wm * 4096 + lane * 16;
      unsigned brd[4];
#pragma unroll
      for (int n = 0; n < 4; ++n) brd[n] = lds_addr(smem) + 8192 + timg_rd<2>(lane, wn * 4 + n);
      f32x4 acc[2][4] = {};
      BARRIER();
      gemm12<2, 4, true, false, 0, 8192, SLOT, 0, FTX>(acc, aa, aa + 1024, brd, 0, ws, Pending{-1, -1, arr}, lane);
      store_tiles<8, 16>(rs, (int)(e < 6 ? OFF_VT1 : OFF_VT2), lane,
                         [&](int i) { return tiled_blk(wm * 48 + (e % 6) * 8 + wn * 4 + (i & 3), KK, 64 + (i >> 2)); },
                         [&](int i) { return acc[i >> 2][i & 3]; });
      if (last_wave(arr + 4, 4, lane)) signal(ws, C_X + e);
    }
  }
}

constexpr int K3_NS = 4, K3_STAGE = 35 * 1024, K3_ST = 33 * 1024;
__device__ __forceinline__ void fmac(f32x4& a, const f32x4& x, float f, int e) {
  float a0 = a[e], a1 = a[e + 1];
  asm volatile("v_fmac_f32 %0, %2, %4\n\tv_fmac_f32 %1, %3, %4" : "+v"(a0), "+v"(a1) : "v"(x[e]), "v"(x[e + 1]), "v"(f));
  a[e] = a0; a[e + 1] = a1;
}
template <int QT>
__device__ __forceinline__ void k3_mfma(LDS_AS char* smem, int wn, int qoff, int nb, int lane, u16* __restrict__ outp) {
  const unsigned vrd = lds_addr(smem) + wn * 9216 + lane * 16, prd = lds_addr(smem) + 18432 + qoff * 3072 + lane * 16;
  unsigned srd[QT];
#pragma unroll
  for (int j = 0; j < QT; ++j) srd[j] = lds_addr(smem) + K3_ST + (qoff + j) * 256 + (qoff + j >= 3 ? 256 : 0) + (lane & 15) * 16;
  f32x4 acc[3][QT] = {}, t0[3][QT], t1[3][QT] = {};
  f32x4 st[QT];
  float R[QT], L[QT], w[QT];
#pragma unroll
  for (int j = 0; j < QT; ++j) { R[j] = -1e30f; L[j] = 0.f; st[j] = f32x4{-1e30f, 0.f, 0.f, 0.f}; }
  Frags<3, QT> f0, f1, f2;
  unsigned bc[QT];
  bc[0] = prd;
  BARRIER();
  rd_set<3, QT, false, 3072, 3072, 0, 0>(f0, vrd, bc);
  rd_set<3, QT, false, 3072, 3072, 1024, 1024>(f1, vrd, bc);
  int so = 0;
  auto weigh = [&](auto sc, const f32x4 (&sp)[QT]) {
    constexpr int S = decltype(sc)::value;
    if constexpr (S == 0) {
      bool big = false;
#pragma unroll
      for (int j = 0; j < QT; ++j) { w[j] = sp[j][0] - R[j]; big |= w[j] > 64.f; }
      if (__builtin_amdgcn_ballot_w64(big) != 0) {
#pragma unroll
        for (int j = 0; j < QT; ++j) {
          const bool up = w[j] > 64.f;
          const float sc2 = up ? __builtin_amdgcn_exp2f(-w[j]) : 1.0f;
          R[j] = up ? sp[j][0] : R[j];
          w[j] = up ? 0.f : w[j];
          L[j] *= sc2;
#pragma unroll
          for (int i = 0; i < 3; ++i) acc[i][j] *= sc2;
        }
      }
    } else if constexpr (S <= QT) {
      w[S - 1] = __builtin_amdgcn_exp2f(w[S - 1]);
      tie1(w[S - 1]);
    } else if constexpr (S <= 2 * QT) {
      L[S - QT - 1] = fmaf(w[S - QT - 1], sp[S - QT - 1][1] + sp[S - QT - 1][3], L[S - QT - 1]);
      tie1(L[S - QT - 1]);
    }
  };
  auto stage = [&](f32x4 (&cur)[3][QT], f32x4 (&prv)[3][QT]) {
    BARRIER();
    f32x4 sp[QT];
#pragma unroll
    for (int j = 0; j < QT; ++j) { sp[j] = st[j]; rd_f4<0>(st[j], srd[j] + so); }
    kstep<3, QT, false, false, true, 3072, 3072, 2048, 2048, true>(cur, f0, f2, vrd + so, bc, [&](auto ic) { weigh(ic, sp); });
    so = wrapn(so + K3_STAGE, K3_NS * K3_STAGE);
    bc[0] = prd + so;
    auto fold = [&](auto sc) {
      constexpr int S = decltype(sc)::value, T = S / 2, E = (S % 2) * 2;
      fmac(acc[T / QT][T % QT], prv[T / QT][T % QT], w[T % QT], E);
    };
    kstep<3, QT, false, false, false, 3072, 3072, 0, 0, true>(cur, f1, f0, vrd + so, bc, [&](auto ic) {
      if constexpr (decltype(ic)::value == 0) {
#pragma unroll
        for (int j = 0; j < QT; ++j) tie4(st[j]);
      }
      fold(ic);
    });
    kstep<3, QT, false, false, false, 3072, 3072, 1024, 1024, true>(cur, f2, f1, vrd + so, bc, [&](auto ic) {
      fold(std::integral_constant<int, 3 * QT + decltype(ic)::value>{});
    });
  };
#pragma unroll 1
  for (int t = 0; t < 10; t += 2) {
    stage(t0, t1);
    stage(t1, t0);
  }
  stage(t0, t1);
  land<3, QT, false>(f0);
  land<3, QT, false>(f1);
  static_for<0, 2 * QT + 1>([&](auto sc) { weigh(sc, st); });
  LDS_AS char* xs = smem + 3 * K3_STAGE;
  const int cw = wn * 48 + (lane >> 4) * 4;
  f32x4 o[QT * 3];
#pragma unroll
  for (int j = 0; j < QT; ++j) L[j] = 1.0f / L[j];
#pragma unroll
  for (int i = 0; i < 3; ++i) {
    const f32x4 gate = *(LDS_AS f32x4*)(xs + 15360 + (cw + i * 16) * 4) + *(LDS_AS f32x4*)(xs + 15360 + 384 + (cw + i * 16) * 4);
#pragma unroll
    for (int j = 0; j < QT; ++j) {
      const u32x2 xv = *(LDS_AS u32x2*)(xs + ((qoff + j) * 16 + (lane & 15)) * 192 + (cw + i * 16) * 2);
      const f32x4 a = (acc[i][j] + t0[i][j] * w[j]) * L[j];
      o[j * 3 + i] = f32x4{fmaf(gate[0], a[0], bflo(xv[0])), fmaf(gate[1], a[1], bfhi(xv[0])),
                           fmaf(gate[2], a[2], bflo(xv[1])), fmaf(gate[3], a[3], bfhi(xv[1]))};
    }
  }
  const bool odd = (lane >> 4) & 1;
  u16* ob = outp + (size_t)(qoff * 16 + (lane & 15)) * D + nb * 96 + wn * 48;
  auto toff = [&](int i) { return (i / 3) * 16 * D + (i % 3) * 16; };
#pragma unroll
  for (int i = 0; i + 1 < QT * 3; i += 2) *(u32x4*)(ob + (odd ? toff(i + 1) : toff(i)) + (lane >> 5) * 8) = pair16(o[i], o[i + 1]);
  if (QT & 1) *(u32x2*)(ob + toff(QT * 3 - 1) + (lane >> 4) * 4) = pack4(o[QT * 3 - 1]);
}

__device__ __forceinline__ void phase3(LDS_AS char* smem, char* __restrict__ ws, const u16* __restrict__ x_img,
                                       const u16* __restrict__ x_txt, u16* __restrict__ out, int bid, int wave8, int lane) {
  const int wave = wave8 & 3, x = bid & 7, idx = bid >> 3, b = x & 1, qd = x >> 1;
  int rt0, nrt, nb, txt;
  if (idx < 26) {
    const int u = (qd >> 1) * 26 + idx, rb = u >> 2;
    nb = (qd & 1) * 4 + (u & 3); rt0 = rb * 5; nrt = rb == 12 ? 4 : 5; txt = 0;
  } else {
    nb = qd * 2 + idx - 26; rt0 = 64; nrt = 2; txt = 1;
  }
  if (wave8 >= 4) {
    rsrc_t rs = make_rsrc(ws, WS_END);
    const u32x4 srd_ws = make_srd(ws, WS_END);
    const bool rev = qd == 1 || qd == 2;
    auto ktile = [&](int t) { return rev ? (t < 5 ? 6 + t : (t == 5 ? 5 : t - 6)) : t; };
    auto tail = [&](int slot) {
      const u16* xres = txt ? x_txt + (size_t)b * NTX * D : x_img + (size_t)(b * NI + rt0 * 16) * D;
      rsrc_t rx = make_rsrc(xres, nrt * 16 * D * 2);
      LDS_AS char* sb = smem + slot * K3_STAGE + wave * 4096;
#pragma unroll
      for (int i = 0; i < 4; ++i) {
        const int c = (wave * 4 + i) * 64 + lane;
        if (wave * 4 + i < 15) dma<0>(rx, sb + i * 1024, (c / 12) * (D * 2) + nb * 192 + (c % 12) * 16, 0);
        else dma<0>(rs, sb + i * 1024, lane < 24 * KSPL ? (int)OFF_MOD + ((((lane / 24) * 2 + txt) * 2 + b) * 2304 + 1536 + nb * 96 + (lane % 24) * 4) * 4 : OOB, 0);
      }
    };
    if (wave < 2) {
      poll(srd_ws, C_V + (b * 2 + txt) * 8 + nb, 8);
      const int vb = (int)(txt ? OFF_VT2 : OFF_VT1) + (b * 48 + nb * 6 + wave * 3) * KK * 1024;
      loader_loop<11, K3_NS, 9, 4>([&](int t, int slot) {
        const int kt = ktile(t);
        if (kt == 10) poll(srd_ws, C_X + txt * 6 + (nb * 96 + (lane & 1) * 95) / 128, 1);
        LDS_AS char* sb = smem + slot * K3_STAGE + wave * 9216;
#pragma unroll
        for (int i = 0; i < 3; ++i) {
          const int so = vb + i * KK * 1024 + kt * 3072;
          dma_sc1<0>(rs, sb + i * 3072, lane * 16, so);
          dma_sc1<1024>(rs, sb + i * 3072, lane * 16, so);
          dma_sc1<2048>(rs, sb + i * 3072, lane * 16, so);
        }
      }, NoHook{}, tail);
    } else {
      const int cp = C_P + (b * 11 + (rt0 * 16 + (lane & 1) * (nrt * 16 - 1)) / 96) * 3;
      poll(srd_ws, cp + (rev ? 2 : 0), 5);
      const int pb = (int)OFF_P + (b * RT + rt0) * KK * 1024;
      const int su = (wave - 2) * 3 + (lane >> 4);
      const int sv = (su < nrt && (lane >> 4) < 3) ? (int)OFF_ST + (b * RT + rt0 + su) * 2816 + (lane & 15) * 16 : OOB;
      loader_loop<11, K3_NS, 9, 4>([&](int t, int slot) {
        if (t == 5) poll(srd_ws, cp + ((lane & 2) ? 1 : (rev ? 0 : 2)), (lane & 2) ? 1 : 5);
        const int kt = ktile(t);
#pragma unroll
        for (int i = 0; i < 8; ++i) {
          const int pc = (wave - 2) * 8 + i, u = pc / 3, ks = pc % 3;
          if (i < 7 || wave == 2)
            dma_sc1(rs, smem + slot * K3_STAGE + 18432 + pc * 1024, lane * 16, u < nrt ? pb + (u * KK + kt * 3 + ks) * 1024 : OOB);
          else
            dma_sc1(rs, smem + RED, OOB, 0);
        }
        dma_sc1(rs, smem + slot * K3_STAGE + K3_ST + (wave - 2) * 1024, sv, kt * 256);
      }, NoHook{}, tail);
    }
    return;
  }
  u16* outp = txt ? out + (size_t)2 * NI * D + (size_t)b * NTX * D : out + (size_t)(b * NI + rt0 * 16) * D;
  const int wq = wave >> 1, q0 = (nrt + 1) >> 1;
  const int qt = wq ? nrt - q0 : q0, qoff = wq ? q0 : 0;
  if (qt == 3) k3_mfma<3>(smem, wave & 1, qoff, nb, lane, outp);
  else if (qt == 2) k3_mfma<2>(smem, wave & 1, qoff, nb, lane, outp);
  else k3_mfma<1>(smem, wave & 1, qoff, nb, lane, outp);
}

__global__ __launch_bounds__(512, 2) void kmain(const u16* __restrict__ x_img, const u16* __restrict__ x_txt,
                                                const u16* __restrict__ wqkv_img, const u16* __restrict__ wqkv_txt,
                                                const u16* __restrict__ wo_img, const u16* __restrict__ wo_txt,
                                                char* __restrict__ ws, u16* __restrict__ out) {
  __shared__ __attribute__((aligned(1024))) char smem_[MAIN_LDS];
  LDS_AS char* smem = (LDS_AS char*)smem_;
  const int bid = blockIdx.x, tid = threadIdx.x, lane = tid & 63;
  const int wave = __builtin_amdgcn_readfirstlane(tid >> 6);
  asm volatile(".Lmmdit_main_begin:");
  if (tid < 8) ((LDS_AS unsigned*)(smem + ARR))[tid] = 0u;
  WAIT_LGKM(0);
  if (bid < 224) {
    big_wg(smem, ws, wqkv_img, wo_img, wo_txt, bid, wave, lane);
    phase3(smem, ws, x_img, x_txt, out, bid, wave, lane);
  } else {
    small_wg(smem, ws, wqkv_img, wqkv_txt, wo_img, wo_txt, bid, wave, lane);
  }
  asm volatile(".Lmmdit_main_end:");
}

extern "C" void kernel_launch(void* const* d_in, const int* in_sizes, int n_in, void* d_out, int out_size, void* d_ws,
                              size_t ws_size, hipStream_t stream) {
  const u16* x_img = (const u16*)d_in[0];
  const u16* x_txt = (const u16*)d_in[1];
  const u16* cvec = (const u16*)d_in[2];
  const u16* wqkv_img = (const u16*)d_in[3];
  const u16* wqkv_txt = (const u16*)d_in[4];
  const u16* wo_img = (const u16*)d_in[5];
  const u16* wo_txt = (const u16*)d_in[6];
  const u16* wmod_img = (const u16*)d_in[7];
  const u16* wmod_txt = (const u16*)d_in[8];
  char* ws = (char*)d_ws;
  k0_prep<<<K0_T1 + K0_T2 + K0_Z + K0_PF, 256, 0, stream>>>(x_img, x_txt, cvec, wmod_img, wmod_txt, wqkv_img, wqkv_txt, wo_img, wo_txt, ws);
  kmain<<<256, 512, 0, stream>>>(x_img, x_txt, wqkv_img, wqkv_txt, wo_img, wo_txt, ws, (u16*)d_out);
}
```
